```python
import torch

def _softmax(x, axis=-1):
    return torch.softmax(x, dim=axis)
def _gelu(x):
    return torch.nn.functional.gelu(x, approximate="tanh")
def _silu(x):
    return torch.nn.functional.silu(x)
def _sigmoid(x):
    return torch.sigmoid(x)
def _layernorm(x, axis=-1, eps=1e-5):
    m = x.mean(dim=axis, keepdim=True)
    v = x.var(dim=axis, keepdim=True, unbiased=False)
    return (x - m) / torch.sqrt(v + eps)
def _scatter_add(tgt, idx, src, axis=0):
    return tgt.clone().index_add_(axis, idx, src)
def _segment_sum(src, seg, n):
    out = torch.zeros((n,) + src.shape[1:], dtype=src.dtype)
    return out.index_add_(0, seg, src)
def _pad(x, pw):
    flat = tuple(v for pair in reversed(pw) for v in pair)
    return torch.nn.functional.pad(x, flat)

kernel_name = 'gated_mix__mha_block__spectral_fnet__prompt'

def setup_inputs():

    _w = (lambda a: (lambda t: t.to(torch.bfloat16) if t.is_floating_point() else t)(torch.as_tensor(a)))
    def _A_setup():
        B, S, D, H = 1, 2048, 640, 32
        return {
            "x": _w((lambda *s: torch.randn(*s, dtype=torch.bfloat16) * 0.05)(B, S, D)),
            "wqkv": _w((lambda *s: torch.randn(*s, dtype=torch.bfloat16) * 1.6)(D, 3 * D)),
            "wo": _w((lambda *s: torch.randn(*s, dtype=torch.bfloat16) * 1.6)(D, D)),
        }
    _A_in = _A_setup()

    def _B_setup():
        B, S, D = 1, 2048, 640
        ns = torch.arange(S)[:, None] * torch.arange(S)[None, :]
        nd = torch.arange(D)[:, None] * torch.arange(D)[None, :]
        Ws_re = torch.cos(-2.0 * torch.pi * ns / S).to(float)
        Ws_im = torch.sin(-2.0 * torch.pi * ns / S).to(float)
        Wd_re = torch.cos(-2.0 * torch.pi * nd / D).to(float)
        Wd_im = torch.sin(-2.0 * torch.pi * nd / D).to(float)
        return {
            "x": _w((lambda *s: torch.randn(*s, dtype=torch.bfloat16) * 0.05)(B, S, D)),
            "dft_seq_re": _w(Ws_re),
            "dft_seq_im": _w(Ws_im),
            "dft_hid_re": _w(Wd_re),
            "dft_hid_im": _w(Wd_im),
        }
    _B_in = _B_setup()

    return {
        "A_x": _A_in["x"],
        "A_wqkv": _A_in["wqkv"],
        "A_wo": _A_in["wo"],
        "B_x": _B_in["x"],
        "B_dft_seq_re": _B_in["dft_seq_re"],
        "B_dft_seq_im": _B_in["dft_seq_im"],
        "B_dft_hid_re": _B_in["dft_hid_re"],
        "B_dft_hid_im": _B_in["dft_hid_im"],
        "gate_w": _w((lambda *s: torch.randn(*s, dtype=torch.bfloat16) * 0.05)(640) * 16.0),
    }

def reference(A_x, A_wqkv, A_wo, B_x, B_dft_seq_re, B_dft_seq_im, B_dft_hid_re, B_dft_hid_im, gate_w):
    def _A_ref(x, wqkv, wo):
        B, S, D = x.shape
        H = 32
        Dh = D // H
        qkv = (lambda x, *d: x.permute(*d))((x @ wqkv).reshape(B, S, 3, H, Dh), 2, 0, 3, 1, 4)
        q, k, v = qkv[0], qkv[1], qkv[2]
        scores = torch.einsum("bhqd,bhkd->bhqk", q, k) * (Dh ** -0.5)
        p = _softmax(scores, axis=-1)
        o = torch.einsum("bhqk,bhkd->bhqd", p, v)
        return (lambda x, *d: x.permute(*d))(o, 0, 2, 1, 3).reshape(B, S, D) @ wo

    def _B_ref(x, dft_seq_re, dft_seq_im, dft_hid_re, dft_hid_im):
        a = torch.einsum("bsd,ed->bse", x, dft_hid_re)
        b = torch.einsum("bsd,ed->bse", x, dft_hid_im)
        y = torch.einsum("ts,bse->bte", dft_seq_re, a) - torch.einsum("ts,bse->bte", dft_seq_im, b)
        return y

    x = A_x
    g = _sigmoid((x * gate_w).sum(-1, keepdims=True))
    a = _A_ref(x, A_wqkv, A_wo)
    b = _B_ref(x, B_dft_seq_re, B_dft_seq_im, B_dft_hid_re, B_dft_hid_im)
    return g * a + (1 - g) * b
```

```cpp
#include <hip/hip_runtime.h>
#include <hip/hip_bf16.h>
#include <stdint.h>

typedef unsigned short u16;
typedef __attribute__((ext_vector_type(8))) __bf16 bf16x8;
typedef __attribute__((ext_vector_type(4))) short s16x4;
typedef __attribute__((ext_vector_type(16))) float f32x16;
typedef __attribute__((ext_vector_type(4))) float f32x4;
typedef __attribute__((ext_vector_type(4))) unsigned u32x4;
typedef __attribute__((ext_vector_type(2))) unsigned u32x2;
#define LAS __attribute__((address_space(3)))
#define DI __device__ __forceinline__

constexpr int SEQ = 2048, DM = 640, NH = 32, DH = 20;
constexpr int QT_B = 1280;
constexpr int KVT_B = 2624;
constexpr size_t WS_Q = 0;
constexpr size_t WS_KV = WS_Q + (size_t)NH * 64 * QT_B;
constexpr size_t WS_AB = WS_KV + (size_t)NH * 64 * KVT_B;
constexpr size_t WS_YP = WS_AB + (size_t)20 * 256 * 1024;
constexpr size_t WS_O = WS_YP + (size_t)4 * 64 * 20 * 2048;
constexpr size_t WS_WO = WS_O + (size_t)128 * 20 * 1024;
constexpr size_t WS_G = WS_WO + (size_t)40 * 20 * 1024;
constexpr float QSCALE = 0.22360679774997896f * 1.4426950408889634f;

#define MFMA32(a, b, c) __builtin_amdgcn_mfma_f32_32x32x16_bf16((a), (b), (c), 0, 0, 0)
#define MFMA16(a, b, c) __builtin_amdgcn_mfma_f32_16x16x32_bf16((a), (b), (c), 0, 0, 0)
#define SBAR() __builtin_amdgcn_sched_barrier(0)

typedef __attribute__((ext_vector_type(2))) __bf16 bf16x2;
typedef __attribute__((ext_vector_type(2))) float f32x2;
DI unsigned pk2(float lo, float hi) {
    f32x2 v = {lo, hi};
    return __builtin_bit_cast(unsigned, __builtin_convertvector(v, bf16x2));
}
DI float bf2f(unsigned short v) { return __uint_as_float((unsigned)v << 16); }
DI void dma16(const void* sbase, unsigned voff, unsigned lds) {
    asm volatile("s_mov_b32 m0, %2\n\ts_nop 0\n\tglobal_load_lds_dwordx4 %0, %1" ::"v"(voff), "s"(sbase), "s"(lds) : "memory");
}
template <int N> DI void wait_vm() { asm volatile("s_waitcnt vmcnt(%0)" ::"i"(N) : "memory"); }
DI void bar() { asm volatile("s_waitcnt lgkmcnt(0)\n\ts_barrier" ::: "memory"); }
DI bf16x8 lds128(unsigned a) { return *(const LAS bf16x8*)(uintptr_t)a; }
DI u32x2 lds64(unsigned a) { return *(const LAS u32x2*)(uintptr_t)a; }
DI s16x4 ldstr(unsigned a) {
    typedef short v4i16_t __attribute__((ext_vector_type(4)));
    return __builtin_bit_cast(s16x4, __builtin_amdgcn_ds_read_tr16_b64_v4i16((LAS v4i16_t*)(uintptr_t)a));
}
DI u32x4 pack8(const f32x16& x, int s, float sc) {
    u32x4 p;
    p[0] = pk2(x[8 * s + 0] * sc, x[8 * s + 1] * sc);
    p[1] = pk2(x[8 * s + 2] * sc, x[8 * s + 3] * sc);
    p[2] = pk2(x[8 * s + 4] * sc, x[8 * s + 5] * sc);
    p[3] = pk2(x[8 * s + 6] * sc, x[8 * s + 7] * sc);
    return p;
}

constexpr int P_STG = 45056;
constexpr int P_LDS = 3 * P_STG;

DI void store_ab(const f32x16& acc, int dt, int stile, char* ws, int lane) {
    const float sc = dt >= 20 ? -1.f : 1.f;
    const int nt = dt >= 20 ? dt - 20 : dt;
    const int ks0 = (dt >= 20 ? 128 : 0) + stile * 2;
    char* dst = ws + WS_AB + ((size_t)nt * 256 + ks0) * 1024 + lane * 16;
#pragma unroll
    for (int s = 0; s < 2; ++s) {
        u32x4 p = pack8(acc, s, sc);
        auto r0 = __builtin_amdgcn_permlane32_swap(p[0], p[2], false, false);
        auto r1 = __builtin_amdgcn_permlane32_swap(p[1], p[3], false, false);
        u32x4 o = {r0[0], r1[0], r0[1], r1[1]};
        *(u32x4*)(dst + s * 1024) = o;
    }
}

template <int PATH>
DI void proj_body(const u16* __restrict__ x, const u16* __restrict__ wqkv, const u16* __restrict__ cd, const u16* __restrict__ sd,
                  char* __restrict__ ws, int rb, int grp, unsigned lds0) {
    const int tid = threadIdx.x, lane = tid & 63, r = lane & 31, h = lane >> 5;
    const int wid = __builtin_amdgcn_readfirstlane(tid >> 6);
    constexpr bool PATH_A = PATH < 2;
    constexpr int NW = PATH_A ? 5 : 0, ND = PATH_A ? 1 : 7, NT = NW + ND;
    constexpr int NDMA = 4 + (PATH_A ? 5 + 1 : 7);
    const int dt0 = PATH_A ? 28 + grp : 7 * grp;
    const unsigned vrow = (unsigned)(lane >> 3) * 1280u + (unsigned)(((lane & 7) ^ ((4 * (wid & 1) + (lane >> 4)) & 7)) * 16);
    const char* xb = (const char*)x + ((size_t)rb * 128 + wid * 8) * 1280;
    unsigned vw[5];
    if (PATH_A) {
#pragma unroll
        for (int j = 0; j < 5; ++j) { const int i = (wid + 4 * j) * 64 + lane; vw[j] = (unsigned)(i / 20) * 3840u + (unsigned)(i % 20) * 16u; }
    }
    const char* wb = (const char*)wqkv + grp * 320;
    const char* db[ND];
#pragma unroll
    for (int j = 0; j < ND; ++j) {
        const int dt = dt0 + j;
        db[j] = (dt >= 20 ? (const char*)sd + (size_t)(dt - 20) * 32 * 1280 : (const char*)cd + (size_t)dt * 32 * 1280) + wid * 8 * 1280;
    }
    auto issue = [&](int st) {
        const unsigned base = lds0 + (st % 3) * P_STG + wid * 1024;
#pragma unroll
        for (int j = 0; j < 4; ++j) dma16(xb + j * 32 * 1280 + st * 128, vrow, base + j * 4096);
        if (PATH_A) {
#pragma unroll
            for (int j = 0; j < 5; ++j) dma16(wb + (size_t)st * 64 * 3840, vw[j], base + 16384 + j * 4096);
        }
#pragma unroll
        for (int j = 0; j < ND; ++j) dma16(db[j] + st * 128, vrow, base + 16384 + NW * 4096 + j * 4096);
    };
    const unsigned sw = (unsigned)((h ^ ((r >> 1) & 7)) * 16);
    unsigned ax[4], ad[4];
#pragma unroll
    for (int ks = 0; ks < 4; ++ks) {
        ad[ks] = lds0 + 16384 + NW * 4096 + ((unsigned)r * 128 + (sw ^ (unsigned)(ks * 32)));
        ax[ks] = lds0 + wid * 4096 + ((unsigned)r * 128 + (sw ^ (unsigned)(ks * 32)));
    }
    const unsigned aw = lds0 + 16384 + (unsigned)(8 * h + ((lane & 15) >> 2)) * 320u + (unsigned)(((lane >> 4) & 1) * 32 + (lane & 3) * 8);

    f32x16 acc[NT];
#pragma unroll
    for (int t = 0; t < NT; ++t) acc[t] = f32x16{};
    bf16x8 fx[2], fw[2][NT];
    auto rd = [&](int st, int ks, int b) {
        const unsigned so = (st % 3) * P_STG;
        fx[b] = lds128(ax[ks] + so);
#pragma unroll
        for (int t = 0; t < NW; ++t) {
            s16x4 lo = ldstr(aw + so + ks * 16 * 320 + t * 64), hi = ldstr(aw + so + ks * 16 * 320 + 4 * 320 + t * 64);
            fw[b][t] = __builtin_bit_cast(bf16x8, __builtin_shufflevector(lo, hi, 0, 1, 2, 3, 4, 5, 6, 7));
        }
#pragma unroll
        for (int t = 0; t < ND; ++t) fw[b][NW + t] = lds128(ad[ks] + so + t * 4096);
    };
    constexpr bool qk = PATH == 0;
    auto mm = [&](int b) {
        if (qk) {
#pragma unroll
            for (int t = 0; t < NW; ++t) acc[t] = MFMA32(fw[b][t], fx[b], acc[t]);
        } else {
#pragma unroll
            for (int t = 0; t < NW; ++t) acc[t] = MFMA32(fx[b], fw[b][t], acc[t]);
        }
#pragma unroll
        for (int t = NW; t < NT; ++t) acc[t] = MFMA32(fx[b], fw[b][t], acc[t]);
    };
    issue(0); issue(1); issue(2);
    wait_vm<2 * NDMA>(); bar();
    rd(0, 0, 0);
#pragma unroll
    for (int st = 0; st < 10; ++st) {
        rd(st, 1, 1); mm(0);
        rd(st, 2, 0); mm(1);
        rd(st, 3, 1); mm(0);
        if (st < 9) {
            if (st < 8) wait_vm<NDMA>(); else wait_vm<0>();
            bar();
            if (st + 3 < 10) issue(st + 3);
            rd(st + 1, 0, 0);
        }
        mm(1);
    }
    const int stile = rb * 4 + wid;
    if (PATH_A) {
        if (PATH == 0) {
            const bool isq = grp < 4;
            const float sc = isq ? QSCALE : 1.f;
            const int h0 = (grp & 3) * 8;
            const size_t tb = isq ? QT_B : KVT_B;
            char* base = ws + (isq ? WS_Q : WS_KV) + ((size_t)h0 * 64 + stile) * tb;
#pragma unroll
            for (int m = 0; m < 4; ++m) {
                u32x2 q[5];
#pragma unroll
                for (int i = 0; i < 5; ++i) {
                    const int p = 5 * m + i, t = p >> 2, g = p & 3;
                    q[i][0] = pk2(acc[t][4 * g] * sc, acc[t][4 * g + 1] * sc);
                    q[i][1] = pk2(acc[t][4 * g + 2] * sc, acc[t][4 * g + 3] * sc);
                }
                char* e = base + (size_t)(2 * m) * 64 * tb;
                char* o = e + 64 * tb;
                *(u32x4*)(e + lane * 16) = u32x4{q[0][0], q[0][1], q[1][0], q[1][1]};
                *(u32x4*)(o + lane * 16) = u32x4{q[3][0], q[3][1], q[4][0], q[4][1]};
                *(u32x2*)((h ? o : e) + 1024 + r * 8) = q[2];
            }
        } else {
            const int h0 = (grp - 8) * 8;
            char* base = ws + WS_KV + ((size_t)h0 * 64 + stile) * KVT_B + 1280;
#pragma unroll
            for (int t = 0; t < 5; ++t) {
                const int c = 32 * t + r, hl = c / 20, d = c - hl * 20;
                char* dst = base + (size_t)hl * 64 * KVT_B + (h * 21 + d) * 16;
                *(u32x4*)(dst) = pack8(acc[t], 0, 1.f);
                *(u32x4*)(dst + 42 * 16) = pack8(acc[t], 1, 1.f);
            }
            if (lane < 32) *(u32x4*)(base + (size_t)(lane >> 2) * 64 * KVT_B + ((lane & 3) * 21 + 20) * 16) = u32x4{0x3F803F80u, 0x3F803F80u, 0x3F803F80u, 0x3F803F80u};
        }
    }
#pragma unroll
    for (int t = 0; t < ND; ++t) store_ab(acc[NW + t], dt0 + t, stile, ws, lane);
}

__global__ __launch_bounds__(256) void k_proj(const u16* __restrict__ x, const u16* __restrict__ wqkv, const u16* __restrict__ cd,
                                              const u16* __restrict__ sd, char* __restrict__ ws) {
    extern __shared__ __attribute__((aligned(16))) char smem[];
    const unsigned lds0 = (unsigned)(uintptr_t)smem;
    const int b = blockIdx.x, xcd = b & 7, i = b >> 3;
    const int rb = (xcd & 1) * 8 + (i & 7), cq = xcd >> 1, gl = i >> 3;
    if (gl == 3) proj_body<2>(x, wqkv, cd, sd, ws, rb, cq, lds0);
    else if (cq * 3 + gl < 8) proj_body<0>(x, wqkv, cd, sd, ws, rb, cq * 3 + gl, lds0);
    else proj_body<1>(x, wqkv, cd, sd, ws, rb, cq * 3 + gl, lds0);
}

constexpr int M_GSTG = 36864;
constexpr int M_KVSTG = 4 * KVT_B;
constexpr int M_KV0 = 3 * M_GSTG;
constexpr int M_LDS = M_KV0 + 4 * M_KVSTG;

DI float xhalf_max(float v) {
    auto rr = __builtin_amdgcn_permlane32_swap(__float_as_uint(v), __float_as_uint(v), false, false);
    return fmaxf(__uint_as_float(rr[0]), __uint_as_float(rr[1]));
}
DI float max16(const f32x16& s) {
    float a = fmaxf(fmaxf(s[0], s[1]), s[2]), b = fmaxf(fmaxf(s[3], s[4]), s[5]);
    a = fmaxf(fmaxf(a, s[6]), s[7]); b = fmaxf(fmaxf(b, s[8]), s[9]);
    a = fmaxf(fmaxf(a, s[10]), s[11]); b = fmaxf(fmaxf(b, s[12]), s[13]);
    a = fmaxf(fmaxf(a, s[14]), s[15]);
    return fmaxf(a, b);
}
DI bf16x8 as_frag(u32x4 v) { return __builtin_bit_cast(bf16x8, v); }
DI void exp_pack(const f32x16& s, u32x4& p0, u32x4& p1) {
#pragma unroll
    for (int i = 0; i < 4; ++i) {
        p0[i] = pk2(__builtin_amdgcn_exp2f(s[2 * i]), __builtin_amdgcn_exp2f(s[2 * i + 1]));
        p1[i] = pk2(__builtin_amdgcn_exp2f(s[8 + 2 * i]), __builtin_amdgcn_exp2f(s[8 + 2 * i + 1]));
    }
}

DI f32x16 attn_safe(const char* kvh, u32x4 qa, u32x4 qb, int lane, unsigned voff) {
    const int r = lane & 31;
    qb[2] = 0u;
    f32x16 o = f32x16{};
    float m = -INFINITY;
    for (int t = 0; t < 64; ++t) {
        const char* kt = kvh + (size_t)t * KVT_B;
        const u32x4 ka = *(const u32x4*)(kt + lane * 16);
        const u32x2 kb = *(const u32x2*)(kt + 1024 + r * 8);
        const u32x4 v0 = *(const u32x4*)(kt + voff), v1 = *(const u32x4*)(kt + voff + 42 * 16);
        f32x16 s = MFMA32(as_frag(ka), as_frag(qa), f32x16{});
        s = MFMA32(as_frag(u32x4{kb[0], kb[1], 0u, 0u}), as_frag(qb), s);
        const float mn = fmaxf(m, xhalf_max(max16(s)));
        const float al = __builtin_amdgcn_exp2f(m - mn);
        m = mn;
#pragma unroll
        for (int i = 0; i < 16; ++i) { o[i] *= al; s[i] -= mn; }
        u32x4 p0, p1;
        exp_pack(s, p0, p1);
        o = MFMA32(as_frag(v0), as_frag(p0), o);
        o = MFMA32(as_frag(v1), as_frag(p1), o);
    }
    return o;
}

DI void mix_attn(char* __restrict__ ws, int head, int qb, int wid, unsigned lds0, char* smem) {
    const int lane = threadIdx.x & 63, r = lane & 31, h = lane >> 5;
    const int kh = wid >> 2;
    const int qt0 = qb * 8 + (wid & 3) * 2;
    __builtin_amdgcn_s_setprio(2);
    u32x4 qa[2], qbf[2];
#pragma unroll
    for (int i = 0; i < 2; ++i) {
        const char* qp = ws + WS_Q + ((size_t)head * 64 + qt0 + i) * QT_B;
        qa[i] = *(const u32x4*)(qp + lane * 16);
        const u32x2 t = *(const u32x2*)(qp + 1024 + r * 8);
        qbf[i] = h ? u32x4{0u, 0u, 0u, 0u} : u32x4{t[0], t[1], 0u, 0u};
    }
    const int rv = r <= 20 ? r : (r < 28 ? 20 : 16);
    const unsigned voff = 1280u + (unsigned)(h * 21 + rv) * 16u;
    const unsigned aka = lds0 + M_KV0 + lane * 16, akb = lds0 + M_KV0 + 1024 + r * 8, av = lds0 + M_KV0 + voff;
    bar();
    {
        const bf16x8 ka = lds128(aka);
        const u32x2 kb = lds64(akb);
#pragma unroll
        for (int i = 0; i < 2; ++i) {
            f32x16 s = MFMA32(ka, as_frag(qa[i]), f32x16{});
            s = MFMA32(as_frag(u32x4{kb[0], kb[1], 0u, 0u}), as_frag(qbf[i]), s);
            const float m = xhalf_max(max16(s));
            qbf[i][2] = h ? 0u : (pk2(-m, 0.f) & 0xFFFFu);
        }
    }
    f32x16 oA = f32x16{}, oB = f32x16{}, sA, sB;
    bf16x8 ka, v0, v1;
    u32x4 kb = {0u, 0u, 0x00003F80u, 0u};
    {
        const unsigned o = kh * 2 * KVT_B;
        ka = lds128(aka + o);
        const u32x2 t = lds64(akb + o); kb[0] = t[0]; kb[1] = t[1];
        sA = MFMA32(ka, as_frag(qa[0]), f32x16{});
        sA = MFMA32(as_frag(kb), as_frag(qbf[0]), sA);
    }
    unsigned cur = kh * 2 * KVT_B;
    for (int st = 0; st < 16; ++st) {
#pragma unroll
        for (int j = 0; j < 2; ++j) {
            v0 = lds128(av + cur); v1 = lds128(av + cur + 42 * 16);
            sB = MFMA32(ka, as_frag(qa[1]), f32x16{});
            sB = MFMA32(as_frag(kb), as_frag(qbf[1]), sB);
            unsigned nxt;
            if (j == 0) nxt = cur + KVT_B;
            else {
                nxt = ((st + 1) & 3) * M_KVSTG + kh * 2 * KVT_B;
                bar();
            }
            ka = lds128(aka + nxt);
            { const u32x2 t = lds64(akb + nxt); kb[0] = t[0]; kb[1] = t[1]; }
            u32x4 p0, p1;
            exp_pack(sA, p0, p1);
            oA = MFMA32(v0, as_frag(p0), oA);
            oA = MFMA32(v1, as_frag(p1), oA);
            sA = MFMA32(ka, as_frag(qa[0]), f32x16{});
            sA = MFMA32(as_frag(kb), as_frag(qbf[0]), sA);
            exp_pack(sB, p0, p1);
            oB = MFMA32(v0, as_frag(p0), oB);
            oB = MFMA32(v1, as_frag(p1), oB);
            cur = nxt;
        }
    }
    float* xch = (float*)smem + (wid & 3) * 2048 + lane;
    if (kh == 1) {
#pragma unroll
        for (int i = 0; i < 12; ++i) { xch[i * 64] = oA[i]; xch[(16 + i) * 64] = oB[i]; }
    }
    bar();
    if (kh == 1) return;
#pragma unroll
    for (int i = 0; i < 12; ++i) { oA[i] += xch[i * 64]; oB[i] += xch[(16 + i) * 64]; }
#pragma unroll
    for (int i = 0; i < 2; ++i) {
        f32x16 o = i ? oB : oA;
        auto rl = __builtin_amdgcn_permlane32_swap(__float_as_uint(o[8]), __float_as_uint(o[8]), false, false);
        float l = __uint_as_float(rl[1]);
        float amax = fmaxf(fmaxf(fabsf(o[0]), fabsf(o[1])), fmaxf(fabsf(o[2]), fabsf(o[3])));
        bool fin = true;
#pragma unroll
        for (int k = 0; k < 8; ++k) fin = fin && (fabsf(o[k]) <= 3.0e38f);
        if (h == 0) {
#pragma unroll
            for (int k = 8; k < 12; ++k) fin = fin && (fabsf(o[k]) <= 3.0e38f);
        }
        (void)amax;
        const bool ok = fin && (l >= 8.6736174e-19f) && (l <= 1.2676506e30f);
        if (__builtin_expect(!__all(ok), 0)) {
            o = attn_safe(ws + WS_KV + (size_t)head * 64 * KVT_B, qa[i], qbf[i], lane, voff);
            rl = __builtin_amdgcn_permlane32_swap(__float_as_uint(o[8]), __float_as_uint(o[8]), false, false);
            l = __uint_as_float(rl[1]);
        }
        const float inv = 1.0f / l;
        const int s = (qt0 + i) * 32 + r;
        char* ob = ws + WS_O + (size_t)(s >> 4) * 20 * 1024 + (s & 15) * 16;
#pragma unroll
        for (int g = 0; g < 3; ++g) {
            const int qd = 5 * head + 2 * g + h;
            u32x2 w = {pk2(o[4 * g] * inv, o[4 * g + 1] * inv), pk2(o[4 * g + 2] * inv, o[4 * g + 3] * inv)};
            if (g < 2 || h == 0) *(u32x2*)(ob + (qd >> 3) * 1024 + ((qd >> 1) & 3) * 256 + (qd & 1) * 8) = w;
        }
    }
}

DI void mix_gemm(const u16* __restrict__ x, const u16* __restrict__ wo, const u16* __restrict__ cs, const u16* __restrict__ ss,
                 const u16* __restrict__ gw, char* __restrict__ ws, int head, int tb, int eg, int sp, int gwid, unsigned lds0) {
    const int lane = threadIdx.x & 63, r = lane & 31, h = lane >> 5;
    const unsigned vrow = (unsigned)(lane >> 3) * 4096u + (unsigned)(((lane & 7) ^ ((4 * (gwid & 1) + (lane >> 4)) & 7)) * 16);
    const char* ab_ = (const char*)(sp >= 2 ? ss : cs) + ((size_t)tb * 128 + gwid * 8) * 4096 + (sp & 1) * 2048;
    const char* bb = ws + WS_AB + ((size_t)eg * 5 * 256 + sp * 64 + gwid) * 1024;
    const char* kvb = ws + WS_KV + ((size_t)head * 64 + gwid) * KVT_B;
    const unsigned vl = lane * 16;
    auto issue = [&](int st, int slot3) {
        const unsigned base = lds0 + slot3 * M_GSTG + gwid * 1024;
#pragma unroll
        for (int j = 0; j < 4; ++j) dma16(ab_ + (size_t)j * 32 * 4096 + st * 128, vrow, base + j * 4096);
#pragma unroll
        for (int j = 0; j < 5; ++j) dma16(bb + (size_t)j * 256 * 1024 + st * 4096, vl, base + 16384 + j * 4096);
        const unsigned kd = lds0 + M_KV0 + (st & 3) * M_KVSTG + gwid * KVT_B;
        const char* ks = kvb + (size_t)st * M_KVSTG;
        dma16(ks, vl, kd);
        dma16(ks + 1024, vl, kd + 1024);
        if (lane < 36) dma16(ks + 2048, vl, kd + 2048);
    };
    issue(0, 0); issue(1, 1); issue(2, 2);
    {
        const int gi = blockIdx.x * 4 + gwid;
        const int row = gi * 2 + h;
        float acc = 0.f;
        if (r < 20) {
            const u32x4* xp = (const u32x4*)(x + (size_t)row * DM + r * 32);
            const u32x4* gp = (const u32x4*)(gw + r * 32);
#pragma unroll
            for (int c = 0; c < 4; ++c) {
                const u32x4 a = xp[c], b = gp[c];
#pragma unroll
                for (int k = 0; k < 4; ++k) {
                    acc += __uint_as_float(a[k] << 16) * __uint_as_float(b[k] << 16);
                    acc += __uint_as_float(a[k] & 0xFFFF0000u) * __uint_as_float(b[k] & 0xFFFF0000u);
                }
            }
        }
#pragma unroll
        for (int o = 16; o > 0; o >>= 1) acc += __shfl_xor(acc, o);
        if (r == 0) ((float*)(ws + WS_G))[row] = 1.0f / (1.0f + __expf(-acc));
        if (gi < 800) {
            const int nt = gi / 20, ks = gi - nt * 20;
            const u16* src = wo + (size_t)(ks * 32 + (lane >> 4) * 8) * DM + nt * 16 + (lane & 15);
            u32x4 w;
#pragma unroll
            for (int k = 0; k < 4; ++k) w[k] = (unsigned)src[(2 * k) * DM] | ((unsigned)src[(2 * k + 1) * DM] << 16);
            *(u32x4*)(ws + WS_WO + (size_t)gi * 1024 + lane * 16) = w;
        }
    }
    const unsigned sw = (unsigned)((h ^ ((r >> 1) & 7)) * 16);
    unsigned aa[4];
#pragma unroll
    for (int ks = 0; ks < 4; ++ks) aa[ks] = lds0 + gwid * 4096 + ((unsigned)r * 128 + (sw ^ (unsigned)(ks * 32)));
    const unsigned abf = lds0 + 16384 + lane * 16;
    f32x16 acc[5];
#pragma unroll
    for (int t = 0; t < 5; ++t) acc[t] = f32x16{};
    bf16x8 fa[2], fb[2][5];
    auto rd = [&](unsigned so, int ks, int b) {
        fa[b] = lds128(aa[ks] + so);
#pragma unroll
        for (int t = 0; t < 5; ++t) fb[b][t] = lds128(abf + so + t * 4096 + ks * 1024);
    };
    auto mm = [&](int b) {
#pragma unroll
        for (int t = 0; t < 5; ++t) acc[t] = MFMA32(fb[b][t], fa[b], acc[t]);
    };
    wait_vm<0>();
    bar();
    int slot = 0;
    rd(0, 0, 0);
    for (int st = 0; st < 16; ++st) {
        const unsigned so = slot * M_GSTG;
        const int nslot = slot == 2 ? 0 : slot + 1;
        rd(so, 1, 1); mm(0);
        rd(so, 2, 0); mm(1);
        rd(so, 3, 1); mm(0);
        if (st < 14) wait_vm<12>(); else wait_vm<0>();
        bar();
        if (st + 3 < 16) issue(st + 3, slot);
        if (st < 15) rd(nslot * M_GSTG, 0, 0);
        mm(1);
        slot = nslot;
    }
    bar();
    char* yp = ws + WS_YP + ((((size_t)sp * 64 + tb * 4 + gwid) * 20) + eg * 5) * 2048 + lane * 16;
#pragma unroll
    for (int t = 0; t < 5; ++t) {
        *(u32x4*)(yp + t * 2048) = pack8(acc[t], 0, 1.f);
        *(u32x4*)(yp + t * 2048 + 1024) = pack8(acc[t], 1, 1.f);
    }
}

__global__ __launch_bounds__(768) void k_mix(const u16* __restrict__ x, const u16* __restrict__ wo, const u16* __restrict__ cs,
                                             const u16* __restrict__ ss, const u16* __restrict__ gw, char* __restrict__ ws) {
    extern __shared__ __attribute__((aligned(16))) char smem[];
    const unsigned lds0 = (unsigned)(uintptr_t)smem;
    const int wid = __builtin_amdgcn_readfirstlane(threadIdx.x >> 6);
    const int b = blockIdx.x, xcd = b & 7, i = b >> 3;
    const int head = xcd * 4 + (i >> 3);
    if (wid < 8) mix_attn(ws, head, i & 7, wid, lds0, smem);
    else mix_gemm(x, wo, cs, ss, gw, ws, head, (xcd >> 2) * 8 + (i & 7), i >> 3, xcd & 3, wid - 8, lds0);
}

constexpr int O_STG = 36864;
constexpr int O_LDS = 4 * O_STG;

__global__ __launch_bounds__(256) void k_out(const char* __restrict__ ws, u16* __restrict__ out) {
    extern __shared__ __attribute__((aligned(16))) char smem[];
    const unsigned lds0 = (unsigned)(uintptr_t)smem;
    const int lane = threadIdx.x & 63, c = lane & 15, q = lane >> 4;
    const int wid = __builtin_amdgcn_readfirstlane(threadIdx.x >> 6);
    const int b = blockIdx.x, xcd = b & 7, i = b >> 3;
    const int sb = xcd * 4 + (i & 3), ng = i >> 2;
    const char* ob = ws + WS_O + ((size_t)sb * 4 * 20 + wid) * 1024;
    const char* wb = ws + WS_WO + ((size_t)ng * 5 * 20 + wid) * 1024;
    const unsigned vl = lane * 16;
    auto issue = [&](int st) {
        const unsigned base = lds0 + (st & 3) * O_STG + wid * 1024;
#pragma unroll
        for (int j = 0; j < 4; ++j) dma16(ob + (j * 20 + st * 4) * 1024, vl, base + j * 4096);
#pragma unroll
        for (int j = 0; j < 5; ++j) dma16(wb + (j * 20 + st * 4) * 1024, vl, base + 16384 + j * 4096);
    };
    issue(0); issue(1); issue(2); issue(3);
    const int s = sb * 64 + wid * 16 + c;
    f32x4 acc[5];
#pragma unroll
    for (int t = 0; t < 5; ++t) acc[t] = f32x4{};
#pragma unroll
    for (int st = 0; st < 5; ++st) {
        if (st == 0) wait_vm<27>(); else if (st == 1) wait_vm<27>(); else if (st == 2) wait_vm<18>(); else if (st == 3) wait_vm<9>(); else wait_vm<0>();
        bar();
        const unsigned so = lds0 + (st & 3) * O_STG + vl;
#pragma unroll
        for (int ks = 0; ks < 4; ++ks) {
            const bf16x8 fo = lds128(so + wid * 4096 + ks * 1024);
#pragma unroll
            for (int t = 0; t < 5; ++t) acc[t] = MFMA16(lds128(so + 16384 + t * 4096 + ks * 1024), fo, acc[t]);
        }
        if (st == 0) { bar(); issue(4); }
    }
    const float g = ((const float*)(ws + WS_G))[s];
#pragma unroll
    for (int t = 0; t < 5; ++t) {
        const int n = ng * 80 + t * 16 + q * 4;
        const int nq = (n & 31) >> 2, gg = nq >> 1;
        const char* yp = ws + WS_YP + (((size_t)(s >> 5) * 20 + (n >> 5)) * 2 + (gg >> 1)) * 1024 + ((nq & 1) * 32 + (s & 31)) * 16 + (gg & 1) * 8;
        float y0 = 0.f, y1 = 0.f, y2 = 0.f, y3 = 0.f;
#pragma unroll
        for (int sp = 0; sp < 4; ++sp) {
            const u32x2 v = *(const u32x2*)(yp + (size_t)sp * 64 * 20 * 2048);
            y0 += __uint_as_float(v[0] << 16); y1 += __uint_as_float(v[0] & 0xFFFF0000u);
            y2 += __uint_as_float(v[1] << 16); y3 += __uint_as_float(v[1] & 0xFFFF0000u);
        }
        const float gm = 1.0f - g;
        u32x2 w = {pk2(g * acc[t][0] + gm * y0, g * acc[t][1] + gm * y1), pk2(g * acc[t][2] + gm * y2, g * acc[t][3] + gm * y3)};
        *(u32x2*)(out + (size_t)s * DM + n) = w;
    }
}

extern "C" void kernel_launch(void* const* d_in, const int* in_sizes, int n_in, void* d_out, int out_size, void* d_ws, size_t ws_size,
                              hipStream_t stream) {
    const u16* x = (const u16*)d_in[0];
    const u16* wqkv = (const u16*)d_in[1];
    const u16* wo = (const u16*)d_in[2];
    const u16* cs = (const u16*)d_in[4];
    const u16* ss = (const u16*)d_in[5];
    const u16* cd = (const u16*)d_in[6];
    const u16* sd = (const u16*)d_in[7];
    const u16* gw = (const u16*)d_in[8];
    char* ws = (char*)d_ws;
    (void)hipFuncSetAttribute((const void*)k_proj, hipFuncAttributeMaxDynamicSharedMemorySize, P_LDS);
    (void)hipFuncSetAttribute((const void*)k_mix, hipFuncAttributeMaxDynamicSharedMemorySize, M_LDS);
    (void)hipFuncSetAttribute((const void*)k_out, hipFuncAttributeMaxDynamicSharedMemorySize, O_LDS);
    k_proj<<<256, 256, P_LDS, stream>>>(x, wqkv, cd, sd, ws);
    k_mix<<<256, 768, M_LDS, stream>>>(x, wo, cs, ss, gw, ws);
    k_out<<<256, 256, O_LDS, stream>>>(ws, (u16*)d_out);
}
```

```cpp
#include <hip/hip_runtime.h>
#include <hip/hip_bf16.h>
#include <stdint.h>

typedef unsigned short u16;
typedef __attribute__((ext_vector_type(8))) __bf16 bf16x8;
typedef __attribute__((ext_vector_type(4))) short s16x4;
typedef __attribute__((ext_vector_type(16))) float f32x16;
typedef __attribute__((ext_vector_type(4))) float f32x4;
typedef __attribute__((ext_vector_type(4))) unsigned u32x4;
typedef __attribute__((ext_vector_type(2))) unsigned u32x2;
#define LAS __attribute__((address_space(3)))
#define DI __device__ __forceinline__
typedef const LAS char* lptr;

constexpr int SEQ = 2048, DM = 640, NH = 32;
constexpr unsigned WS_QA = 0;
constexpr unsigned WS_KA = WS_QA + NH * 64 * 1024;
constexpr unsigned WS_QB = WS_KA + NH * 64 * 1024;
constexpr unsigned WS_KB = WS_QB + NH * 64 * 512;
constexpr unsigned WS_VV = WS_KB + NH * 64 * 512;
constexpr unsigned WS_AB = WS_VV + 64 * 4 * 640 * 16;
constexpr unsigned WS_YP = WS_AB + 20 * 256 * 1024;
constexpr unsigned WS_OA = WS_YP + 4 * 64 * 20 * 2048;
constexpr unsigned WS_OB = WS_OA + 64 * NH * 1024;
constexpr unsigned WS_WO = WS_OB + 64 * NH * 256;
constexpr unsigned WS_G = WS_WO + 40 * 20 * 1024;
constexpr unsigned WS_ONE = WS_G + SEQ * 4;
constexpr unsigned WS_END = WS_ONE + 128;
constexpr int KVT_B = 2880;
constexpr float QSCALE = 0.22360679774997896f * 1.4426950408889634f;
#define ST_AUX 16

#define TR_DECL do {} while (0)
#define TR() do {} while (0)
#define TR_FLUSH(k) do {} while (0)
#define STAMP(k, i) do {} while (0)
#define STAMP_END(k) do {} while (0)
#define MFMA32(a, b, c) __builtin_amdgcn_mfma_f32_32x32x16_bf16((a), (b), (c), 0, 0, 0)
#define MFMA16(a, b, c) __builtin_amdgcn_mfma_f32_16x16x32_bf16((a), (b), (c), 0, 0, 0)
#define SBAR() __builtin_amdgcn_sched_barrier(0)
#define PIN(x) asm volatile("" : "+v"(x))
#define KEEP(x) asm volatile("" ::"v"(x))

typedef __attribute__((ext_vector_type(2))) __bf16 bf16x2;
typedef __attribute__((ext_vector_type(2))) float f32x2;
DI unsigned pk2(float lo, float hi) {
    f32x2 v = {lo, hi};
    return __builtin_bit_cast(unsigned, __builtin_convertvector(v, bf16x2));
}
DI float bflo(unsigned v) { return __uint_as_float(v << 16); }
DI float bfhi(unsigned v) { return __uint_as_float(v & 0xFFFF0000u); }
DI void dma16(const void* sbase, unsigned voff, unsigned lds) {
    asm volatile("s_mov_b32 m0, %2\n\ts_nop 0\n\tglobal_load_lds_dwordx4 %0, %1" ::"v"(voff), "s"(sbase), "s"(lds) : "memory");
}
template <int N> DI void dma16_lo(const void* sbase, unsigned voff, unsigned lds) {
    unsigned long long sv;
    asm volatile("s_mov_b32 m0, %3\n\ts_mov_b64 %0, exec\n\ts_mov_b64 exec, %4\n\tglobal_load_lds_dwordx4 %1, %2\n\ts_mov_b64 exec, %0"
                 : "=&s"(sv) : "v"(voff), "s"(sbase), "s"(lds), "i"((1 << N) - 1) : "memory");
}
template <int N> DI void wait_vm() { asm volatile("s_waitcnt vmcnt(%0)" ::"i"(N) : "memory"); }
DI void bar() { asm volatile("s_waitcnt lgkmcnt(0)\n\ts_barrier" ::: "memory"); }
DI void bar_raw() { asm volatile("s_barrier" ::: "memory"); }
DI lptr lbase(unsigned a) { asm volatile("" : "+v"(a)); return (lptr)(uintptr_t)a; }
DI bf16x8 lds128(lptr p) { return *(const LAS bf16x8*)p; }
DI u32x2 lds64(lptr p) { return *(const LAS u32x2*)p; }
DI s16x4 ldstr(lptr p) {
    typedef short v4i16_t __attribute__((ext_vector_type(4)));
    return __builtin_bit_cast(s16x4, __builtin_amdgcn_ds_read_tr16_b64_v4i16((LAS v4i16_t*)p));
}
DI u32x4 pack8(const f32x16& x, int s, float sc) {
    u32x4 p;
    p[0] = pk2(x[8 * s + 0] * sc, x[8 * s + 1] * sc);
    p[1] = pk2(x[8 * s + 2] * sc, x[8 * s + 3] * sc);
    p[2] = pk2(x[8 * s + 4] * sc, x[8 * s + 5] * sc);
    p[3] = pk2(x[8 * s + 6] * sc, x[8 * s + 7] * sc);
    return p;
}
typedef __amdgpu_buffer_rsrc_t rsrc_t;
DI rsrc_t ws_rsrc(const void* ws) { return __builtin_amdgcn_make_buffer_rsrc((void*)ws, (short)0, (int)WS_END, 0x00020000); }
DI void st128(rsrc_t r, unsigned voff, unsigned soff, u32x4 v) { __builtin_amdgcn_raw_buffer_store_b128(v, r, voff + soff, 0, ST_AUX); }
DI void st64(rsrc_t r, unsigned voff, unsigned soff, u32x2 v) { __builtin_amdgcn_raw_buffer_store_b64(v, r, voff, soff, ST_AUX); }
DI void st32(rsrc_t r, unsigned voff, unsigned soff, unsigned v) { __builtin_amdgcn_raw_buffer_store_b32(v, r, voff, soff, ST_AUX); }
DI bf16x8 as_frag(u32x4 v) { return __builtin_bit_cast(bf16x8, v); }
typedef __attribute__((ext_vector_type(16))) __bf16 bf16x16;
template <int HI> DI f32x16 pv_mma(bf16x8 v, const f32x16& p, const f32x16& o, int idx) {
    const auto ph = __builtin_shufflevector(p, p, 8 * HI, 8 * HI + 1, 8 * HI + 2, 8 * HI + 3, 8 * HI + 4, 8 * HI + 5, 8 * HI + 6, 8 * HI + 7);
    return __builtin_amdgcn_smfmac_f32_32x32x32_bf16(v, __builtin_bit_cast(bf16x16, ph), o, idx, 0, 0);
}
DI u32x4 pack8n(const f32x16& x, int s, float sc) {
    const u32x4 p = pack8(x, s, sc);
    const auto r0 = __builtin_amdgcn_permlane32_swap(p[0], p[2], false, false);
    const auto r1 = __builtin_amdgcn_permlane32_swap(p[1], p[3], false, false);
    return u32x4{r0[0], r1[0], r0[1], r1[1]};
}

constexpr int P_STG = 45056;
constexpr int P_LDS = 3 * P_STG;

DI void store_ab(const f32x16& acc, int dt, int stile, rsrc_t ws, int lane) {
    const float sc = dt >= 20 ? -1.f : 1.f;
    const int nt = dt >= 20 ? dt - 20 : dt;
    const int ks0 = (dt >= 20 ? 128 : 0) + stile * 2;
    const unsigned dst = WS_AB + (unsigned)(nt * 256 + ks0) * 1024u;
#pragma unroll
    for (int s = 0; s < 2; ++s) st128(ws, lane * 16 + s * 1024, dst, pack8n(acc, s, sc));
}

template <bool PATH_A>
DI void proj_load(const u16* __restrict__ x, const u16* __restrict__ wqkv, const u16* __restrict__ cd, const u16* __restrict__ sd,
                  int rb, int grp, unsigned lds0, int wid) {
    const int lane = threadIdx.x & 63;
    constexpr int ND = PATH_A ? 1 : 7, NDMA = PATH_A ? 10 : 11;
    const int dt0 = PATH_A ? 28 + grp : 7 * grp;
    const unsigned vrow = (unsigned)(lane >> 3) * 1280u + (unsigned)(((lane & 7) ^ ((4 * (wid & 1) + (lane >> 4)) & 7)) * 16);
    const char* xb = (const char*)x + ((size_t)rb * 128 + wid * 8) * 1280;
    unsigned vw[5];
    if (PATH_A) {
#pragma unroll
        for (int j = 0; j < 5; ++j) { const int i = (wid + 4 * j) * 64 + lane; vw[j] = (unsigned)(i / 20) * 3840u + (unsigned)(i % 20) * 16u; }
    }
    const char* wb = (const char*)wqkv + grp * 320;
    const char* db[ND];
#pragma unroll
    for (int j = 0; j < ND; ++j) {
        const int dt = dt0 + j;
        db[j] = (dt >= 20 ? (const char*)sd + (size_t)(dt - 20) * 32 * 1280 : (const char*)cd + (size_t)dt * 32 * 1280) + wid * 8 * 1280;
    }
    unsigned slot = lds0 + wid * 1024;
    auto issue = [&](int st) {
#pragma unroll
        for (int j = 0; j < 4; ++j) dma16(xb + j * 32 * 1280 + st * 128, vrow, slot + j * 4096);
#pragma unroll
        for (int j = 0; j < ND; ++j) dma16(db[j] + st * 128, vrow, slot + 16384 + j * 4096);
        if (PATH_A) {
#pragma unroll
            for (int j = 0; j < 5; ++j) dma16(wb + (size_t)st * 64 * 3840, vw[j], slot + 20480 + j * 4096);
        }
        slot = slot + P_STG == lds0 + wid * 1024 + P_LDS ? lds0 + wid * 1024 : slot + P_STG;
    };
#pragma unroll 1
    for (int st = 0; st < 10; ++st) {
        issue(st);
        if (st == 2) { wait_vm<2 * NDMA>(); bar_raw(); }
        if (st >= 2) { wait_vm<NDMA>(); bar_raw(); }
    }
    wait_vm<0>(); bar_raw();
    bar_raw();
}

template <int PATH>
DI void proj_mma(char* __restrict__ ws, int rb, int grp, unsigned lds0, int wid) {
    const int lane = threadIdx.x & 63, r = lane & 31, h = lane >> 5;
    constexpr bool PATH_A = PATH < 2;
    constexpr int NW = PATH_A ? 5 : 0, ND = PATH_A ? 1 : 7, NT = NW + ND;
    const int dt0 = PATH_A ? 28 + grp : 7 * grp;
    const unsigned sw = lds0 + (unsigned)r * 128u + (unsigned)((h ^ ((r >> 1) & 7)) * 16);
    const unsigned awo = lds0 + 20480u + (unsigned)(8 * h + ((lane & 15) >> 2)) * 320u + (unsigned)(((lane >> 4) & 1) * 32 + (lane & 3) * 8);
    f32x16 acc[NT];
#pragma unroll
    for (int t = 0; t < NT; ++t) acc[t] = f32x16{};
    bf16x8 fx[2], fw[2][NT];
    lptr pr, px, pw;
    auto setp = [&](unsigned so, int ks) {
        pr = lbase((sw ^ (unsigned)(ks * 32)) + so);
        px = lbase((sw ^ (unsigned)(ks * 32)) + so + wid * 4096);
        if (PATH_A && ks == 0) pw = lbase(awo + so);
    };
    auto rd = [&](int ks, int b, int t) {
        if (t == 0) fx[b] = lds128(px);
        if (t < ND) fw[b][t] = lds128(pr + 16384 + t * 4096);
        else {
            s16x4 lo = ldstr(pw + ks * 16 * 320 + (t - ND) * 64), hi = ldstr(pw + ks * 16 * 320 + 4 * 320 + (t - ND) * 64);
            fw[b][t] = __builtin_bit_cast(bf16x8, __builtin_shufflevector(lo, hi, 0, 1, 2, 3, 4, 5, 6, 7));
        }
    };
    auto mm = [&](int b, int t) {
        if (PATH == 0 && t >= ND) acc[t] = MFMA32(fw[b][t], fx[b], acc[t]);
        else acc[t] = MFMA32(fx[b], fw[b][t], acc[t]);
    };
    STAMP(0, 0);
    TR_DECL;
    bar_raw();
    STAMP(0, 1);
    TR();
    unsigned so = 0;
    setp(0, 0);
#pragma unroll
    for (int t = 0; t < NT; ++t) rd(0, 0, t);
    bf16x8 lastw = {}, lastx = {};
    PIN(lastw); PIN(lastx);
    SBAR();
#pragma unroll 1
    for (int st = 0; st < 10; ++st) {
#pragma unroll
        for (int ks = 0; ks < 3; ++ks) {
            setp(so, ks + 1);
#pragma unroll
            for (int t = 0; t < NT; ++t) {
                const bf16x8 k0 = t ? fw[ks & 1][t ? t - 1 : 0] : lastw, k1 = t ? fx[ks & 1] : lastx;
                rd(ks + 1, (ks + 1) & 1, t); KEEP(k0); KEEP(k1); SBAR(); mm(ks & 1, t); SBAR();
            }
            lastw = fw[ks & 1][NT - 1]; lastx = fx[ks & 1];
        }
        constexpr int NB = NT - 3;
#pragma unroll
        for (int t = 0; t < NB; ++t) { mm(1, t); SBAR(); }
        TR();
        bar();
        TR();
        so = so == 2 * P_STG ? 0 : so + P_STG;
        setp(so, 0);
#pragma unroll
        for (int t = NB; t < NT; ++t) {
#pragma unroll
            for (int u = (t - NB) * NT / 3; u < (t - NB + 1) * NT / 3; ++u) rd(0, 0, u);
            KEEP(fw[1][t - 1]);
            SBAR(); mm(1, t); SBAR();
        }
        lastw = fw[1][NT - 1]; lastx = fx[1];
    }
    TR(); TR_FLUSH(0);
    STAMP(0, 2);
    const rsrc_t wr = ws_rsrc(ws);
    const int stile = rb * 4 + wid;
    if (PATH == 0) {
        const float sc = grp < 4 ? QSCALE : 1.f;
        const unsigned t0 = (unsigned)(grp * 8 * 64 + stile);
        const unsigned va = WS_QA + t0 * 1024 + lane * 16, vb = WS_QB + (t0 + h * 64) * 512 + r * 16;
#pragma unroll
        for (int m = 0; m < 4; ++m) {
            u32x2 q[5];
#pragma unroll
            for (int i = 0; i < 5; ++i) {
                const int p = 5 * m + i, t = ND + (p >> 2), g = p & 3;
                q[i][0] = pk2(acc[t][4 * g] * sc, acc[t][4 * g + 1] * sc);
                q[i][1] = pk2(acc[t][4 * g + 2] * sc, acc[t][4 * g + 3] * sc);
            }
            st128(wr, va, (2 * m) * 64 * 1024, u32x4{q[0][0], q[0][1], q[1][0], q[1][1]});
            st128(wr, va, (2 * m + 1) * 64 * 1024, u32x4{q[3][0], q[3][1], q[4][0], q[4][1]});
            st128(wr, vb, (2 * m) * 64 * 512, u32x4{q[2][0], q[2][1], 0x00003F80u, 0u});
        }
    } else if (PATH == 1) {
        const unsigned vv = WS_VV + (unsigned)((stile * 4 + h) * 640 + (grp - 8) * 160 + r) * 16;
#pragma unroll
        for (int t = 0; t < 5; ++t) {
            st128(wr, vv, t * 512, pack8n(acc[ND + t], 0, 1.f));
            st128(wr, vv, t * 512 + 2 * 640 * 16, pack8n(acc[ND + t], 1, 1.f));
        }
    }
#pragma unroll
    for (int t = 0; t < ND; ++t) store_ab(acc[t], dt0 + t, stile, wr, lane);
    STAMP_END(0);
}

__global__ __launch_bounds__(512) void k_proj(const u16* __restrict__ x, const u16* __restrict__ wqkv, const u16* __restrict__ cd,
                                              const u16* __restrict__ sd, const u16* __restrict__ wo, const u16* __restrict__ gw,
                                              char* __restrict__ ws) {
    extern __shared__ __attribute__((aligned(16))) char smem[];
    const unsigned lds0 = (unsigned)(uintptr_t)smem;
    const int wid = __builtin_amdgcn_readfirstlane(threadIdx.x >> 6);
    const int b = blockIdx.x, xcd = b & 7, i = b >> 3;
    const int rb = (xcd & 1) * 8 + (i & 7), cq = xcd >> 1, gl = i >> 3;
    if (wid < 4) {
        if (gl == 3) proj_mma<2>(ws, rb, cq, lds0, wid);
        else if (cq * 3 + gl < 8) proj_mma<0>(ws, rb, cq * 3 + gl, lds0, wid);
        else proj_mma<1>(ws, rb, cq * 3 + gl, lds0, wid);
        return;
    }
    const int lane = threadIdx.x & 63, r = lane & 31, h = lane >> 5;
    const int gi = b * 4 + wid - 4;
    const int grow = gi * 2 + h;
    u32x2 gx[5], gg[5];
#pragma unroll
    for (int c = 0; c < 5; ++c) {
        gx[c] = *(const u32x2*)(x + (size_t)grow * DM + r * 20 + c * 4);
        gg[c] = *(const u32x2*)(gw + r * 20 + c * 4);
    }
    const int wf = gi < 800 ? gi : 0, wnt = wf / 20, wks = wf - wnt * 20, q4 = lane >> 4;
    const int k0 = wks < 16 ? (2 * wks + (q4 >> 1)) * 20 + 4 * (q4 & 1) : (8 * (wks - 16) + 2 * q4) * 20 + 16;
    const int k1 = wks < 16 ? k0 + 8 : k0 + 20;
    const u16* wsrc = wo + wnt * 16 + (lane & 15);
    u16 wv[8];
#pragma unroll
    for (int k = 0; k < 4; ++k) { wv[k] = wsrc[(k0 + k) * DM]; wv[4 + k] = wsrc[(k1 + k) * DM]; }

    if (gl == 3) proj_load<false>(x, wqkv, cd, sd, rb, cq, lds0, wid - 4);
    else proj_load<true>(x, wqkv, cd, sd, rb, cq * 3 + gl, lds0, wid - 4);

    const rsrc_t wr = ws_rsrc(ws);
    if (gi < 800)
        st128(wr, lane * 16, WS_WO + gi * 1024, u32x4{wv[0] | ((unsigned)wv[1] << 16), wv[2] | ((unsigned)wv[3] << 16), wv[4] | ((unsigned)wv[5] << 16), wv[6] | ((unsigned)wv[7] << 16)});
    if (gi == 1023 && lane < 8) st128(wr, lane * 16, WS_ONE, u32x4{0x3F803F80u, 0x3F803F80u, 0x3F803F80u, 0x3F803F80u});
    float z = 0.f;
#pragma unroll
    for (int c = 0; c < 5; ++c)
#pragma unroll
        for (int k = 0; k < 2; ++k) z += bflo(gx[c][k]) * bflo(gg[c][k]) + bfhi(gx[c][k]) * bfhi(gg[c][k]);
#pragma unroll
    for (int o = 16; o > 0; o >>= 1) z += __shfl_xor(z, o);
    if (r == 0) st32(wr, grow * 4, WS_G, __float_as_uint(1.0f / (1.0f + __expf(-z))));
}

constexpr int M_GSTG = 36864;
constexpr int M_KVSTG = 4 * KVT_B;
constexpr int M_KV0 = 3 * M_GSTG;
constexpr int M_CNT = M_KV0 + 4 * M_KVSTG;
constexpr int M_LDS = M_CNT + 64;

DI void arrive(unsigned cnt, unsigned one) {
    unsigned long long sv;
    asm volatile("s_mov_b64 %0, exec\n\ts_mov_b64 exec, 1\n\tds_add_u32 %1, %2\n\ts_mov_b64 exec, %0" : "=&s"(sv) : "v"(cnt), "v"(one) : "memory");
}
DI unsigned peek(unsigned cnt) { return *(const volatile LAS unsigned*)(uintptr_t)cnt; }
DI void await(unsigned cnt, unsigned seen, unsigned target) {
    if (__builtin_expect(__builtin_amdgcn_readfirstlane(seen) < target, 0)) {
        do { __builtin_amdgcn_s_sleep(1); } while (__builtin_amdgcn_readfirstlane(peek(cnt)) < target);
    }
    asm volatile("" ::: "memory");
}

DI float xhalf_max(float v) {
    auto rr = __builtin_amdgcn_permlane32_swap(__float_as_uint(v), __float_as_uint(v), false, false);
    return fmaxf(__uint_as_float(rr[0]), __uint_as_float(rr[1]));
}
DI float max16(const f32x16& s) {
    float a = fmaxf(fmaxf(s[0], s[1]), s[2]), b = fmaxf(fmaxf(s[3], s[4]), s[5]);
    a = fmaxf(fmaxf(a, s[6]), s[7]); b = fmaxf(fmaxf(b, s[8]), s[9]);
    a = fmaxf(fmaxf(a, s[10]), s[11]); b = fmaxf(fmaxf(b, s[12]), s[13]);
    a = fmaxf(fmaxf(a, s[14]), s[15]);
    return fmaxf(a, b);
}
#define EX2(v) __builtin_amdgcn_exp2f(v)

DI f32x16 attn_safe(const char* ws, int head, u32x4 qa, u32x4 qb, int lane) {
    const int r = lane & 31, h = lane >> 5;
    qb[2] = 0u;
    f32x16 o = f32x16{};
    float m = -INFINITY;
    const u32x4 ones = {0x3F803F80u, 0x3F803F80u, 0x3F803F80u, 0x3F803F80u};
#pragma unroll 1
    for (int t = 0; t < 64; ++t) {
        const u32x4 ka = *(const u32x4*)(ws + WS_KA + (size_t)(head * 64 + t) * 1024 + lane * 16);
        const u32x2 kb = *(const u32x2*)(ws + WS_KB + (size_t)(head * 64 + t) * 512 + r * 16);
        const char* vp = ws + WS_VV + (size_t)((t * 4 + h) * 640 + head * 20 + (r < 20 ? r : 0)) * 16;
        u32x4 v0 = *(const u32x4*)vp, v1 = *(const u32x4*)(vp + 2 * 640 * 16);
        if (r >= 20) { v0 = ones; v1 = ones; }
        f32x16 s = MFMA32(as_frag(ka), as_frag(qa), f32x16{});
        s = MFMA32(as_frag(u32x4{kb[0], kb[1], 0u, 0u}), as_frag(qb), s);
        const float mn = fmaxf(m, xhalf_max(max16(s)));
        const float al = EX2(m - mn);
        m = mn;
#pragma unroll
        for (int i = 0; i < 16; ++i) { o[i] *= al; s[i] = EX2(s[i] - mn); }
        o = pv_mma<0>(as_frag(v0), s, o, (int)0xDDDDDDDD);
        o = pv_mma<1>(as_frag(v1), s, o, (int)0xDDDDDDDD);
    }
    return o;
}

DI void mix_attn(char* __restrict__ ws, int head, int qb, int wid, unsigned lds0, char* smem) {
    const int lane = threadIdx.x & 63, r = lane & 31, h = lane >> 5;
    const int kh = wid >> 2;
    const int qt0 = qb * 8 + (wid & 3) * 2;
    STAMP(1, 0);
    const unsigned kv0 = lds0 + M_KV0 + kh * 2 * KVT_B;
    unsigned so0, so1, sd0, sd1;
    {
        auto src = [&](int c, unsigned& off, unsigned& step) {
            const int tl = wid >> 1;
            if (c < 64) { off = WS_KA + (unsigned)(head * 64 + tl) * 1024 + c * 16; step = 4096; }
            else if (c < 96) { off = WS_KB + (unsigned)(head * 64 + tl) * 512 + (c - 64) * 16; step = 2048; }
            else {
                const int v = c - 96, g = v / 21, d = v - g * 21;
                if (d < 20) { off = WS_VV + (unsigned)((tl * 4 + g) * 640 + head * 20 + d) * 16; step = 4 * 4 * 640 * 16; }
                else { off = WS_ONE; step = 0; }
            }
        };
        src((wid & 1) * 90 + lane, so0, sd0);
        src((wid & 1) * 90 + 64 + (lane < 26 ? lane : 0), so1, sd1);
    }
    const unsigned kdst = kv0 + (wid & 3) * 90 * 16;
    auto stage_kv = [&](int st) {
        const unsigned d = kdst + (st & 3) * M_KVSTG;
        dma16(ws, so0, d);
        dma16_lo<26>(ws, so1, d + 1024);
        so0 += sd0; so1 += sd1;
    };
    u32x4 qaA, qaB, qbA, qbB;
    {
        const char* qp = ws + WS_QA + (size_t)(head * 64 + qt0) * 1024 + lane * 16;
        const char* qq = ws + WS_QB + (size_t)(head * 64 + qt0) * 512 + r * 16;
        qaA = *(const u32x4*)qp;
        qaB = *(const u32x4*)(qp + 1024);
        const u32x2 t0 = *(const u32x2*)qq, t1 = *(const u32x2*)(qq + 512);
        const u32x4 ka = *(const u32x4*)(ws + WS_KA + (size_t)head * 64 * 1024 + lane * 16);
        const u32x4 kb = *(const u32x4*)(ws + WS_KB + (size_t)head * 64 * 512 + r * 16);
        stage_kv(0); stage_kv(1); stage_kv(2);
        qbA = h ? u32x4{0u, 0u, 0u, 0u} : u32x4{t0[0], t0[1], 0u, 0u};
        qbB = h ? u32x4{0u, 0u, 0u, 0u} : u32x4{t1[0], t1[1], 0u, 0u};
        f32x16 s0 = MFMA32(as_frag(ka), as_frag(qaA), f32x16{}), s1 = MFMA32(as_frag(ka), as_frag(qaB), f32x16{});
        s0 = MFMA32(as_frag(kb), as_frag(qbA), s0); s1 = MFMA32(as_frag(kb), as_frag(qbB), s1);
        const float m0 = xhalf_max(max16(s0)), m1 = xhalf_max(max16(s1));
        qbA[2] = h ? 0u : (pk2(-m0, 0.f) & 0xFFFFu);
        qbB[2] = h ? 0u : (pk2(-m1, 0.f) & 0xFFFFu);
    }
    const int rv = r <= 20 ? r : (r < 28 ? 20 : 16);
    const unsigned oka = kv0 + lane * 16, okb = kv0 + 1024 + r * 16, ov = kv0 + 1536u + (unsigned)(h * 21 + rv) * 16u;
    unsigned cnt = lds0 + M_CNT + kh * 16, one = 1;
    PIN(cnt); PIN(one);
    wait_vm<2>();
    bar_raw();
    STAMP(1, 1);
    f32x16 oA = f32x16{}, oB = f32x16{}, sA, sB = f32x16{};
    bf16x8 kaE, kaO, kbE, kbO, vE0, vE1, vO0, vO1;
    int idx = (int)0xDDDDDDDD;
    PIN(idx);
    lptr pka = lbase(oka), pkb = lbase(okb), pv = lbase(ov);
#define LDK(KA, KB, off) do { KA = lds128(pka + (off)); KB = lds128(pkb + (off)); } while (0)
    LDK(kaE, kbE, 0);
    LDK(kaO, kbO, KVT_B);
    vE0 = lds128(pv); vE1 = lds128(pv + 42 * 16);
    vO1 = vE1;
    sA = MFMA32(kaE, as_frag(qaA), f32x16{});
    sA = MFMA32(kbE, as_frag(qbA), sA);
    SBAR();
#define GRP(MF, S, i) do { MF; SBAR(); S[i] = EX2(S[i]); S[i + 1] = EX2(S[i + 1]); S[i + 2] = EX2(S[i + 2]); S[i + 3] = EX2(S[i + 3]); PIN(S); SBAR(); } while (0)
#define SLOT(X, OX, VX0, Y, OY, VY1, KA, KB, QA, QB, MID) do { \
        GRP(OY = pv_mma<1>(VY1, Y, OY, idx), X, 0); \
        GRP(Y = MFMA32(KA, as_frag(QA), f32x16{}), X, 4); \
        GRP(Y = MFMA32(KB, as_frag(QB), Y), X, 8); \
        MID; SBAR(); \
        GRP(OX = pv_mma<0>(VX0, X, OX, idx), X, 12); } while (0)
#pragma unroll 1
    for (int st = 0; st < 16; ++st) {
        const unsigned nx = (unsigned)((st + 1) & 3) * M_KVSTG;
        unsigned seen;
        SLOT(sA, oA, vE0, sB, oB, vO1, kaE, kbE, qaB, qbB,
             do { vO0 = lds128(pv + KVT_B); vO1 = lds128(pv + KVT_B + 42 * 16); seen = peek(cnt); } while (0));
        SLOT(sB, oB, vE0, sA, oA, vE1, kaO, kbO, qaA, qbA,
             do { await(cnt, seen, 4 * st); pka = lbase(oka + nx); pkb = lbase(okb + nx); LDK(kaE, kbE, 0); } while (0));
        SLOT(sA, oA, vO0, sB, oB, vE1, kaO, kbO, qaB, qbB,
             do { pv = lbase(ov + nx); vE0 = lds128(pv); vE1 = lds128(pv + 42 * 16); stage_kv(st + 3); } while (0));
        SLOT(sB, oB, vO0, sA, oA, vO1, kaE, kbE, qaA, qbA, LDK(kaO, kbO, KVT_B));
        wait_vm<2>();
        arrive(cnt, one);
    }
    oB = pv_mma<1>(vO1, sB, oB, idx);
    wait_vm<0>();
    STAMP(1, 2);
    bar();
    float* xch = (float*)smem + wid * 1024 + lane;
    float* xin = (float*)smem + (wid ^ 4) * 1024 + lane;
#pragma unroll
    for (int i = 0; i < 12; ++i) xch[i * 64] = kh ? oA[i] : oB[i];
    bar();
    f32x16 o = kh ? oB : oA;
#pragma unroll
    for (int i = 0; i < 12; ++i) o[i] += xin[i * 64];
    auto rl = __builtin_amdgcn_permlane32_swap(__float_as_uint(o[8]), __float_as_uint(o[8]), false, false);
    float l = __uint_as_float(rl[1]);
    bool fin = true;
#pragma unroll
    for (int k = 0; k < 12; ++k) fin = fin && (fabsf(o[k]) <= 3.0e38f);
    const bool ok = fin && (l >= 8.6736174e-19f) && (l <= 1.2676506e30f);
    if (__builtin_expect(!__all(ok), 0)) {
        o = attn_safe(ws, head, kh ? qaB : qaA, kh ? qbB : qbA, lane);
        rl = __builtin_amdgcn_permlane32_swap(__float_as_uint(o[8]), __float_as_uint(o[8]), false, false);
        l = __uint_as_float(rl[1]);
    }
    const float inv = 1.0f / l;
    const rsrc_t wr = ws_rsrc(ws);
    const unsigned ot = (unsigned)((qt0 + kh) * NH + head);
    st128(wr, lane * 16, WS_OA + ot * 1024, pack8(o, 0, inv));
    if (h == 0) st64(wr, r * 8, WS_OB + ot * 256, u32x2{pk2(o[8] * inv, o[9] * inv), pk2(o[10] * inv, o[11] * inv)});
    STAMP_END(1);
}

#define GPRIO 0
#define PACE() do {} while (0)
DI void mix_gemm(const u16* __restrict__ cs, const u16* __restrict__ ss, char* __restrict__ ws, int tb, int eg, int sp, int gwid, unsigned lds0) {
    const int lane = threadIdx.x & 63, r = lane & 31, h = lane >> 5;
    const unsigned vrow = (unsigned)(lane >> 3) * 4096u + (unsigned)(((lane & 7) ^ ((4 * (gwid & 1) + (lane >> 4)) & 7)) * 16);
    const char* ab_ = (const char*)(sp >= 2 ? ss : cs) + ((size_t)tb * 128 + gwid * 8) * 4096 + (sp & 1) * 2048;
    const char* bb = ws + WS_AB + (size_t)(eg * 5 * 256 + sp * 64 + gwid) * 1024;
    const unsigned vl = lane * 16;
    auto piece = [&](int st, unsigned so, int j) {
        const unsigned base = lds0 + so + gwid * 1024 + j * 4096;
        if (j < 4) dma16(ab_ + (size_t)j * 32 * 4096 + st * 128, vrow, base);
        else dma16(bb + (size_t)(j - 4) * 256 * 1024 + st * 4096, vl, base);
    };
    STAMP(1, 0);
    if (gwid == 0 && lane < 16) *(LAS unsigned*)(uintptr_t)(lds0 + M_CNT + lane * 4) = 0u;
#pragma unroll
    for (int st = 0; st < 2; ++st)
#pragma unroll
        for (int j = 0; j < 9; ++j) piece(st, st * M_GSTG, j);
    const unsigned sw = lds0 + gwid * 4096 + (unsigned)r * 128u + (unsigned)((h ^ ((r >> 1) & 7)) * 16);
    const unsigned abf = lds0 + 16384 + lane * 16;
    unsigned cnt = lds0 + M_CNT + 32, one = 1;
    PIN(cnt); PIN(one);
    f32x16 acc[5];
#pragma unroll
    for (int t = 0; t < 5; ++t) acc[t] = f32x16{};
    bf16x8 fa[2], fb[2][5];
    lptr pa[4], pb;
    auto rd = [&](int ks, int b) {
        fa[b] = lds128(pa[ks]);
#pragma unroll
        for (int t = 0; t < 5; ++t) fb[b][t] = lds128(pb + t * 4096 + ks * 1024);
        SBAR();
    };
    unsigned so = 0, sp2 = 2 * M_GSTG;
    auto meet = [&](int n) {
        __builtin_amdgcn_s_setprio(3);
        arrive(cnt, one);
#pragma unroll
        for (int ks = 0; ks < 4; ++ks) pa[ks] = lbase((sw ^ (unsigned)(ks * 32)) + so);
        pb = lbase(abf + so);
        await(cnt, peek(cnt), 4 * (n + 1));
        __builtin_amdgcn_s_setprio(GPRIO);
        SBAR();
    };
    bar();
    wait_vm<9>();
    meet(0);
    STAMP(1, 1);
    TR_DECL; TR();
    rd(0, 0);
    auto step = [&](int st, bool pf, bool last) {
#pragma unroll
        for (int ks = 0; ks < 3; ++ks) {
            rd(ks + 1, (ks + 1) & 1);
#pragma unroll
            for (int t = 0; t < 5; ++t) {
                if (t < 3 && pf) piece(st + 2, sp2, ks * 3 + t);
                SBAR();
                acc[t] = MFMA32(fb[ks & 1][t], fa[ks & 1], acc[t]);
                SBAR();
                PACE();
            }
        }
        sp2 = so;
        so = so == 2 * M_GSTG ? 0 : so + M_GSTG;
        if (!last) {
            TR();
            if (pf) asm volatile("s_waitcnt vmcnt(9) lgkmcnt(0)" ::: "memory"); else asm volatile("s_waitcnt vmcnt(0) lgkmcnt(0)" ::: "memory");
            TR();
            meet(st + 1);
            TR();
            rd(0, 0);
        }
#pragma unroll
        for (int t = 0; t < 5; ++t) { acc[t] = MFMA32(fb[1][t], fa[1], acc[t]); SBAR(); PACE(); }
    };
#pragma unroll 1
    for (int st = 0; st < 14; ++st) step(st, true, false);
    step(14, false, false);
    step(15, false, true);
    TR(); TR_FLUSH(1);
    STAMP(1, 2);
    __builtin_amdgcn_s_setprio(3);
    const rsrc_t wr = ws_rsrc(ws);
    const unsigned yp = WS_YP + (unsigned)(((sp * 64 + tb * 4 + gwid) * 20) + eg * 5) * 2048u;
#pragma unroll
    for (int t = 0; t < 5; ++t) {
        st128(wr, vl + t * 2048, yp, pack8(acc[t], 0, 1.f));
        st128(wr, vl + t * 2048 + 1024, yp, pack8(acc[t], 1, 1.f));
    }
    STAMP(1, 3);
    bar(); bar();
    STAMP(1, 4);
}

__global__ __launch_bounds__(768) void k_mix(const u16* __restrict__ cs, const u16* __restrict__ ss, char* __restrict__ ws) {
    extern __shared__ __attribute__((aligned(16))) char smem[];
    const unsigned lds0 = (unsigned)(uintptr_t)smem;
    const int wid = __builtin_amdgcn_readfirstlane(threadIdx.x >> 6);
    const int b = blockIdx.x, xcd = b & 7, i = b >> 3;
    if (wid < 8) mix_attn(ws, xcd * 4 + (i >> 3), i & 7, wid, lds0, smem);
    else mix_gemm(cs, ss, ws, (xcd >> 2) * 8 + (i & 7), i >> 3, xcd & 3, wid - 8, lds0);
}

constexpr int O_STG = 36864;
constexpr int O_LDS = 4 * O_STG;

__global__ __launch_bounds__(256) void k_out(const char* __restrict__ ws, u16* __restrict__ out) {
    extern __shared__ __attribute__((aligned(16))) char smem[];
    const unsigned lds0 = (unsigned)(uintptr_t)smem;
    const int lane = threadIdx.x & 63, c = lane & 15, q = lane >> 4;
    const int wid = __builtin_amdgcn_readfirstlane(threadIdx.x >> 6);
    const int b = blockIdx.x, xcd = b & 7, i = b >> 3;
    const int sb = xcd * 4 + (i & 3), ng = i >> 2;
    STAMP(2, 0);
    const int s = sb * 64 + wid * 16 + c;
    u32x2 yv[5][4];
#pragma unroll
    for (int t = 0; t < 5; ++t) {
        const int n = ng * 80 + t * 16 + q * 4;
        const int nq = (n & 31) >> 2, gg = nq >> 1;
        const char* yp = ws + WS_YP + (size_t)(((s >> 5) * 20 + (n >> 5)) * 2 + (gg >> 1)) * 1024 + ((nq & 1) * 32 + (s & 31)) * 16 + (gg & 1) * 8;
#pragma unroll
        for (int sp = 0; sp < 4; ++sp) yv[t][sp] = *(const u32x2*)(yp + (size_t)sp * 64 * 20 * 2048);
    }
    const float g = ((const float*)(ws + WS_G))[s];
    const char* oa = ws + WS_OA + (size_t)(sb * 2 * NH + wid) * 1024;
    const char* ob = ws + WS_OB + (size_t)(sb * 2 * 8 + wid) * 1024;
    const char* wb = ws + WS_WO + (size_t)(ng * 5 * 20 + wid) * 1024;
    const unsigned vl = lane * 16;
    auto issue = [&](int st) {
        const unsigned base = lds0 + (st & 3) * O_STG + wid * 1024;
#pragma unroll
        for (int j = 0; j < 4; ++j) dma16(st < 4 ? oa + ((j >> 1) * NH + (j & 1) * 4 + st * 8) * 1024 : ob + j * 4096, vl, base + j * 4096);
#pragma unroll
        for (int j = 0; j < 5; ++j) dma16(wb + (j * 20 + st * 4) * 1024, vl, base + 16384 + j * 4096);
    };
    issue(0); issue(1); issue(2); issue(3);
    f32x4 acc[5];
#pragma unroll
    for (int t = 0; t < 5; ++t) acc[t] = f32x4{};
    const unsigned row = (unsigned)((wid & 1) * 16 + c), st8 = (unsigned)(wid >> 1) * 8192u;
#pragma unroll
    for (int st = 0; st < 5; ++st) {
        if (st == 0) wait_vm<27>(); else if (st == 1) wait_vm<27>(); else if (st == 2) wait_vm<18>(); else if (st == 3) wait_vm<9>(); else wait_vm<0>();
        bar_raw();
        const lptr p = lbase(lds0 + (st & 3) * O_STG + vl);
        const lptr po = lbase(lds0 + (st & 3) * O_STG + st8 + (st < 4 ? q * 512 + row * 16 : q * 512 + row * 8));
#pragma unroll
        for (int ks = 0; ks < 4; ++ks) {
            bf16x8 fo;
            if (st < 4) fo = lds128(po + ks * 2048);
            else { const u32x2 a = lds64(po + ks * 2048), bq = lds64(po + ks * 2048 + 256); fo = as_frag(u32x4{a[0], a[1], bq[0], bq[1]}); }
#pragma unroll
            for (int t = 0; t < 5; ++t) acc[t] = MFMA16(lds128(p + 16384 + t * 4096 + ks * 1024), fo, acc[t]);
        }
        if (st == 0) { bar(); issue(4); }
    }
    STAMP(2, 2);
    const float gm = 1.0f - g;
#pragma unroll
    for (int t = 0; t < 5; ++t) {
        const int n = ng * 80 + t * 16 + q * 4;
        float y0 = 0.f, y1 = 0.f, y2 = 0.f, y3 = 0.f;
#pragma unroll
        for (int sp = 0; sp < 4; ++sp) {
            y0 += bflo(yv[t][sp][0]); y1 += bfhi(yv[t][sp][0]);
            y2 += bflo(yv[t][sp][1]); y3 += bfhi(yv[t][sp][1]);
        }
        u32x2 w = {pk2(g * acc[t][0] + gm * y0, g * acc[t][1] + gm * y1), pk2(g * acc[t][2] + gm * y2, g * acc[t][3] + gm * y3)};
        *(u32x2*)(out + (size_t)s * DM + n) = w;
    }
    STAMP_END(2);
}

extern "C" void kernel_launch(void* const* d_in, const int* in_sizes, int n_in, void* d_out, int out_size, void* d_ws, size_t ws_size,
                              hipStream_t stream) {
    const u16* x = (const u16*)d_in[0];
    const u16* wqkv = (const u16*)d_in[1];
    const u16* wo = (const u16*)d_in[2];
    const u16* cs = (const u16*)d_in[4];
    const u16* ss = (const u16*)d_in[5];
    const u16* cd = (const u16*)d_in[6];
    const u16* sd = (const u16*)d_in[7];
    const u16* gw = (const u16*)d_in[8];
    char* ws = (char*)d_ws;
    (void)hipFuncSetAttribute((const void*)k_proj, hipFuncAttributeMaxDynamicSharedMemorySize, P_LDS);
    (void)hipFuncSetAttribute((const void*)k_mix, hipFuncAttributeMaxDynamicSharedMemorySize, M_LDS);
    (void)hipFuncSetAttribute((const void*)k_out, hipFuncAttributeMaxDynamicSharedMemorySize, O_LDS);
    k_proj<<<256, 512, P_LDS, stream>>>(x, wqkv, cd, sd, wo, gw, ws);
    k_mix<<<256, 768, M_LDS, stream>>>(cs, ss, ws);
    k_out<<<256, 256, O_LDS, stream>>>(ws, (u16*)d_out);
}
```

```cpp
#include <hip/hip_runtime.h>
#include <hip/hip_bf16.h>
#include <stdint.h>

typedef unsigned short u16;
typedef __attribute__((ext_vector_type(8))) __bf16 bf16x8;
typedef __attribute__((ext_vector_type(4))) short s16x4;
typedef __attribute__((ext_vector_type(16))) float f32x16;
typedef __attribute__((ext_vector_type(4))) float f32x4;
typedef __attribute__((ext_vector_type(4))) unsigned u32x4;
typedef __attribute__((ext_vector_type(2))) unsigned u32x2;
#define LAS __attribute__((address_space(3)))
#define DI __device__ __forceinline__
typedef const LAS char* lptr;

constexpr int SEQ = 2048, DM = 640, NH = 32;
constexpr unsigned WS_QA = 0;
constexpr unsigned WS_KA = WS_QA + NH * 64 * 1024;
constexpr unsigned WS_QB = WS_KA + NH * 64 * 1024;
constexpr unsigned WS_KB = WS_QB + NH * 64 * 512;
constexpr unsigned WS_VV = WS_KB + NH * 64 * 512;
constexpr unsigned WS_AB = WS_VV + 64 * 4 * 640 * 16;
constexpr unsigned WS_YP = WS_AB + 40 * 128 * 1024;
constexpr unsigned WS_OA = WS_YP + 4 * 64 * 40 * 1024;
constexpr unsigned WS_OB = WS_OA + 64 * NH * 1024;
constexpr unsigned WS_WO = WS_OB + 64 * NH * 256;
constexpr unsigned WS_G = WS_WO + 40 * 20 * 1024;
constexpr unsigned WS_ONE = WS_G + SEQ * 4;
constexpr unsigned WS_FLAG = WS_ONE + 128;
constexpr unsigned WS_END = WS_FLAG + 72 * 128;
constexpr float QSCALE = 0.22360679774997896f * 1.4426950408889634f;
#define ST_AUX 16

#define TR_DECL do {} while (0)
#define TR() do {} while (0)
#define TR_FLUSH(k) do {} while (0)
#define STAMP(k, i) do {} while (0)
#define STAMP_END(k) do {} while (0)
#define MFMA32(a, b, c) __builtin_amdgcn_mfma_f32_32x32x16_bf16((a), (b), (c), 0, 0, 0)
#define MFMA16(a, b, c) __builtin_amdgcn_mfma_f32_16x16x32_bf16((a), (b), (c), 0, 0, 0)
#define SBAR() __builtin_amdgcn_sched_barrier(0)
#define PIN(x) asm volatile("" : "+v"(x))
#define KEEP(x) asm volatile("" ::"v"(x))

typedef __attribute__((ext_vector_type(2))) __bf16 bf16x2;
typedef __attribute__((ext_vector_type(2))) float f32x2;
DI unsigned pk2(float lo, float hi) {
    f32x2 v = {lo, hi};
    return __builtin_bit_cast(unsigned, __builtin_convertvector(v, bf16x2));
}
DI float bflo(unsigned v) { return __uint_as_float(v << 16); }
DI float bfhi(unsigned v) { return __uint_as_float(v & 0xFFFF0000u); }
DI void dma16(const void* sbase, unsigned voff, unsigned lds) {
    asm volatile("s_mov_b32 m0, %2\n\ts_nop 0\n\tglobal_load_lds_dwordx4 %0, %1" ::"v"(voff), "s"(sbase), "s"(lds) : "memory");
}
template <int N> DI void dma16_lo(const void* sbase, unsigned voff, unsigned lds) {
    unsigned long long sv;
    asm volatile("s_mov_b32 m0, %3\n\ts_mov_b64 %0, exec\n\ts_mov_b64 exec, %4\n\tglobal_load_lds_dwordx4 %1, %2\n\ts_mov_b64 exec, %0"
                 : "=&s"(sv) : "v"(voff), "s"(sbase), "s"(lds), "i"((1 << N) - 1) : "memory");
}
DI void touch(const void* sbase, unsigned voff, unsigned lds) {
    asm volatile("s_mov_b32 m0, %2\n\ts_nop 0\n\tglobal_load_lds_dword %0, %1" ::"v"(voff), "s"(sbase), "s"(lds) : "memory");
}
template <int N> DI void wait_vm() { asm volatile("s_waitcnt vmcnt(%0)" ::"i"(N) : "memory"); }
DI void bar() { asm volatile("s_waitcnt lgkmcnt(0)\n\ts_barrier" ::: "memory"); }
DI void bar_raw() { asm volatile("s_barrier" ::: "memory"); }
DI lptr lbase(unsigned a) { asm volatile("" : "+v"(a)); return (lptr)(uintptr_t)a; }
DI bf16x8 lds128(lptr p) { return *(const LAS bf16x8*)p; }
DI u32x2 lds64(lptr p) { return *(const LAS u32x2*)p; }
DI s16x4 ldstr(lptr p) {
    typedef short v4i16_t __attribute__((ext_vector_type(4)));
    return __builtin_bit_cast(s16x4, __builtin_amdgcn_ds_read_tr16_b64_v4i16((LAS v4i16_t*)p));
}
DI u32x4 pack8(const f32x16& x, int s, float sc) {
    u32x4 p;
    p[0] = pk2(x[8 * s + 0] * sc, x[8 * s + 1] * sc);
    p[1] = pk2(x[8 * s + 2] * sc, x[8 * s + 3] * sc);
    p[2] = pk2(x[8 * s + 4] * sc, x[8 * s + 5] * sc);
    p[3] = pk2(x[8 * s + 6] * sc, x[8 * s + 7] * sc);
    return p;
}
typedef __amdgpu_buffer_rsrc_t rsrc_t;
DI rsrc_t ws_rsrc(const void* ws) { return __builtin_amdgcn_make_buffer_rsrc((void*)ws, (short)0, (int)WS_END, 0x00020000); }
DI void st128(rsrc_t r, unsigned voff, unsigned soff, u32x4 v) { __builtin_amdgcn_raw_buffer_store_b128(v, r, voff + soff, 0, ST_AUX); }
DI void st64(rsrc_t r, unsigned voff, unsigned soff, u32x2 v) { __builtin_amdgcn_raw_buffer_store_b64(v, r, voff, soff, ST_AUX); }
DI void st32(rsrc_t r, unsigned voff, unsigned soff, unsigned v) { __builtin_amdgcn_raw_buffer_store_b32(v, r, voff, soff, ST_AUX); }
#define LD_AUX 0
DI u32x4 ld128(rsrc_t r, unsigned voff) { return __builtin_amdgcn_raw_buffer_load_b128(r, voff, 0, LD_AUX); }
DI bf16x8 as_frag(u32x4 v) { return __builtin_bit_cast(bf16x8, v); }
typedef __attribute__((ext_vector_type(16))) __bf16 bf16x16;
template <int HI> DI f32x16 pv_mma(bf16x8 v, const f32x16& p, const f32x16& o, int idx) {
    const auto ph = __builtin_shufflevector(p, p, 8 * HI, 8 * HI + 1, 8 * HI + 2, 8 * HI + 3, 8 * HI + 4, 8 * HI + 5, 8 * HI + 6, 8 * HI + 7);
    return __builtin_amdgcn_smfmac_f32_32x32x32_bf16(v, __builtin_bit_cast(bf16x16, ph), o, idx, 0, 0);
}
DI u32x4 pack8n(const f32x16& x, int s, float sc) {
    const u32x4 p = pack8(x, s, sc);
    const auto r0 = __builtin_amdgcn_permlane32_swap(p[0], p[2], false, false);
    const auto r1 = __builtin_amdgcn_permlane32_swap(p[1], p[3], false, false);
    return u32x4{r0[0], r1[0], r0[1], r1[1]};
}

constexpr int P_STG = 45056;
constexpr int P_RING = 3 * P_STG;

DI void store_ab(const f32x16& acc, int dt, int stile, rsrc_t ws, int lane) {
    const float sc = dt >= 20 ? -1.f : 1.f;
    const int nt = dt >= 20 ? dt - 20 : dt;
    const int ks = (dt >= 20 ? 64 : 0) + stile;
    const unsigned dst = WS_AB + (unsigned)((nt * 2 + ((lane >> 4) & 1)) * 128 + ks) * 1024u + (unsigned)((lane >> 5) * 256 + (lane & 15) * 16);
#pragma unroll
    for (int s = 0; s < 2; ++s) st128(ws, dst, s * 512, pack8n(acc, s, sc));
}

template <bool PATH_A>
DI void proj_load(const u16* __restrict__ x, const u16* __restrict__ wqkv, const u16* __restrict__ cd, const u16* __restrict__ sd,
                  int rb, int grp, unsigned lds0, int wid) {
    const int lane = threadIdx.x & 63;
    constexpr int ND = PATH_A ? 1 : 7, NDMA = PATH_A ? 10 : 11;
    const int dt0 = PATH_A ? 28 + grp : 7 * grp;
    const unsigned vrow = (unsigned)(lane >> 3) * 1280u + (unsigned)(((lane & 7) ^ ((4 * (wid & 1) + (lane >> 4)) & 7)) * 16);
    const char* xb = (const char*)x + ((size_t)rb * 128 + wid * 8) * 1280;
    unsigned vw[5];
    if (PATH_A) {
#pragma unroll
        for (int j = 0; j < 5; ++j) { const int i = (wid + 4 * j) * 64 + lane; vw[j] = (unsigned)(i / 20) * 3840u + (unsigned)(i % 20) * 16u; }
    }
    const char* wb = (const char*)wqkv + grp * 320;
    const char* db[ND];
#pragma unroll
    for (int j = 0; j < ND; ++j) {
        const int dt = dt0 + j;
        db[j] = (dt >= 20 ? (const char*)sd + (size_t)(dt - 20) * 40960 : (const char*)cd + (size_t)dt * 40960) + wid * 8 * 1280;
    }
    unsigned slot = lds0 + wid * 1024;
#pragma unroll 1
    for (int st = 0; st < 10; ++st) {
#pragma unroll
        for (int j = 0; j < 4; ++j) dma16(xb + j * 40960 + st * 128, vrow, slot + j * 4096);
#pragma unroll
        for (int j = 0; j < ND; ++j) dma16(db[j] + st * 128, vrow, slot + 16384 + j * 4096);
        if (PATH_A) {
#pragma unroll
            for (int j = 0; j < 5; ++j) dma16(wb + (size_t)st * 64 * 3840, vw[j], slot + 20480 + j * 4096);
        }
        slot = slot + P_STG == lds0 + wid * 1024 + P_RING ? lds0 + wid * 1024 : slot + P_STG;
        if (st == 2) { wait_vm<2 * NDMA>(); bar_raw(); }
        if (st >= 2) { wait_vm<NDMA>(); bar_raw(); }
    }
    wait_vm<0>();
    bar_raw();
    bar_raw();
}

template <int PATH>
DI void proj_mma(char* __restrict__ ws, int rb, int grp, unsigned lds0, int wid) {
    const int lane = threadIdx.x & 63, r = lane & 31, h = lane >> 5;
    constexpr bool PATH_A = PATH < 2;
    constexpr int NW = PATH_A ? 5 : 0, ND = PATH_A ? 1 : 7, NT = NW + ND;
    const int dt0 = PATH_A ? 28 + grp : 7 * grp;
    const unsigned sw = lds0 + (unsigned)r * 128u + (unsigned)((h ^ ((r >> 1) & 7)) * 16);
    const unsigned awo = lds0 + 20480u + (unsigned)(8 * h + ((lane & 15) >> 2)) * 320u + (unsigned)(((lane >> 4) & 1) * 32 + (lane & 3) * 8);
    f32x16 acc[NT];
#pragma unroll
    for (int t = 0; t < NT; ++t) acc[t] = f32x16{};
    bf16x8 fx[2], fw[2][NT];
    lptr pr, px, pw;
    auto setp = [&](unsigned so, int ks) {
        pr = lbase((sw ^ (unsigned)(ks * 32)) + so);
        px = lbase((sw ^ (unsigned)(ks * 32)) + so + wid * 4096);
        if (PATH_A && ks == 0) pw = lbase(awo + so);
    };
    auto rd = [&](int ks, int b, int t) {
        if (t == 0) fx[b] = lds128(px);
        if (t < ND) fw[b][t] = lds128(pr + 16384 + t * 4096);
        else {
            s16x4 lo = ldstr(pw + ks * 16 * 320 + (t - ND) * 64), hi = ldstr(pw + ks * 16 * 320 + 4 * 320 + (t - ND) * 64);
            fw[b][t] = __builtin_bit_cast(bf16x8, __builtin_shufflevector(lo, hi, 0, 1, 2, 3, 4, 5, 6, 7));
        }
    };
    auto mm = [&](int b, int t) {
        if (PATH == 0 && t >= ND) acc[t] = MFMA32(fw[b][t], fx[b], acc[t]);
        else acc[t] = MFMA32(fx[b], fw[b][t], acc[t]);
    };
    unsigned so = 0;
    setp(0, 0);
#pragma unroll
    for (int t = 0; t < NT; ++t) rd(0, 0, t);
    SBAR();
#pragma unroll 1
    for (int st = 0; st < 10; ++st) {
#pragma unroll
        for (int ks = 0; ks < 3; ++ks) {
            setp(so, ks + 1);
#pragma unroll
            for (int t = 0; t < NT; ++t) { rd(ks + 1, (ks + 1) & 1, t); SBAR(); mm(ks & 1, t); SBAR(); }
        }
        constexpr int NB = NT - 3;
#pragma unroll
        for (int t = 0; t < NB; ++t) { mm(1, t); SBAR(); }
        bar();
        so = so == 2 * P_STG ? 0 : so + P_STG;
        setp(so, 0);
#pragma unroll
        for (int t = NB; t < NT; ++t) {
#pragma unroll
            for (int u = (t - NB) * NT / 3; u < (t - NB + 1) * NT / 3; ++u) rd(0, 0, u);
            SBAR(); mm(1, t); SBAR();
        }
    }
    STAMP(0, 2);
    const rsrc_t wr = ws_rsrc(ws);
    const int stile = rb * 4 + wid;
    if (PATH == 0) {
        const float sc = grp < 4 ? QSCALE : 1.f;
        const unsigned t0 = (unsigned)(grp * 8 * 64 + stile);
        const unsigned va = WS_QA + t0 * 1024 + lane * 16, vb = WS_QB + (t0 + h * 64) * 512 + r * 16;
#pragma unroll
        for (int m = 0; m < 4; ++m) {
            u32x2 q[5];
#pragma unroll
            for (int i = 0; i < 5; ++i) {
                const int p = 5 * m + i, t = ND + (p >> 2), g = p & 3;
                q[i][0] = pk2(acc[t][4 * g] * sc, acc[t][4 * g + 1] * sc);
                q[i][1] = pk2(acc[t][4 * g + 2] * sc, acc[t][4 * g + 3] * sc);
            }
            st128(wr, va, (2 * m) * 64 * 1024, u32x4{q[0][0], q[0][1], q[1][0], q[1][1]});
            st128(wr, va, (2 * m + 1) * 64 * 1024, u32x4{q[3][0], q[3][1], q[4][0], q[4][1]});
            st128(wr, vb, (2 * m) * 64 * 512, u32x4{q[2][0], q[2][1], 0x00003F80u, 0u});
        }
    } else if (PATH == 1) {
        const unsigned vv = WS_VV + (unsigned)((stile * 4 + h) * 640 + (grp - 8) * 160 + r) * 16;
#pragma unroll
        for (int t = 0; t < 5; ++t) {
            st128(wr, vv, t * 512, pack8n(acc[ND + t], 0, 1.f));
            st128(wr, vv, t * 512 + 2 * 640 * 16, pack8n(acc[ND + t], 1, 1.f));
        }
    }
#pragma unroll
    for (int t = 0; t < ND; ++t) store_ab(acc[t], dt0 + t, stile, wr, lane);
    STAMP_END(0);
}
DI void side_jobs(const u16* __restrict__ x, const u16* __restrict__ wo, const u16* __restrict__ gw, char* __restrict__ ws, int gi) {
    const int lane = threadIdx.x & 63, r = lane & 31, h = lane >> 5;
    const int grow = gi * 2 + h;
    u32x2 gx[5], gg[5];
#pragma unroll
    for (int c = 0; c < 5; ++c) {
        gx[c] = *(const u32x2*)(x + (size_t)grow * DM + r * 20 + c * 4);
        gg[c] = *(const u32x2*)(gw + r * 20 + c * 4);
    }
    const int wf = gi < 800 ? gi : 0, wnt = wf / 20, wks = wf - wnt * 20, q4 = lane >> 4;
    const int k0 = wks < 16 ? (2 * wks + (q4 >> 1)) * 20 + 4 * (q4 & 1) : (8 * (wks - 16) + 2 * q4) * 20 + 16;
    const int k1 = wks < 16 ? k0 + 8 : k0 + 20;
    const u16* wsrc = wo + wnt * 16 + (lane & 15);
    u16 wv[8];
#pragma unroll
    for (int k = 0; k < 4; ++k) { wv[k] = wsrc[(k0 + k) * DM]; wv[4 + k] = wsrc[(k1 + k) * DM]; }
    const rsrc_t wr = ws_rsrc(ws);
    if (gi < 800)
        st128(wr, lane * 16, WS_WO + gi * 1024, u32x4{wv[0] | ((unsigned)wv[1] << 16), wv[2] | ((unsigned)wv[3] << 16), wv[4] | ((unsigned)wv[5] << 16), wv[6] | ((unsigned)wv[7] << 16)});
    float z = 0.f;
#pragma unroll
    for (int c = 0; c < 5; ++c)
#pragma unroll
        for (int k = 0; k < 2; ++k) z += bflo(gx[c][k]) * bflo(gg[c][k]) + bfhi(gx[c][k]) * bfhi(gg[c][k]);
#pragma unroll
    for (int o = 16; o > 0; o >>= 1) z += __shfl_xor(z, o);
    if (r == 0) st32(wr, grow * 4, WS_G, __float_as_uint(1.0f / (1.0f + __expf(-z))));
}

constexpr int F_STG = 36864;
constexpr int F_RING = 4 * F_STG;
DI void yg_issue(const u16* __restrict__ cs, const u16* __restrict__ ss, const char* __restrict__ ws, unsigned lds0, int lw, int st) {
    const int lane = threadIdx.x & 63;
    const int b = blockIdx.x, xcd = b & 7, i = b >> 3;
    const int tb = (xcd >> 2) * 8 + (i & 7), eg = i >> 3, sp = xcd & 3;
    const unsigned vrow = (unsigned)(lane >> 3) * 4096u + (unsigned)(((lane & 7) ^ ((4 * (lw & 1) + (lane >> 4)) & 7)) * 16);
    const char* rp = (const char*)(sp >= 2 ? ss : cs) + (size_t)(tb * 128 + lw * 8) * 4096 + (sp & 1) * 2048;
    const char* ab = ws + WS_AB + (size_t)((eg * 10 + (lw >> 1)) * 128 + sp * 32 + (lw & 1)) * 1024;
    const unsigned d = lds0 + lw * 1024 + (st & 3) * F_STG;
#pragma unroll
    for (int j = 0; j < 4; ++j) dma16(rp + j * 32 * 4096 + st * 128, vrow, d + j * 4096);
#pragma unroll
    for (int j = 0; j < 5; ++j) dma16(ab + (j * 256 + st * 2) * 1024, lane * 16, d + 16384 + j * 4096);
}

DI float xhalf_max(float v) {
    auto rr = __builtin_amdgcn_permlane32_swap(__float_as_uint(v), __float_as_uint(v), false, false);
    return fmaxf(__uint_as_float(rr[0]), __uint_as_float(rr[1]));
}
DI float max16(const f32x16& s) {
    float a = fmaxf(fmaxf(s[0], s[1]), s[2]), b = fmaxf(fmaxf(s[3], s[4]), s[5]);
    a = fmaxf(fmaxf(a, s[6]), s[7]); b = fmaxf(fmaxf(b, s[8]), s[9]);
    a = fmaxf(fmaxf(a, s[10]), s[11]); b = fmaxf(fmaxf(b, s[12]), s[13]);
    a = fmaxf(fmaxf(a, s[14]), s[15]);
    return fmaxf(a, b);
}
#define EX2(v) __builtin_amdgcn_exp2f(v)
constexpr unsigned VV_T = 4 * 640 * 16, VV_S = 2 * 640 * 16;
constexpr unsigned ONES = 0x3F803F80u;

DI u32x4 fix_v(u32x4 v, int r) {
#pragma unroll
    for (int i = 0; i < 4; ++i) v[i] = r < 20 ? v[i] : (r == 20 ? ONES : 0u);
    return v;
}
DI f32x16 attn_safe(rsrc_t wr, int head, u32x4 qa, u32x4 qb, int lane) {
    const int r = lane & 31, h = lane >> 5;
    qb[2] = 0u;
    f32x16 o = f32x16{};
    float m = -INFINITY;
#pragma unroll 1
    for (int t = 0; t < 64; ++t) {
        const u32x4 ka = ld128(wr, WS_KA + (unsigned)(head * 64 + t) * 1024 + lane * 16);
        const u32x4 kb = ld128(wr, WS_KB + (unsigned)(head * 64 + t) * 512 + r * 16);
        const unsigned vp = WS_VV + (unsigned)((t * 4 + h) * 640 + head * 20 + (r < 20 ? r : 0)) * 16;
        const u32x4 v0 = fix_v(ld128(wr, vp), r), v1 = fix_v(ld128(wr, vp + VV_S), r);
        f32x16 s = MFMA32(as_frag(ka), as_frag(qa), f32x16{});
        s = MFMA32(as_frag(kb), as_frag(qb), s);
        const float mn = fmaxf(m, xhalf_max(max16(s)));
        const float al = EX2(m - mn);
        m = mn;
#pragma unroll
        for (int i = 0; i < 16; ++i) { o[i] *= al; s[i] = EX2(s[i] - mn); }
        o = pv_mma<0>(as_frag(v0), s, o, (int)0xDDDDDDDD);
        o = pv_mma<1>(as_frag(v1), s, o, (int)0xDDDDDDDD);
    }
    return o;
}

#define ASW 5
DI void phase_attn(const u16* __restrict__ cs, const u16* __restrict__ ss, char* __restrict__ ws, char* smem, int wid) {
    const int lane = threadIdx.x & 63, r = lane & 31, h = lane >> 5;
    const int b = blockIdx.x, head = (b & 7) * 4 + (b >> 6), qb = (b >> 3) & 7;
    const int kq = wid >> 1, qt0 = qb * 8 + (wid & 1) * 4;
    const rsrc_t wr = ws_rsrc(ws);
    STAMP(1, 0);
    unsigned oka = WS_KA + (unsigned)(head * 64 + kq * 16) * 1024 + lane * 16;
    unsigned okb = WS_KB + (unsigned)(head * 64 + kq * 16) * 512 + r * 16;
    unsigned ov0 = r < 20 ? WS_VV + (unsigned)((kq * 64 + h) * 640 + head * 20 + r) * 16 : (r == 20 ? WS_ONE : 0x80000000u);
    unsigned ov1 = r < 20 ? ov0 + VV_S : ov0;
    const unsigned vstep = r < 20 ? VV_T : 0u;
    u32x4 qa[4], qbv[4];
    bf16x8 ka0, kb0, ka1, kb1, ka2, kb2, ka3, kb3, v00, v01, v10, v11, v20, v21, v30, v31;
#define LDK(KA, KB) do { KA = as_frag(ld128(wr, oka)); SBAR(); KB = as_frag(ld128(wr, okb)); oka += 1024; okb += 512; } while (0)
#define LDV0(V) do { V = as_frag(ld128(wr, ov0)); ov0 += vstep; SBAR(); } while (0)
#define LDV1(V) do { V = as_frag(ld128(wr, ov1)); ov1 += vstep; } while (0)
    {
        const u32x4 ka = ld128(wr, WS_KA + (unsigned)head * 64 * 1024 + lane * 16);
        const u32x4 kb = ld128(wr, WS_KB + (unsigned)head * 64 * 512 + r * 16);
#pragma unroll
        for (int i = 0; i < 4; ++i) {
            qa[i] = ld128(wr, WS_QA + (unsigned)(head * 64 + qt0 + i) * 1024 + lane * 16);
            qbv[i] = ld128(wr, h ? 0x80000000u : WS_QB + (unsigned)(head * 64 + qt0 + i) * 512 + r * 16);
        }
        SBAR();
        LDK(ka0, kb0); SBAR(); LDV0(v00); LDV1(v01); SBAR();
        LDK(ka1, kb1); SBAR(); LDV0(v10); LDV1(v11); SBAR();
        LDK(ka2, kb2); SBAR(); LDV0(v20); LDV1(v21); SBAR();
        LDK(ka3, kb3); SBAR();
#pragma unroll
        for (int i = 0; i < 4; ++i) {
            qbv[i][2] = 0u;
            f32x16 s0 = MFMA32(as_frag(ka), as_frag(qa[i]), f32x16{});
            s0 = MFMA32(as_frag(kb), as_frag(qbv[i]), s0);
            const float m0 = xhalf_max(max16(s0));
            qbv[i][2] = h ? 0u : (pk2(-m0, 0.f) & 0xFFFFu);
        }
    }
    STAMP(1, 1);
    f32x16 o0 = f32x16{}, o1 = f32x16{}, o2 = f32x16{}, o3 = f32x16{}, sA, sB = f32x16{};
    int idx = (int)0xDDDDDDDD;
    PIN(idx);
    v31 = v01;
    sA = MFMA32(ka0, as_frag(qa[0]), f32x16{});
    sA = MFMA32(kb0, as_frag(qbv[0]), sA);
    SBAR();
#define GRP(MF, S, i) do { MF; SBAR(); S[i] = EX2(S[i]); S[i + 1] = EX2(S[i + 1]); S[i + 2] = EX2(S[i + 2]); S[i + 3] = EX2(S[i + 3]); PIN(S); SBAR(); } while (0)
#define SLOT(X, OX, VX0, Y, OY, VY1, KA, KB, Q, MID) do { \
        GRP(OY = pv_mma<1>(VY1, Y, OY, idx), X, 0); \
        GRP(Y = MFMA32(KA, as_frag(qa[Q]), f32x16{}), X, 4); \
        GRP(Y = MFMA32(KB, as_frag(qbv[Q]), Y), X, 8); \
        MID; SBAR(); \
        GRP(OX = pv_mma<0>(VX0, X, OX, idx), X, 12); } while (0)
#define TILE(KA, KB, V0, V1, PV0, PV1, NA, NB, PO) do { \
        SLOT(sA, o0, V0, sB, o3, PV1, KA, KB, 1, do { LDV0(PV0); LDV1(PV1); PFS(PO); } while (0)); \
        SLOT(sB, o1, V0, sA, o0, V1, KA, KB, 2, (void)0); \
        SLOT(sA, o2, V0, sB, o1, V1, KA, KB, 3, (void)0); \
        SLOT(sB, o3, V0, sA, o2, V1, NA, NB, 0, LDK(KA, KB)); } while (0)
    if (wid < 4) __builtin_amdgcn_s_setprio(2); else __builtin_amdgcn_s_setprio(1);
    const int sw_at = wid < 4 ? ASW : 99;
    const unsigned pfo = (unsigned)(lane >> 4) * 128u + (lane & 15) * 4, pfd = (unsigned)(uintptr_t)smem + 147456;
#define PFS(o) touch(pfi + (o), pfo, pfd)
    const char* pfp = (const char*)((b & 2) ? ss : cs) + (size_t)((((b & 7) >> 2) * 8 + qb) * 128 + (b >> 6) * 32 + wid * 4) * 4096 + (b & 1) * 2048;
#pragma unroll 1
    for (int it = 0; it < 4; ++it) {
        if (2 * it == sw_at) __builtin_amdgcn_s_setprio(0);
        const char* pfi = pfp + it * 4096;
        TILE(ka0, kb0, v00, v01, v30, v31, ka1, kb1, 0);
        TILE(ka1, kb1, v10, v11, v00, v01, ka2, kb2, 512);
        if (2 * it + 1 == sw_at) __builtin_amdgcn_s_setprio(0);
        TILE(ka2, kb2, v20, v21, v10, v11, ka3, kb3, 1024);
        TILE(ka3, kb3, v30, v31, v20, v21, ka0, kb0, 1536);
    }
    o3 = pv_mma<1>(v31, sB, o3, idx);
    __builtin_amdgcn_s_setprio(0);
    STAMP(1, 2);
    if (wid >= 4) { yg_issue(cs, ss, ws, (unsigned)(uintptr_t)smem, wid - 4, 0); yg_issue(cs, ss, ws, (unsigned)(uintptr_t)smem, wid - 4, 1); }
    f32x16 o = f32x16{};
    {
        float* xb = (float*)(smem + 2 * F_STG) + (wid & 1) * 2304 + lane;
#define PUT(O, T) do { float* d_ = xb + (T) * 4608 + ((kq - (T) - 1) & 3) * 768; _Pragma("unroll") for (int i = 0; i < 12; ++i) d_[i * 64] = O[i]; } while (0)
        if (kq != 0) PUT(o0, 0);
        if (kq != 1) PUT(o1, 1);
        if (kq != 2) PUT(o2, 2);
        if (kq != 3) PUT(o3, 3);
        int kv = kq;
        PIN(kv);
#pragma unroll
        for (int i = 0; i < 12; ++i) o[i] = (kv & 2) ? ((kv & 1) ? o3[i] : o2[i]) : ((kv & 1) ? o1[i] : o0[i]);
        bar();
        const float* xr = xb + kq * 4608;
#pragma unroll
        for (int i = 0; i < 12; ++i) o[i] = (o[i] + xr[i * 64]) + (xr[768 + i * 64] + xr[1536 + i * 64]);
    }
    auto rl = __builtin_amdgcn_permlane32_swap(__float_as_uint(o[8]), __float_as_uint(o[8]), false, false);
    float l = __uint_as_float(rl[1]);
    bool fin = true;
#pragma unroll
    for (int k = 0; k < 12; ++k) fin = fin && (fabsf(o[k]) <= 3.0e38f);
    const bool ok = fin && (l >= 8.6736174e-19f) && (l <= 1.2676506e30f);
    if (__builtin_expect(!__all(ok), 0)) {
        const u32x4 q0 = ld128(wr, WS_QA + (unsigned)(head * 64 + qt0 + kq) * 1024 + lane * 16);
        const u32x4 q1 = ld128(wr, h ? 0x80000000u : WS_QB + (unsigned)(head * 64 + qt0 + kq) * 512 + r * 16);
        o = attn_safe(wr, head, q0, q1, lane);
        rl = __builtin_amdgcn_permlane32_swap(__float_as_uint(o[8]), __float_as_uint(o[8]), false, false);
        l = __uint_as_float(rl[1]);
    }
    const float inv = 1.0f / l;
    const unsigned ot = (unsigned)((qt0 + kq) * NH + head);
    st128(wr, lane * 16, WS_OA + ot * 1024, pack8(o, 0, inv));
    if (h == 0) st64(wr, r * 8, WS_OB + ot * 256, u32x2{pk2(o[8] * inv, o[9] * inv), pk2(o[10] * inv, o[11] * inv)});
    STAMP(1, 3);
    wait_vm<0>();
    bar();
    if (threadIdx.x == 0) __hip_atomic_fetch_add((__attribute__((address_space(1))) unsigned*)(ws + WS_FLAG + qb * 128), 1u, __ATOMIC_RELAXED, __HIP_MEMORY_SCOPE_AGENT);
    STAMP(1, 4);
}

typedef __attribute__((address_space(1))) unsigned gu32;
#define RLX_AGENT __ATOMIC_RELAXED, __HIP_MEMORY_SCOPE_AGENT
DI void wait_count(gu32* c, unsigned seen, unsigned want) {
    for (unsigned spins = 0; (unsigned)__builtin_amdgcn_readfirstlane(seen) < want && spins < (1u << 22); ++spins) {
        __builtin_amdgcn_s_sleep(2);
        seen = __hip_atomic_load(c, RLX_AGENT);
    }
}

DI void phase_tail(const u16* __restrict__ cs, const u16* __restrict__ ss, char* __restrict__ ws, u16* __restrict__ out, unsigned lds0, int wid) {
    const int lane = threadIdx.x & 63, c = lane & 15, q = lane >> 4;
    const int b = blockIdx.x, xcd = b & 7, i = b >> 3;
    const int tb = (xcd >> 2) * 8 + (i & 7), eg = i >> 3, sp = xcd & 3;
    const int sb = tb * 2 + (sp & 1), ng = eg * 2 + (sp >> 1);
    gu32* ycnt = (gu32*)(ws + WS_FLAG + (8 + tb * 4 + eg) * 128);
    STAMP(3, 0);
    if (wid >= 4) {
        const int lw = wid - 4;
        const unsigned vl = lane * 16;
        const unsigned dst = lds0 + lw * 1024;
#pragma unroll 1
        for (int st = 2; st < 16; ++st) {
            yg_issue(cs, ss, ws, lds0, lw, st);
            wait_vm<18>(); bar_raw();
        }
        const char* oa = ws + WS_OA + (size_t)(sb * 2 * NH + lw) * 1024;
        const char* ob = ws + WS_OB + (size_t)(sb * 2 * 8 + lw) * 1024;
        const char* wb = ws + WS_WO + (size_t)(ng * 5 * 20 + lw) * 1024;
#pragma unroll 1
        for (int st = 0; st < 5; ++st) {
            const unsigned d = dst + (st & 3) * F_STG;
#pragma unroll
            for (int j = 0; j < 4; ++j) dma16(st < 4 ? oa + ((j >> 1) * NH + (j & 1) * 4 + st * 8) * 1024 : ob + j * 4096, vl, d + j * 4096);
#pragma unroll
            for (int j = 0; j < 5; ++j) dma16(wb + (j * 20 + st * 4) * 1024, vl, d + 16384 + j * 4096);
            wait_vm<18>(); bar_raw();
        }
        wait_vm<9>(); bar_raw();
        wait_vm<0>(); bar_raw();
        if (lw == 0) {
            unsigned seen = 0;
            if (lane == 0) seen = __hip_atomic_fetch_add(ycnt, 1u, RLX_AGENT) + 1u;
            wait_count(ycnt, seen, 4u);
        }
        bar_raw();
        bar_raw();
        return;
    }
    const rsrc_t wr = ws_rsrc(ws);
    {
        const int rh = wid & 1, ch = wid >> 1;
        f32x4 acc[4][5];
#pragma unroll
        for (int u = 0; u < 4; ++u)
#pragma unroll
            for (int t = 0; t < 5; ++t) acc[u][t] = f32x4{};
        bf16x8 frE[2][4], fnE[2][5], frO[2][4], fnO[2][5];
        const unsigned af = lds0 + 16384 + ch * 10240 + lane * 16;
        const unsigned a0 = lds0 + (unsigned)(rh * 64 + c) * 128u + (unsigned)((q ^ (c >> 1)) * 16);
        auto step = [&](bf16x8 (&fr)[2][4], bf16x8 (&fn)[2][5], const bf16x8 (&pr)[2][4], const bf16x8 (&pn)[2][5], int st, bool ld, bool mm) {
            lptr p, r0, r1;
            if (ld) {
                bar();
                const unsigned so = (st & 3) * F_STG;
                p = lbase(af + so); r0 = lbase(a0 + so); r1 = lbase((a0 ^ 64u) + so);
            }
#pragma unroll
            for (int k = 0; k < 2; ++k)
#pragma unroll
                for (int t = 0; t < 5; ++t) {
                    if (ld) {
                        fn[k][t] = lds128(p + t * 2048 + k * 1024);
                        if (t < 4) fr[k][t] = lds128((k ? r1 : r0) + t * 2048);
                    }
                    SBAR();
                    if (mm) {
#pragma unroll
                        for (int u = 0; u < 4; ++u) acc[u][t] = MFMA16(pn[k][t], pr[k][u], acc[u][t]);
                    }
                    SBAR();
                }
        };
        gu32* ocnt = (gu32*)(ws + WS_FLAG + (sb >> 2) * 128);
        unsigned oseen = 0;
        step(frE, fnE, frO, fnO, 0, true, false);
        STAMP(3, 1);
#pragma unroll 1
        for (int st = 0; st < 14; st += 2) {
            if (wid == 0) {
                if (st == 6) oseen = __hip_atomic_load(ocnt, RLX_AGENT);
                if (st == 8) { wait_count(ocnt, oseen, 32u); __builtin_amdgcn_fence(__ATOMIC_ACQUIRE, "agent"); }
                if (st == 12) wait_vm<0>();
            }
            step(frO, fnO, frE, fnE, st + 1, true, true);
            step(frE, fnE, frO, fnO, st + 2, true, true);
        }
        step(frO, fnO, frE, fnE, 15, true, true);
        step(frE, fnE, frO, fnO, 16, false, true);
        STAMP(3, 2);
        const unsigned yp = WS_YP + (unsigned)(((tb * 4 + eg) * 4 + wid) * 4 + sp) * 10240u + lane * 16;
#pragma unroll
        for (int u = 0; u < 2; ++u)
#pragma unroll
            for (int t = 0; t < 5; ++t) {
                const f32x4 v0 = acc[2 * u][t], v1 = acc[2 * u + 1][t];
                st128(wr, yp, (u * 5 + t) * 1024, u32x4{pk2(v0[0], v0[1]), pk2(v0[2], v0[3]), pk2(v1[0], v1[1]), pk2(v1[2], v1[3])});
            }
        STAMP(3, 3);
    }
    const int rh = wid & 1, kk = wid >> 1;
    const int s0 = sb * 64 + rh * 32 + c;
    const float g0 = ((const float*)(ws + WS_G))[s0], g1 = ((const float*)(ws + WS_G))[s0 + 16];
    f32x4 acc[2][5];
#pragma unroll
    for (int t = 0; t < 5; ++t) { acc[0][t] = f32x4{}; acc[1][t] = f32x4{}; }
    const unsigned af = lds0 + 16384 + kk * 2048 + lane * 16, ao = lds0 + (unsigned)(rh * 8192 + kk * 4096 + q * 512);
#pragma unroll 1
    for (int st = 0; st < 4; ++st) {
        bar();
        const lptr p = lbase(af + st * F_STG), po = lbase(ao + c * 16 + st * F_STG);
        bf16x8 fr[2][2], fn[2][5];
#pragma unroll
        for (int k = 0; k < 2; ++k) {
            fr[k][0] = lds128(po + k * 2048); fr[k][1] = lds128(po + k * 2048 + 256);
#pragma unroll
            for (int t = 0; t < 5; ++t) fn[k][t] = lds128(p + t * 4096 + k * 1024);
        }
        SBAR();
#pragma unroll
        for (int k = 0; k < 2; ++k)
#pragma unroll
            for (int t = 0; t < 5; ++t) { acc[0][t] = MFMA16(fn[k][t], fr[k][0], acc[0][t]); acc[1][t] = MFMA16(fn[k][t], fr[k][1], acc[1][t]); }
    }
    STAMP(2, 0);
    {
        wait_vm<0>();
        bar();
        const lptr p = lbase(af), po = lbase(ao + c * 8);
        bf16x8 fr[2][2], fn[2][5];
#pragma unroll
        for (int k = 0; k < 2; ++k) {
#pragma unroll
            for (int u = 0; u < 2; ++u) {
                const u32x2 a = lds64(po + k * 2048 + u * 128), bq = lds64(po + k * 2048 + u * 128 + 256);
                fr[k][u] = as_frag(u32x4{a[0], a[1], bq[0], bq[1]});
            }
#pragma unroll
            for (int t = 0; t < 5; ++t) fn[k][t] = lds128(p + t * 4096 + k * 1024);
        }
        SBAR();
#pragma unroll
        for (int k = 0; k < 2; ++k)
#pragma unroll
            for (int t = 0; t < 5; ++t) { acc[0][t] = MFMA16(fn[k][t], fr[k][0], acc[0][t]); acc[1][t] = MFMA16(fn[k][t], fr[k][1], acc[1][t]); }
    }
    STAMP(2, 1);
    bar();
    STAMP(2, 2);
    u32x4 yv[2][5];
#pragma unroll
    for (int u = 0; u < 2; ++u)
#pragma unroll
        for (int t = 0; t < 5; ++t)
            yv[u][t] = __builtin_amdgcn_raw_buffer_load_b128(wr, WS_YP + (unsigned)(((tb * 4 + eg) * 4 + sp) * 4 + kk * 2 + u) * 10240u + (rh * 5 + t) * 1024 + lane * 16, 0, 16);
    f32x4 keep[5];
    LAS f32x4* xw = (LAS f32x4*)(uintptr_t)(lds0 + F_STG + wid * 5120 + lane * 16);
    const LAS f32x4* xr = (const LAS f32x4*)(uintptr_t)(lds0 + F_STG + (wid ^ 2) * 5120 + lane * 16);
#pragma unroll
    for (int t = 0; t < 5; ++t) {
        const f32x4 y0 = {bflo(yv[0][t][0]) + bflo(yv[1][t][0]), bfhi(yv[0][t][0]) + bfhi(yv[1][t][0]), bflo(yv[0][t][1]) + bflo(yv[1][t][1]), bfhi(yv[0][t][1]) + bfhi(yv[1][t][1])};
        const f32x4 y1 = {bflo(yv[0][t][2]) + bflo(yv[1][t][2]), bfhi(yv[0][t][2]) + bfhi(yv[1][t][2]), bflo(yv[0][t][3]) + bflo(yv[1][t][3]), bfhi(yv[0][t][3]) + bfhi(yv[1][t][3])};
        const f32x4 v0 = acc[0][t] * g0 + y0 * (1.0f - g0), v1 = acc[1][t] * g1 + y1 * (1.0f - g1);
        keep[t] = kk ? v1 : v0;
        xw[t * 64] = kk ? v0 : v1;
    }
    bar();
    u16* op = out + (size_t)(s0 + kk * 16) * DM + ng * 80 + q * 4;
#pragma unroll
    for (int t = 0; t < 5; ++t) {
        const f32x4 v = keep[t] + xr[t * 64];
        *(u32x2*)(op + t * 16) = u32x2{pk2(v[0], v[1]), pk2(v[2], v[3])};
    }
    STAMP_END(2);
}

constexpr int LDS_B = F_RING + 256;

__global__ __launch_bounds__(512) void k_proj(const u16* __restrict__ x, const u16* __restrict__ wqkv, const u16* __restrict__ cd,
                                              const u16* __restrict__ sd, const u16* __restrict__ wo, const u16* __restrict__ gw,
                                              char* __restrict__ ws) {
    extern __shared__ __attribute__((aligned(16))) char smem[];
    const unsigned lds0 = (unsigned)(uintptr_t)smem;
    const int wid = __builtin_amdgcn_readfirstlane(threadIdx.x >> 6);
    const int b = blockIdx.x, xcd = b & 7, i = b >> 3;
    const int rb = (xcd & 1) * 8 + (i & 7), cq = xcd >> 1, gl = i >> 3;
    if (wid >= 4) {
        if (gl == 3) proj_load<false>(x, wqkv, cd, sd, rb, cq, lds0, wid - 4);
        else proj_load<true>(x, wqkv, cd, sd, rb, cq * 3 + gl, lds0, wid - 4);
        return;
    }
    STAMP(0, 0);
    side_jobs(x, wo, gw, ws, b * 4 + wid);
    if (b == 255) {
        const unsigned tl = threadIdx.x;
        if (tl < 8) st128(ws_rsrc(ws), tl * 16, WS_ONE, u32x4{ONES, ONES, ONES, ONES});
        if (tl < 72) st32(ws_rsrc(ws), tl * 128, WS_FLAG, 0u);
    }
    bar_raw();
    STAMP(0, 1);
    if (gl == 3) proj_mma<2>(ws, rb, cq, lds0, wid);
    else if (cq * 3 + gl < 8) proj_mma<0>(ws, rb, cq * 3 + gl, lds0, wid);
    else proj_mma<1>(ws, rb, cq * 3 + gl, lds0, wid);
}

__global__ __launch_bounds__(512) void k_mix(const u16* __restrict__ x, const u16* __restrict__ wo, const u16* __restrict__ gw,
                                             const u16* __restrict__ cs, const u16* __restrict__ ss, char* __restrict__ ws, u16* __restrict__ out) {
    extern __shared__ __attribute__((aligned(16))) char smem[];
    const int wid = __builtin_amdgcn_readfirstlane(threadIdx.x >> 6);
    phase_attn(cs, ss, ws, smem, wid);
    phase_tail(cs, ss, ws, out, (unsigned)(uintptr_t)smem, wid);
}

extern "C" void kernel_launch(void* const* d_in, const int* in_sizes, int n_in, void* d_out, int out_size, void* d_ws, size_t ws_size,
                              hipStream_t stream) {
    const u16* x = (const u16*)d_in[0];
    const u16* wqkv = (const u16*)d_in[1];
    const u16* wo = (const u16*)d_in[2];
    const u16* cs = (const u16*)d_in[4];
    const u16* ss = (const u16*)d_in[5];
    const u16* cd = (const u16*)d_in[6];
    const u16* sd = (const u16*)d_in[7];
    const u16* gw = (const u16*)d_in[8];
    char* ws = (char*)d_ws;
    (void)hipFuncSetAttribute((const void*)k_proj, hipFuncAttributeMaxDynamicSharedMemorySize, LDS_B);
    (void)hipFuncSetAttribute((const void*)k_mix, hipFuncAttributeMaxDynamicSharedMemorySize, LDS_B);
    k_proj<<<256, 512, LDS_B, stream>>>(x, wqkv, cd, sd, wo, gw, ws);
    k_mix<<<256, 512, LDS_B, stream>>>(x, wo, gw, cs, ss, ws, (u16*)d_out);
}
```

```cpp
#include <hip/hip_runtime.h>
#include <hip/hip_bf16.h>
#include <stdint.h>

typedef unsigned short u16;
typedef __attribute__((ext_vector_type(8))) __bf16 bf16x8;
typedef __attribute__((ext_vector_type(4))) short s16x4;
typedef __attribute__((ext_vector_type(16))) float f32x16;
typedef __attribute__((ext_vector_type(4))) float f32x4;
typedef __attribute__((ext_vector_type(4))) unsigned u32x4;
typedef __attribute__((ext_vector_type(2))) unsigned u32x2;
#define LAS __attribute__((address_space(3)))
#define DI __device__ __forceinline__
typedef const LAS char* lptr;

constexpr int SEQ = 2048, DM = 640, NH = 32;
constexpr int VP = 648;
constexpr unsigned WS_QA = 0;
constexpr unsigned WS_KA = WS_QA + NH * 64 * 1024;
constexpr unsigned WS_QB = WS_KA + NH * 64 * 1024;
constexpr unsigned WS_KB = WS_QB + NH * 64 * 512;
constexpr unsigned WS_VV = WS_KB + NH * 64 * 512;
constexpr unsigned WS_AB = WS_VV + 64 * 4 * VP * 16;
constexpr unsigned WS_YP = WS_AB + 40 * 128 * 1024;
constexpr unsigned WS_OA = WS_YP + 4 * 64 * 40 * 1024;
constexpr unsigned WS_OB = WS_OA + 64 * NH * 1024;
constexpr unsigned WS_WO = WS_OB + 64 * NH * 256;
constexpr unsigned WS_G = WS_WO + 40 * 20 * 1024;
constexpr unsigned WS_FLAG = WS_G + SEQ * 4;
constexpr unsigned WS_END = WS_FLAG + 264 * 128;
constexpr float QSCALE = 0.22360679774997896f * 1.4426950408889634f;
#define ST_AUX 16

#define STAMP(k, i) do {} while (0)
#define STAMP_END(k) do {} while (0)
#define STAMP_FLUSH(kk) do {} while (0)
#define MFMA32(a, b, c) __builtin_amdgcn_mfma_f32_32x32x16_bf16((a), (b), (c), 0, 0, 0)
#define MFMA16(a, b, c) __builtin_amdgcn_mfma_f32_16x16x32_bf16((a), (b), (c), 0, 0, 0)
#define SBAR() __builtin_amdgcn_sched_barrier(0)
#define PIN(x) asm volatile("" : "+v"(x))
#define KEEP(x) asm volatile("" ::"v"(x))

typedef __attribute__((ext_vector_type(2))) __bf16 bf16x2;
typedef __attribute__((ext_vector_type(2))) float f32x2;
DI unsigned pk2(float lo, float hi) {
    f32x2 v = {lo, hi};
    return __builtin_bit_cast(unsigned, __builtin_convertvector(v, bf16x2));
}
DI float bsel(unsigned m, float a, float b) { return __uint_as_float((__float_as_uint(a) & m) | (__float_as_uint(b) & ~m)); }
DI float bflo(unsigned v) { return __uint_as_float(v << 16); }
DI float bfhi(unsigned v) { return __uint_as_float(v & 0xFFFF0000u); }
DI void dma16(const void* sbase, unsigned voff, unsigned lds) {
    asm volatile("s_mov_b32 m0, %2\n\ts_nop 0\n\tglobal_load_lds_dwordx4 %0, %1" ::"v"(voff), "s"(sbase), "s"(lds) : "memory");
}
template <int N> DI void dma16_lo(const void* sbase, unsigned voff, unsigned lds) {
    unsigned long long sv;
    asm volatile("s_mov_b32 m0, %3\n\ts_mov_b64 %0, exec\n\ts_mov_b64 exec, %4\n\tglobal_load_lds_dwordx4 %1, %2\n\ts_mov_b64 exec, %0"
                 : "=&s"(sv) : "v"(voff), "s"(sbase), "s"(lds), "s"((1ull << N) - 1) : "memory");
}
DI void touch(const void* sbase, unsigned voff, unsigned lds) {
    asm volatile("s_mov_b32 m0, %2\n\ts_nop 0\n\tglobal_load_lds_dword %0, %1" ::"v"(voff), "s"(sbase), "s"(lds) : "memory");
}
template <int N> DI void wait_vm() { asm volatile("s_waitcnt vmcnt(%0)" ::"i"(N) : "memory"); }
DI void bar() { asm volatile("s_waitcnt lgkmcnt(0)\n\ts_barrier" ::: "memory"); }
DI void bar_raw() { asm volatile("s_barrier" ::: "memory"); }
DI lptr lbase(unsigned a) { asm volatile("" : "+v"(a)); return (lptr)(uintptr_t)a; }
DI bf16x8 lds128(lptr p) { return *(const LAS bf16x8*)p; }
DI u32x2 lds64(lptr p) { return *(const LAS u32x2*)p; }
DI s16x4 ldstr(lptr p) {
    typedef short v4i16_t __attribute__((ext_vector_type(4)));
    return __builtin_bit_cast(s16x4, __builtin_amdgcn_ds_read_tr16_b64_v4i16((LAS v4i16_t*)p));
}
DI u32x4 pack8(const f32x16& x, int s, float sc) {
    u32x4 p;
    p[0] = pk2(x[8 * s + 0] * sc, x[8 * s + 1] * sc);
    p[1] = pk2(x[8 * s + 2] * sc, x[8 * s + 3] * sc);
    p[2] = pk2(x[8 * s + 4] * sc, x[8 * s + 5] * sc);
    p[3] = pk2(x[8 * s + 6] * sc, x[8 * s + 7] * sc);
    return p;
}
typedef __amdgpu_buffer_rsrc_t rsrc_t;
DI rsrc_t ws_rsrc(const void* ws) { return __builtin_amdgcn_make_buffer_rsrc((void*)ws, (short)0, (int)WS_END, 0x00020000); }
DI void st128(rsrc_t r, unsigned voff, unsigned soff, u32x4 v) { __builtin_amdgcn_raw_buffer_store_b128(v, r, voff + soff, 0, ST_AUX); }
DI void st64(rsrc_t r, unsigned voff, unsigned soff, u32x2 v) { __builtin_amdgcn_raw_buffer_store_b64(v, r, voff, soff, ST_AUX); }
DI void st32(rsrc_t r, unsigned voff, unsigned soff, unsigned v) { __builtin_amdgcn_raw_buffer_store_b32(v, r, voff, soff, ST_AUX); }
#define LD_AUX 0
DI u32x4 ld128(rsrc_t r, unsigned voff) { return __builtin_amdgcn_raw_buffer_load_b128(r, voff, 0, LD_AUX); }
DI bf16x8 as_frag(u32x4 v) { return __builtin_bit_cast(bf16x8, v); }
typedef __attribute__((ext_vector_type(16))) __bf16 bf16x16;
template <int HI> DI f32x16 pv_mma(bf16x8 v, const f32x16& p, const f32x16& o, int idx) {
    const auto ph = __builtin_shufflevector(p, p, 8 * HI, 8 * HI + 1, 8 * HI + 2, 8 * HI + 3, 8 * HI + 4, 8 * HI + 5, 8 * HI + 6, 8 * HI + 7);
    return __builtin_amdgcn_smfmac_f32_32x32x32_bf16(v, __builtin_bit_cast(bf16x16, ph), o, idx, 0, 0);
}
DI u32x4 pack8n(const f32x16& x, int s, float sc) {
    const u32x4 p = pack8(x, s, sc);
    const auto r0 = __builtin_amdgcn_permlane32_swap(p[0], p[2], false, false);
    const auto r1 = __builtin_amdgcn_permlane32_swap(p[1], p[3], false, false);
    return u32x4{r0[0], r1[0], r0[1], r1[1]};
}

struct Side { u32x2 gx[5], gg[5]; u16 wv[8]; };
DI void side_load(Side& S, const u16* __restrict__ x, const u16* __restrict__ wo, const u16* __restrict__ gw, int gi) {
    const int lane = threadIdx.x & 63, r = lane & 31, h = lane >> 5;
#pragma unroll
    for (int c = 0; c < 5; ++c) {
        S.gx[c] = *(const u32x2*)(x + (size_t)(gi * 2 + h) * DM + r * 20 + c * 4);
        S.gg[c] = *(const u32x2*)(gw + r * 20 + c * 4);
    }
    const int wf = gi < 800 ? gi : 0, wnt = wf / 20, wks = wf - wnt * 20, q4 = lane >> 4;
    const int k0 = wks < 16 ? (2 * wks + (q4 >> 1)) * 20 + 4 * (q4 & 1) : (8 * (wks - 16) + 2 * q4) * 20 + 16;
    const int k1 = wks < 16 ? k0 + 8 : k0 + 20;
    const u16* wsrc = wo + wnt * 16 + (lane & 15);
#pragma unroll
    for (int k = 0; k < 4; ++k) { S.wv[k] = wsrc[(k0 + k) * DM]; S.wv[4 + k] = wsrc[(k1 + k) * DM]; }
}
DI void side_finish(const Side& S, char* __restrict__ ws, int gi) {
    const int lane = threadIdx.x & 63, r = lane & 31, h = lane >> 5;
    const rsrc_t wr = ws_rsrc(ws);
    if (gi < 800)
        st128(wr, lane * 16, WS_WO + gi * 1024, u32x4{S.wv[0] | ((unsigned)S.wv[1] << 16), S.wv[2] | ((unsigned)S.wv[3] << 16), S.wv[4] | ((unsigned)S.wv[5] << 16), S.wv[6] | ((unsigned)S.wv[7] << 16)});
    float z = 0.f;
#pragma unroll
    for (int c = 0; c < 5; ++c)
#pragma unroll
        for (int k = 0; k < 2; ++k) z += bflo(S.gx[c][k]) * bflo(S.gg[c][k]) + bfhi(S.gx[c][k]) * bfhi(S.gg[c][k]);
#pragma unroll
    for (int o = 16; o > 0; o >>= 1) z += __shfl_xor(z, o);
    if (r == 0) st32(wr, (gi * 2 + h) * 4, WS_G, __float_as_uint(1.0f / (1.0f + __expf(-z))));
}

constexpr int P_STG = 45056;
constexpr int P_RING = 3 * P_STG;

DI void store_ab(const f32x16& acc, int dt, int stile, rsrc_t ws, int lane) {
    const float sc = dt >= 20 ? -1.f : 1.f;
    const int nt = dt >= 20 ? dt - 20 : dt;
    const int ks = (dt >= 20 ? 64 : 0) + stile;
    const unsigned dst = WS_AB + (unsigned)((nt * 2 + ((lane >> 4) & 1)) * 128 + ks) * 1024u + (unsigned)((lane >> 5) * 256 + (lane & 15) * 16);
#pragma unroll
    for (int s = 0; s < 2; ++s) st128(ws, dst, s * 512, pack8n(acc, s, sc));
}

template <bool PATH_A>
DI void proj_load(const u16* __restrict__ x, const u16* __restrict__ wqkv, const u16* __restrict__ cd, const u16* __restrict__ sd,
                  const u16* __restrict__ wo, const u16* __restrict__ gw, Side& S, int rb, int grp, unsigned lds0, int wid) {
    const int lane = threadIdx.x & 63;
    constexpr int ND = PATH_A ? 1 : 7, NDMA = PATH_A ? 10 : 11;
    const int dt0 = PATH_A ? 28 + grp : 7 * grp;
    const unsigned vrow = (unsigned)(lane >> 3) * 1280u + (unsigned)(((lane & 7) ^ ((4 * (wid & 1) + (lane >> 4)) & 7)) * 16);
    const char* xb = (const char*)x + ((size_t)rb * 128 + wid * 8) * 1280;
    unsigned vw[5];
    if (PATH_A) {
#pragma unroll
        for (int j = 0; j < 5; ++j) { const int i = (wid + 4 * j) * 64 + lane; vw[j] = (unsigned)(i / 20) * 3840u + (unsigned)(i % 20) * 16u; }
    }
    const char* wb = (const char*)wqkv + grp * 320;
    const char* db[ND];
#pragma unroll
    for (int j = 0; j < ND; ++j) {
        const int dt = dt0 + j;
        db[j] = (dt >= 20 ? (const char*)sd + (size_t)(dt - 20) * 40960 : (const char*)cd + (size_t)dt * 40960) + wid * 8 * 1280;
    }
    unsigned slot = lds0 + wid * 1024;
#define PROT 3
    int st = PROT == 1 ? ((rb & 7) * 5 / 4 + (int)(blockIdx.x >> 6) * 5 / 2) % 10 : PROT == 3 ? 3 * ((rb + (int)(blockIdx.x >> 6)) % 3) : PROT == 2 ? 5 * ((rb + (int)(blockIdx.x >> 6)) & 1) : PROT == 4 ? ((rb + (int)(blockIdx.x >> 6)) & 3) * 5 / 2 : 0;
    auto issue = [&]() {
#pragma unroll
        for (int j = 0; j < 4; ++j) dma16(xb + j * 40960 + st * 128, vrow, slot + j * 4096);
#pragma unroll
        for (int j = 0; j < ND; ++j) dma16(db[j] + st * 128, vrow, slot + 16384 + j * 4096);
        if (PATH_A) {
#pragma unroll
            for (int j = 0; j < 5; ++j) dma16(wb + (size_t)st * 64 * 3840, vw[j], slot + 20480 + j * 4096);
        }
        slot = slot + P_STG == lds0 + wid * 1024 + P_RING ? lds0 + wid * 1024 : slot + P_STG;
        st = st == 9 ? 0 : st + 1;
    };
#pragma unroll 1
    for (int n = 0; n < 3; ++n) issue();
    wait_vm<2 * NDMA>(); bar_raw();
    wait_vm<NDMA>(); bar_raw(); bar_raw();
#pragma unroll 1
    for (int n = 3; n < 10; ++n) { issue(); wait_vm<NDMA>(); bar_raw(); bar_raw(); }
    wait_vm<0>();
    bar_raw();
    side_load(S, x, wo, gw, blockIdx.x * 4 + wid);
    bar_raw(); bar_raw(); bar_raw();
}

template <int PATH>
DI void proj_mma(char* __restrict__ ws, int rb, int grp, unsigned lds0, int wid ) {
    const int lane = threadIdx.x & 63, r = lane & 31, h = lane >> 5;
    constexpr bool PATH_A = PATH < 2;
    constexpr int NW = PATH_A ? 5 : 0, ND = PATH_A ? 1 : 7, NT = NW + ND;
    const int dt0 = PATH_A ? 28 + grp : 7 * grp;
    const unsigned sw = lds0 + (unsigned)r * 128u + (unsigned)((h ^ ((r >> 1) & 7)) * 16);
    const unsigned awo = lds0 + 20480u + (unsigned)(8 * h + ((lane & 15) >> 2)) * 320u + (unsigned)(((lane >> 4) & 1) * 32 + (lane & 3) * 8);
    f32x16 acc[NT];
#pragma unroll
    for (int t = 0; t < NT; ++t) acc[t] = f32x16{};
    bf16x8 fx[2], fw[2][NT];
    lptr pr, px, pw;
    auto setp = [&](unsigned so, int ks) {
        pr = lbase((sw ^ (unsigned)(ks * 32)) + so);
        px = lbase((sw ^ (unsigned)(ks * 32)) + so + wid * 4096);
        if (PATH_A && ks == 0) pw = lbase(awo + so);
    };
    auto rd = [&](int ks, int b, int t) {
        if (t == 0) fx[b] = lds128(px);
        if (t < ND) fw[b][t] = lds128(pr + 16384 + t * 4096);
        else {
            s16x4 lo = ldstr(pw + ks * 16 * 320 + (t - ND) * 64), hi = ldstr(pw + ks * 16 * 320 + 4 * 320 + (t - ND) * 64);
            fw[b][t] = __builtin_bit_cast(bf16x8, __builtin_shufflevector(lo, hi, 0, 1, 2, 3, 4, 5, 6, 7));
        }
    };
    auto mm = [&](int b, int t) {
        if (PATH == 0 && t >= ND) acc[t] = MFMA32(fw[b][t], fx[b], acc[t]);
        else acc[t] = MFMA32(fx[b], fw[b][t], acc[t]);
    };
    unsigned so = 0;
    setp(0, 0);
#pragma unroll
    for (int t = 0; t < NT; ++t) rd(0, 0, t);
    SBAR();
#pragma unroll 1
    for (int st = 0; st < 10; ++st) {
#pragma unroll
        for (int ks = 0; ks < 4; ++ks) {
            if (ks == 3) { bar_raw(); so = so == 2 * P_STG ? 0 : so + P_STG; }
            setp(so, (ks + 1) & 3);
#pragma unroll
            for (int t = 0; t < NT; ++t) { rd((ks + 1) & 3, (ks + 1) & 1, t); SBAR(); mm(ks & 1, t); SBAR(); }
        }
        bar_raw();
    }
    STAMP(0, 2);
    const rsrc_t wr = ws_rsrc(ws);
    const int stile = rb * 4 + wid;
    if (PATH == 0) {
        const float sc = grp < 4 ? QSCALE : 1.f;
        const unsigned t0 = (unsigned)(grp * 8 * 64 + stile);
        const unsigned va = WS_QA + t0 * 1024 + lane * 16, vb = WS_QB + (t0 + h * 64) * 512 + r * 16;
#pragma unroll
        for (int m = 0; m < 4; ++m) {
            u32x2 q[5];
#pragma unroll
            for (int i = 0; i < 5; ++i) {
                const int p = 5 * m + i, t = ND + (p >> 2), g = p & 3;
                q[i][0] = pk2(acc[t][4 * g] * sc, acc[t][4 * g + 1] * sc);
                q[i][1] = pk2(acc[t][4 * g + 2] * sc, acc[t][4 * g + 3] * sc);
            }
            st128(wr, va, (2 * m) * 64 * 1024, u32x4{q[0][0], q[0][1], q[1][0], q[1][1]});
            st128(wr, va, (2 * m + 1) * 64 * 1024, u32x4{q[3][0], q[3][1], q[4][0], q[4][1]});
            st128(wr, vb, (2 * m) * 64 * 512, u32x4{q[2][0], q[2][1], 0x00003F80u, 0u});
        }
    } else if (PATH == 1) {
        const unsigned vv = WS_VV + (unsigned)((stile * 4 + h) * VP + (grp - 8) * 160 + r) * 16;
#pragma unroll
        for (int t = 0; t < 5; ++t) {
            st128(wr, vv, t * 512, pack8n(acc[ND + t], 0, 1.f));
            st128(wr, vv, t * 512 + 2 * VP * 16, pack8n(acc[ND + t], 1, 1.f));
        }
    }
#pragma unroll
    for (int t = 0; t < ND; ++t) store_ab(acc[t], dt0 + t, stile, wr, lane);
    STAMP(0, 3);
}
constexpr int F_STG = 36864;
constexpr int F_RING = 4 * F_STG;
DI void yg_issue(const u16* __restrict__ cs, const u16* __restrict__ ss, const char* __restrict__ ws, unsigned lds0, int lw, int st) {
    const int lane = threadIdx.x & 63;
    const int b = blockIdx.x, xcd = b & 7, i = b >> 3;
    const int tb = (xcd >> 2) * 8 + (i & 7), eg = i >> 3, sp = xcd & 3;
    const unsigned vrow = (unsigned)(lane >> 3) * 4096u + (unsigned)(((lane & 7) ^ ((4 * (lw & 1) + (lane >> 4)) & 7)) * 16);
    const char* rp = (const char*)(sp >= 2 ? ss : cs) + (size_t)(tb * 128 + lw * 8) * 4096 + (sp & 1) * 2048;
    const char* ab = ws + WS_AB + (size_t)((eg * 10 + (lw >> 1)) * 128 + sp * 32 + (lw & 1)) * 1024;
    const unsigned d = lds0 + lw * 1024 + (st & 3) * F_STG;
#pragma unroll
    for (int j = 0; j < 4; ++j) dma16(rp + j * 32 * 4096 + st * 128, vrow, d + j * 4096);
#pragma unroll
    for (int j = 0; j < 5; ++j) dma16(ab + (j * 256 + st * 2) * 1024, lane * 16, d + 16384 + j * 4096);
}

DI float xhalf_max(float v) {
    auto rr = __builtin_amdgcn_permlane32_swap(__float_as_uint(v), __float_as_uint(v), false, false);
    return fmaxf(__uint_as_float(rr[0]), __uint_as_float(rr[1]));
}
DI float max16(const f32x16& s) {
    float a = fmaxf(fmaxf(s[0], s[1]), s[2]), b = fmaxf(fmaxf(s[3], s[4]), s[5]);
    a = fmaxf(fmaxf(a, s[6]), s[7]); b = fmaxf(fmaxf(b, s[8]), s[9]);
    a = fmaxf(fmaxf(a, s[10]), s[11]); b = fmaxf(fmaxf(b, s[12]), s[13]);
    a = fmaxf(fmaxf(a, s[14]), s[15]);
    return fmaxf(a, b);
}
#define EX2(v) __builtin_amdgcn_exp2f(v)
constexpr unsigned VV_T = 4 * VP * 16, VV_S = 2 * VP * 16;
constexpr unsigned ONES = 0x3F803F80u;

DI u32x4 fix_v(u32x4 v, int r) {
#pragma unroll
    for (int i = 0; i < 4; ++i) v[i] = r < 20 ? v[i] : (r == 20 ? ONES : 0u);
    return v;
}
DI f32x16 attn_safe(rsrc_t wr, int head, u32x4 qa, u32x4 qb, int lane) {
    const int r = lane & 31, h = lane >> 5;
    qb[2] = 0u;
    f32x16 o = f32x16{};
    float m = -INFINITY;
#pragma unroll 1
    for (int t = 0; t < 64; ++t) {
        const u32x4 ka = ld128(wr, WS_KA + (unsigned)(head * 64 + t) * 1024 + lane * 16);
        const u32x4 kb = ld128(wr, WS_KB + (unsigned)(head * 64 + t) * 512 + r * 16);
        const unsigned vp = WS_VV + (unsigned)((t * 4 + h) * VP + head * 20 + (r < 20 ? r : 0)) * 16;
        const u32x4 v0 = fix_v(ld128(wr, vp), r), v1 = fix_v(ld128(wr, vp + VV_S), r);
        f32x16 s = MFMA32(as_frag(ka), as_frag(qa), f32x16{});
        s = MFMA32(as_frag(kb), as_frag(qb), s);
        const float mn = fmaxf(m, xhalf_max(max16(s)));
        const float al = EX2(m - mn);
        m = mn;
#pragma unroll
        for (int i = 0; i < 16; ++i) { o[i] *= al; s[i] = EX2(s[i] - mn); }
        o = pv_mma<0>(as_frag(v0), s, o, (int)0xDDDDDDDD);
        o = pv_mma<1>(as_frag(v1), s, o, (int)0xDDDDDDDD);
    }
    return o;
}

#define H_PACE 8
#define H_START 2
DI void attn_finish(f32x16 o, rsrc_t wr, int head, int qt) {
    const int lane = threadIdx.x & 63, r = lane & 31, h = lane >> 5;
    auto rl = __builtin_amdgcn_permlane32_swap(__float_as_uint(o[8]), __float_as_uint(o[8]), false, false);
    float l = __uint_as_float(rl[1]);
    float z = 0.f;
#pragma unroll
    for (int k = 0; k < 12; ++k) z = __builtin_fmaf(o[k], 0.f, z);
    const bool ok = (z == 0.f) && (l >= 8.6736174e-19f) && (l <= 1.2676506e30f);
    if (__builtin_expect(!__all(ok), 0)) {
        const u32x4 q0 = ld128(wr, WS_QA + (unsigned)(head * 64 + qt) * 1024 + lane * 16);
        const u32x4 q1 = ld128(wr, h ? 0x80000000u : WS_QB + (unsigned)(head * 64 + qt) * 512 + r * 16);
        o = attn_safe(wr, head, q0, q1, lane);
        rl = __builtin_amdgcn_permlane32_swap(__float_as_uint(o[8]), __float_as_uint(o[8]), false, false);
        l = __uint_as_float(rl[1]);
    }
    const float inv = __builtin_amdgcn_rcpf(l);
    const unsigned ot = (unsigned)(qt * NH + head);
    st128(wr, lane * 16, WS_OA + ot * 1024, pack8(o, 0, inv));
    if (h == 0) st64(wr, r * 8, WS_OB + ot * 256, u32x2{pk2(o[8] * inv, o[9] * inv), pk2(o[10] * inv, o[11] * inv)});
}

constexpr int KVT_B = 2880, KVG_B = 4 * KVT_B, KV1 = 3 * F_STG;
constexpr int KV_FULL = KV1 + 4 * KVG_B;
constexpr int KV_FREE = KV_FULL + 16;
constexpr int LDS_SCR = KV_FREE + 16;
DI unsigned kv_off(unsigned g) { return g < 9 ? g * KVG_B : KV1 + ((g - 9) & 3) * KVG_B; }
DI void kv_wait_free(unsigned lds0, unsigned n) {
    for (;;) {
        const u32x4 f = *(const volatile LAS u32x4*)(uintptr_t)(lds0 + KV_FREE);
        if (__builtin_amdgcn_readfirstlane(min(min(f[0], f[1]), min(f[2], f[3]))) >= n) break;
        __builtin_amdgcn_s_sleep(8);
    }
    asm volatile("" ::: "memory");
}
DI void lds_put(unsigned addr, unsigned v) { *(volatile LAS unsigned*)(uintptr_t)addr = v; }

DI void phase_attn(char* __restrict__ ws, unsigned lds0, int wid) {
    const int lane = threadIdx.x & 63, r = lane & 31, h = lane >> 5;
    const int b = blockIdx.x, head = (b & 7) * 4 + (b >> 6), qt0 = ((b >> 3) & 7) * 8 + wid * 2;
    const rsrc_t wr = ws_rsrc(ws);
    STAMP(1, 0);
    u32x4 qa[2], qbv[2];
#pragma unroll
    for (int i = 0; i < 2; ++i) {
        qa[i] = ld128(wr, WS_QA + (unsigned)(head * 64 + qt0 + i) * 1024 + lane * 16);
        qbv[i] = ld128(wr, h ? 0x80000000u : WS_QB + (unsigned)(head * 64 + qt0 + i) * 512 + r * 16);
    }
    const int rv = r <= 20 ? r : (r < 28 ? 20 : 16);
    const unsigned oka = lds0 + lane * 16, okb = lds0 + 1024 + r * 16, ov = lds0 + 1536u + (unsigned)(h * 21 + rv) * 16u;
    unsigned afull = lds0 + KV_FULL, afree = lds0 + KV_FREE + wid * 4;
    PIN(afull); PIN(afree);
    bar_raw();
    bf16x8 ka[2], kb[2], v0[2], v1[2];
    lptr pka = lbase(oka), pkb = lbase(okb), pv = lbase(ov);
#define LDK(n, T) do { ka[n] = lds128(pka + (T) * KVT_B); kb[n] = lds128(pkb + (T) * KVT_B); SBAR(); } while (0)
#define LDV(n, T) do { v0[n] = lds128(pv + (T) * KVT_B); v1[n] = lds128(pv + (T) * KVT_B + 42 * 16); SBAR(); } while (0)
    LDK(0, 0); LDK(1, 1); LDV(0, 0);
#pragma unroll
    for (int i = 0; i < 2; ++i) {
        qbv[i][2] = 0u;
        f32x16 t = MFMA32(ka[0], as_frag(qa[i]), f32x16{});
        t = MFMA32(kb[0], as_frag(qbv[i]), t);
        const float m0 = xhalf_max(max16(t));
        qbv[i][2] = h ? 0u : (pk2(-m0, 0.f) & 0xFFFFu);
    }
    STAMP(1, 1);
    f32x16 oA = f32x16{}, oB = f32x16{}, s0, s1, s2, s3 = f32x16{};
    int idx = (int)0xDDDDDDDD;
    PIN(idx);
    v0[1] = ka[0]; v1[1] = ka[0];
    s0 = MFMA32(ka[0], as_frag(qa[0]), f32x16{});
    s0 = MFMA32(kb[0], as_frag(qbv[0]), s0);
    SBAR();
#define GRP(MF, H, S, i) do { MF; SBAR(); H; SBAR(); S[i] = EX2(S[i]); S[i + 1] = EX2(S[i + 1]); S[i + 2] = EX2(S[i + 2]); S[i + 3] = EX2(S[i + 3]); PIN(S); SBAR(); } while (0)
#define SLOT(X, N, kn, Q, P, O, vn, H0, H1, H2, H3) do { \
        GRP(N = MFMA32(ka[kn], as_frag(qa[Q]), f32x16{}), H0, X, 0); \
        GRP(N = MFMA32(kb[kn], as_frag(qbv[Q]), N), H1, X, 4); \
        GRP(O = pv_mma<0>(v0[vn], P, O, idx), H2, X, 8); \
        GRP(O = pv_mma<1>(v1[vn], P, O, idx), H3, X, 12); } while (0)
#define RKA(n, T) ka[n] = lds128(pka + (T) * KVT_B)
#define RKB(n, T) kb[n] = lds128(pkb + (T) * KVT_B)
#define RV0(n, T) v0[n] = lds128(pv + (T) * KVT_B)
#define RV1(n, T) v1[n] = lds128(pv + (T) * KVT_B + 42 * 16)
#define LGKM0 __builtin_amdgcn_s_waitcnt(0xC07F)
#define NOP (void)0
    unsigned nx = KVG_B, gv = 0;
    lptr pvn;
#pragma unroll 1
    for (int g = 0; g < 16; ++g) {
        u32x2 full;
        SLOT(s0, s1, 0, 1, s3, oB, 1, LGKM0, RKA(0, 2), RKB(0, 2), RV0(1, 1));
        SLOT(s1, s2, 1, 0, s0, oA, 0, RV1(1, 1), NOP, NOP, NOP);
        SLOT(s2, s3, 1, 1, s1, oB, 0, LGKM0, RKA(1, 3), RKB(1, 3), RV0(0, 2));
        SLOT(s3, s0, 0, 0, s2, oA, 1, RV1(0, 2), full = *(const volatile LAS u32x2*)(uintptr_t)afull, pka = lbase(oka + nx), pkb = lbase(okb + nx));
        SLOT(s0, s1, 0, 1, s3, oB, 1,
             do { if (__builtin_expect(__builtin_amdgcn_readfirstlane(min(full[0], full[1])) < (unsigned)g + 2u, 0)) {
                      do { __builtin_amdgcn_s_sleep(1); full = *(const volatile LAS u32x2*)(uintptr_t)afull; } while (__builtin_amdgcn_readfirstlane(min(full[0], full[1])) < (unsigned)g + 2u); }
                  asm volatile("" ::: "memory"); } while (0),
             RKA(0, 0), RKB(0, 0), RV0(1, 3));
        SLOT(s1, s2, 1, 0, s0, oA, 0, RV1(1, 3), pvn = lbase(ov + nx), nx = kv_off(g + 2), NOP);
        pv = pvn;
        SLOT(s2, s3, 1, 1, s1, oB, 0, LGKM0, RKA(1, 1), RKB(1, 1), RV0(0, 0));
        SLOT(s3, s0, 0, 0, s2, oA, 1, RV1(0, 0), do { gv += 1; PIN(gv); } while (0), lds_put(afree, gv)  , NOP);
    }
    oB = pv_mma<0>(v0[1], s3, oB, idx);
    oB = pv_mma<1>(v1[1], s3, oB, idx);
    STAMP(1, 2);
    attn_finish(oA, wr, head, qt0);
    attn_finish(oB, wr, head, qt0 + 1);
    STAMP(1, 3);
}
DI void attn_produce(const char* __restrict__ ws, unsigned lds0, int pw) {
    const int lane = threadIdx.x & 63;
    const int b = blockIdx.x, head = (b & 7) * 4 + (b >> 6);
    unsigned so[6], sd[6];
#pragma unroll
    for (int j = 0; j < 6; ++j) {
        const int cc = min(j * 64 + lane, 359), tl = pw * 2 + (cc >= 180), c = cc >= 180 ? cc - 180 : cc;
        const int v = c - 96, g = v / 21, d = v - g * 21;
        so[j] = c < 64 ? WS_KA + (unsigned)(head * 64 + tl) * 1024 + c * 16
              : c < 96 ? WS_KB + (unsigned)(head * 64 + tl) * 512 + (c - 64) * 16
                       : WS_VV + (unsigned)((tl * 4 + g) * VP + (d < 20 ? head * 20 + d : 640)) * 16;
        sd[j] = c < 64 ? 4096u : c < 96 ? 2048u : 4 * VV_T;
    }
    auto issue = [&](unsigned g) {
        const unsigned dst = lds0 + kv_off(g) + pw * 2 * KVT_B;
#pragma unroll
        for (int j = 0; j < 5; ++j) { dma16(ws, so[j], dst + j * 1024); so[j] += sd[j]; }
        dma16_lo<40>(ws, so[5], dst + 5 * 1024); so[5] += sd[5];
    };
    issue(0); issue(1); issue(2);
    if (pw == 0 && lane < 4) lds_put(lds0 + KV_FREE + lane * 4, 0u);
    lds_put(lds0 + KV_FULL + pw * 4, 1u);
    wait_vm<12>(); bar();
#pragma unroll 1
    for (unsigned g = 3; g < 13; ++g) { issue(g); wait_vm<12>(); lds_put(lds0 + KV_FULL + pw * 4, g - 1); }
#pragma unroll 1
    for (unsigned g = 13; g < 16; ++g) {
        wait_vm<0>(); lds_put(lds0 + KV_FULL + pw * 4, g);
        kv_wait_free(lds0, g - 3);
        issue(g);
    }
    wait_vm<0>(); lds_put(lds0 + KV_FULL + pw * 4, 17u);
}
DI void attn_helper(const u16* __restrict__ cs, const u16* __restrict__ ss, const char* __restrict__ ws, unsigned lds0, int hw) {
    const int lane = threadIdx.x & 63;
    const int b = blockIdx.x, qb = (b >> 3) & 7;
    bar_raw();
    kv_wait_free(lds0, H_START);
    const char* pfp = (const char*)((b & 2) ? ss : cs) + (size_t)((((b & 7) >> 2) * 8 + qb) * 128 + (b >> 6) * 32 + hw * 16) * 4096 + (b & 1) * 2048;
#pragma unroll 1
    for (int n = 0; n < 16; ++n) { touch(pfp + n * 128, (lane >> 2) * 4096, lds0 + LDS_SCR); __builtin_amdgcn_s_sleep(H_PACE); }
    kv_wait_free(lds0, 9);
#pragma unroll 1
    for (int n = 0; n < 6; ++n) { yg_issue(cs, ss, ws, lds0, hw * 2 + (n & 1), n >> 1); __builtin_amdgcn_s_sleep(4); }
}

typedef __attribute__((address_space(1))) unsigned gu32;
#define RLX_AGENT __ATOMIC_RELAXED, __HIP_MEMORY_SCOPE_AGENT
DI void poll1(unsigned& v, gu32* c) {
    unsigned long long sv;
    asm volatile("s_mov_b64 %1, exec\n\ts_mov_b64 exec, 1\n\tglobal_load_dword %0, %2, %3 sc1\n\ts_mov_b64 exec, %1" : "+v"(v), "=&s"(sv) : "v"(0u), "s"(c) : "memory");
}
DI void wait_count(gu32* c, unsigned seen, unsigned want) {
    for (unsigned spins = 0; (unsigned)__builtin_amdgcn_readfirstlane(seen) < want && spins < (1u << 17); ++spins) {
        __builtin_amdgcn_s_sleep(1);
        poll1(seen, c);
        asm volatile("s_waitcnt vmcnt(0)" : "+v"(seen)::"memory");
    }
    asm volatile("" ::: "memory");
}
DI u32x4 ld128c(rsrc_t r, unsigned voff) { return __builtin_amdgcn_raw_buffer_load_b128(r, voff, 0, 16); }
DI u32x2 ld64c(rsrc_t r, unsigned voff) { return __builtin_amdgcn_raw_buffer_load_b64(r, voff, 0, 16); }

DI void phase_tail(const u16* __restrict__ cs, const u16* __restrict__ ss, char* __restrict__ ws, u16* __restrict__ out, unsigned lds0, int wid) {
    const int lane = threadIdx.x & 63, c = lane & 15, q = lane >> 4;
    const int b = blockIdx.x, xcd = b & 7, i = b >> 3;
    const int tb = (xcd >> 2) * 8 + (i & 7), eg = i >> 3, sp = xcd & 3;
    const int sb = tb * 2 + (sp & 1), ng = eg * 2 + (sp >> 1);
    STAMP(3, 0);
    if (wid >= 4) {
        const int lw = wid - 4;
        wait_vm<0>(); bar();
#pragma unroll 1
        for (int st = 3; st < 16; ++st) {
            yg_issue(cs, ss, ws, lds0, lw, st);
            wait_vm<18>(); bar_raw();
            if (st == 6 && threadIdx.x == 256) __hip_atomic_fetch_add((gu32*)(ws + WS_FLAG + (256 + (i & 7)) * 128), 1u, RLX_AGENT);
        }
        const char* wb = ws + WS_WO + (size_t)(ng * 100 + lw) * 1024;
#pragma unroll 1
        for (int st = 0; st < 3; ++st) {
#pragma unroll
            for (int j = 0; j < 7; ++j) dma16(wb + st * F_STG + j * 4096, lane * 16, lds0 + lw * 1024 + st * F_STG + j * 4096);
            if (st < 2) {
#pragma unroll
                for (int j = 7; j < 9; ++j) dma16(wb + st * F_STG + j * 4096, lane * 16, lds0 + lw * 1024 + st * F_STG + j * 4096);
                wait_vm<18>(); bar_raw();
            }
        }
        wait_vm<0>(); bar_raw();
        bar_raw();
        return;
    }
    const rsrc_t wr = ws_rsrc(ws);
    const int rh = wid & 1, kk = wid >> 1;
    bf16x8 fo[5][2][2];
    {
        const int ch = wid >> 1;
        f32x4 acc[4][5];
#pragma unroll
        for (int u = 0; u < 4; ++u)
#pragma unroll
            for (int t = 0; t < 5; ++t) acc[u][t] = f32x4{};
        bf16x8 frE[2][4], fnE[2][5], frO[2][4], fnO[2][5];
        const unsigned af = lds0 + 16384 + ch * 10240 + lane * 16;
        const unsigned a0 = lds0 + (unsigned)(rh * 64 + c) * 128u + (unsigned)((q ^ (c >> 1)) * 16);
        auto step = [&](bf16x8 (&fr)[2][4], bf16x8 (&fn)[2][5], const bf16x8 (&pr)[2][4], const bf16x8 (&pn)[2][5], int st, bool ld, bool mm) {
            lptr p, r0, r1;
            if (ld) {
                bar();
                const unsigned so = (st & 3) * F_STG;
                p = lbase(af + so); r0 = lbase(a0 + so); r1 = lbase((a0 ^ 64u) + so);
            }
#pragma unroll
            for (int k = 0; k < 2; ++k)
#pragma unroll
                for (int t = 0; t < 5; ++t) {
                    if (ld) {
                        fn[k][t] = lds128(p + t * 2048 + k * 1024);
                        if (t < 4) fr[k][t] = lds128((k ? r1 : r0) + t * 2048);
                    }
                    SBAR();
                    if (mm) {
#pragma unroll
                        for (int u = 0; u < 4; ++u) acc[u][t] = MFMA16(pn[k][t], pr[k][u], acc[u][t]);
                    }
                    SBAR();
                }
        };
        gu32* ocnt = (gu32*)(ws + WS_FLAG + (256 + (sb >> 2)) * 128);
        unsigned oseen = 0;
        const int poll_at = wid == 0 ? 8 : 99;
        step(frE, fnE, frO, fnO, 0, true, false);
        STAMP(3, 1);
#pragma unroll 1
        for (int st = 0; st < 14; st += 2) {
            step(frO, fnO, frE, fnE, st + 1, true, true);
            if (st == 2) wait_vm<0>();
            if (st == 2) STAMP(3, 7);
            if (st == poll_at) poll1(oseen, ocnt);
            step(frE, fnE, frO, fnO, st + 2, true, true);
        }
        STAMP(3, 4);
        if (wid == 0) {
            asm volatile("s_waitcnt vmcnt(0)" : "+v"(oseen)::"memory");
            wait_count(ocnt, oseen, 32u);
        }
        step(frO, fnO, frE, fnE, 15, true, true);
        STAMP(3, 5);
        step(frE, fnE, frO, fnO, 16, false, true);
        STAMP(3, 2);
        const unsigned yp = WS_YP + (unsigned)(((tb * 4 + eg) * 4 + wid) * 4 + sp) * 10240u + lane * 16;
#pragma unroll
        for (int u = 0; u < 2; ++u)
#pragma unroll
            for (int t = 0; t < 5; ++t) {
                const f32x4 v0 = acc[2 * u][t], v1 = acc[2 * u + 1][t];
                st128(wr, yp, (u * 5 + t) * 1024, u32x4{pk2(v0[0], v0[1]), pk2(v0[2], v0[3]), pk2(v1[0], v1[1]), pk2(v1[2], v1[3])});
            }
        SBAR();
        STAMP(3, 3);
        const unsigned oa = WS_OA + (unsigned)((sb * 2 + rh) * NH + kk * 4) * 1024u + q * 512 + c * 16;
        const unsigned ob = WS_OB + (unsigned)((sb * 2 + rh) * NH + kk * 16) * 256u + q * 512 + c * 8;
#pragma unroll
        for (int j = 0; j < 4; ++j)
#pragma unroll
            for (int k = 0; k < 2; ++k)
#pragma unroll
                for (int u = 0; u < 2; ++u) fo[j][k][u] = as_frag(ld128c(wr, oa + j * 8192 + k * 2048 + u * 256));
#pragma unroll
        for (int k = 0; k < 2; ++k)
#pragma unroll
            for (int u = 0; u < 2; ++u) {
                const u32x2 a = ld64c(wr, ob + k * 2048 + u * 128), bq = ld64c(wr, ob + k * 2048 + u * 128 + 256);
                fo[4][k][u] = as_frag(u32x4{a[0], a[1], bq[0], bq[1]});
            }
        SBAR();
        STAMP(3, 6);
        wait_vm<24>();
        STAMP(2, 5);
        if (lane == 0) __hip_atomic_fetch_add((gu32*)(ws + WS_FLAG + ((tb * 4 + eg) * 4 + wid) * 128), 1u, RLX_AGENT);
    }
    const int s0 = sb * 64 + rh * 32 + c;
    f32x4 acc[2][5];
#pragma unroll
    for (int t = 0; t < 5; ++t) { acc[0][t] = f32x4{}; acc[1][t] = f32x4{}; }
    bar();
    STAMP(2, 0);
    {
        const lptr p0 = lbase(lds0 + kk * 2048 + lane * 16), p1 = lbase(lds0 + kk * 2048 + lane * 16 + 61440);
        bf16x8 fn[2][5];
#define WOF(n, t) lds128(((t) < 3 ? p0 + (t) * 20480 : p1 + ((t) - 3) * 20480) + ((n) >> 1) * 4096 + ((n) & 1) * 1024)
#pragma unroll
        for (int t = 0; t < 5; ++t) fn[0][t] = WOF(0, t);
#pragma unroll
        for (int n = 0; n < 10; ++n)
#pragma unroll
            for (int t = 0; t < 5; ++t) {
                if (n < 9) fn[(n + 1) & 1][t] = WOF(n + 1, t);
                SBAR();
                acc[0][t] = MFMA16(fn[n & 1][t], fo[n >> 1][n & 1][0], acc[0][t]);
                acc[1][t] = MFMA16(fn[n & 1][t], fo[n >> 1][n & 1][1], acc[1][t]);
                SBAR();
            }
    }
    STAMP(2, 1);
    gu32* ycnt = (gu32*)(ws + WS_FLAG + ((tb * 4 + eg) * 4 + sp) * 128);
    unsigned yseen = 0;
    poll1(yseen, ycnt);
    const float g0 = ((const float*)(ws + WS_G))[s0], g1 = ((const float*)(ws + WS_G))[s0 + 16];
    const int t0 = kk * 3;
    f32x4 fin[2][3];
    {
        LAS f32x4* xw = (LAS f32x4*)(uintptr_t)(lds0 + 3 * F_STG + wid * 6144 + lane * 16);
        const LAS f32x4* xr = (const LAS f32x4*)(uintptr_t)(lds0 + 3 * F_STG + (wid ^ 2) * 6144 + lane * 16);
        unsigned mk = 0u - (unsigned)kk;
        PIN(mk);
#pragma unroll
        for (int u = 0; u < 2; ++u)
#pragma unroll
            for (int t = 0; t < 3; ++t) {
                const f32x4 lo = acc[u][t], hi = acc[u][t < 2 ? 3 + t : 4];
                f32x4 snd;
#pragma unroll
                for (int e = 0; e < 4; ++e) { fin[u][t][e] = bsel(mk, hi[e], lo[e]); snd[e] = bsel(mk, lo[e], hi[e]); }
                xw[(u * 3 + t) * 64] = snd;
            }
        bar();
#pragma unroll
        for (int u = 0; u < 2; ++u)
#pragma unroll
            for (int t = 0; t < 3; ++t) fin[u][t] = (fin[u][t] + xr[(u * 3 + t) * 64]) * (u ? g1 : g0);
    }
    asm volatile("s_waitcnt vmcnt(0)" : "+v"(yseen)::"memory");
    wait_count(ycnt, yseen, 4u);
    STAMP(2, 2);
    u32x4 yv[4][3];
    const unsigned yb = WS_YP + (unsigned)(((tb * 4 + eg) * 4 + sp) * 4) * 10240u + (unsigned)(rh * 5 + t0) * 1024u + lane * 16;
#pragma unroll
    for (int t = 0; t < 3; ++t)
#pragma unroll
        for (int v = 0; v < 4; ++v) yv[v][t] = ld128c(wr, yb + v * 10240 + (t0 + t < 5 ? t : 0) * 1024);
    u16* op = out + (size_t)s0 * DM + ng * 80 + t0 * 16 + q * 4;
#pragma unroll
    for (int t = 0; t < 3; ++t) {
        if (t0 + t < 5) {
#pragma unroll
            for (int u = 0; u < 2; ++u) {
                const float gm = 1.0f - (u ? g1 : g0);
                const float y0 = (bflo(yv[0][t][2 * u]) + bflo(yv[1][t][2 * u])) + (bflo(yv[2][t][2 * u]) + bflo(yv[3][t][2 * u]));
                const float y1 = (bfhi(yv[0][t][2 * u]) + bfhi(yv[1][t][2 * u])) + (bfhi(yv[2][t][2 * u]) + bfhi(yv[3][t][2 * u]));
                const float y2 = (bflo(yv[0][t][2 * u + 1]) + bflo(yv[1][t][2 * u + 1])) + (bflo(yv[2][t][2 * u + 1]) + bflo(yv[3][t][2 * u + 1]));
                const float y3 = (bfhi(yv[0][t][2 * u + 1]) + bfhi(yv[1][t][2 * u + 1])) + (bfhi(yv[2][t][2 * u + 1]) + bfhi(yv[3][t][2 * u + 1]));
                const f32x4 a = fin[u][t];
                *(u32x2*)(op + (size_t)u * 16 * DM + t * 16) = u32x2{pk2(a[0] + gm * y0, a[1] + gm * y1), pk2(a[2] + gm * y2, a[3] + gm * y3)};
            }
        }
    }
    STAMP_END(2);
}

constexpr int LDS_B = LDS_SCR + 256;

__global__ __launch_bounds__(512) void k_proj(const u16* __restrict__ x, const u16* __restrict__ wqkv, const u16* __restrict__ cd,
                                              const u16* __restrict__ sd, const u16* __restrict__ wo, const u16* __restrict__ gw,
                                              char* __restrict__ ws) {
    extern __shared__ __attribute__((aligned(16))) char smem[];
    const unsigned lds0 = (unsigned)(uintptr_t)smem;
    const int wid = __builtin_amdgcn_readfirstlane(threadIdx.x >> 6);
    const int b = blockIdx.x, xcd = b & 7, i = b >> 3;
    const int rb = (xcd & 1) * 8 + (i & 7), cq = xcd >> 1, gl = i >> 3;
    if (wid >= 4) {
        Side S;
        if (gl == 3) proj_load<false>(x, wqkv, cd, sd, wo, gw, S, rb, cq, lds0, wid - 4);
        else proj_load<true>(x, wqkv, cd, sd, wo, gw, S, rb, cq * 3 + gl, lds0, wid - 4);
        side_finish(S, ws, b * 4 + wid - 4);
        return;
    }
    STAMP(0, 0);
    if (b == 255) {
        const unsigned tl = threadIdx.x;
        st128(ws_rsrc(ws), (tl * VP + 640) * 16, WS_VV, u32x4{ONES, ONES, ONES, ONES});
        st32(ws_rsrc(ws), tl * 128, WS_FLAG, 0u);
        if (tl < 8) st32(ws_rsrc(ws), (256 + tl) * 128, WS_FLAG, 0u);
    }
    bar_raw();
    STAMP(0, 1);
    if (wid == 0) touch((const char*)wo + b * 3200, min(threadIdx.x * 128u, 3196u), lds0 + F_RING);
    if (gl == 3) proj_mma<2>(ws, rb, cq, lds0, wid);
    else if (cq * 3 + gl < 8) proj_mma<0>(ws, rb, cq * 3 + gl, lds0, wid);
    else proj_mma<1>(ws, rb, cq * 3 + gl, lds0, wid);
    wait_vm<0>();
    STAMP(0, 4);
    STAMP_FLUSH(0);
}

__global__ __launch_bounds__(512) void k_mix(const u16* __restrict__ x, const u16* __restrict__ wo, const u16* __restrict__ gw,
                                             const u16* __restrict__ cs, const u16* __restrict__ ss, char* __restrict__ ws, u16* __restrict__ out) {
    extern __shared__ __attribute__((aligned(16))) char smem[];
    const int wid = __builtin_amdgcn_readfirstlane(threadIdx.x >> 6);
    if (wid < 4) phase_attn(ws, (unsigned)(uintptr_t)smem, wid);
    else if (wid < 6) attn_produce(ws, (unsigned)(uintptr_t)smem, wid - 4);
    else attn_helper(cs, ss, ws, (unsigned)(uintptr_t)smem, wid - 6);
    phase_tail(cs, ss, ws, out, (unsigned)(uintptr_t)smem, wid);
    STAMP_FLUSH(1);
}

extern "C" void kernel_launch(void* const* d_in, const int* in_sizes, int n_in, void* d_out, int out_size, void* d_ws, size_t ws_size,
                              hipStream_t stream) {
    const u16* x = (const u16*)d_in[0];
    const u16* wqkv = (const u16*)d_in[1];
    const u16* wo = (const u16*)d_in[2];
    const u16* cs = (const u16*)d_in[4];
    const u16* ss = (const u16*)d_in[5];
    const u16* cd = (const u16*)d_in[6];
    const u16* sd = (const u16*)d_in[7];
    const u16* gw = (const u16*)d_in[8];
    char* ws = (char*)d_ws;
    (void)hipFuncSetAttribute((const void*)k_proj, hipFuncAttributeMaxDynamicSharedMemorySize, LDS_B);
    (void)hipFuncSetAttribute((const void*)k_mix, hipFuncAttributeMaxDynamicSharedMemorySize, LDS_B);
    k_proj<<<256, 512, LDS_B, stream>>>(x, wqkv, cd, sd, wo, gw, ws);
    k_mix<<<256, 512, LDS_B, stream>>>(x, wo, gw, cs, ss, ws, (u16*)d_out);
}
```

```cpp
#define K1LINES 225u
#define K2LINES 130u
#include <hip/hip_runtime.h>
#include <hip/hip_bf16.h>
#include <stdint.h>

typedef unsigned short u16;
typedef __attribute__((ext_vector_type(8))) __bf16 bf16x8;
typedef __attribute__((ext_vector_type(4))) short s16x4;
typedef __attribute__((ext_vector_type(16))) float f32x16;
typedef __attribute__((ext_vector_type(4))) float f32x4;
typedef __attribute__((ext_vector_type(4))) unsigned u32x4;
typedef __attribute__((ext_vector_type(2))) unsigned u32x2;
#define LAS __attribute__((address_space(3)))
#define DI __device__ __forceinline__
typedef const LAS char* lptr;

constexpr int SEQ = 2048, DM = 640, NH = 32;
constexpr int VP = 648;
constexpr unsigned WS_QA = 0;
constexpr unsigned WS_KA = WS_QA + NH * 64 * 1024;
constexpr unsigned WS_QB = WS_KA + NH * 64 * 1024;
constexpr unsigned WS_KB = WS_QB + NH * 64 * 512;
constexpr unsigned WS_VV = WS_KB + NH * 64 * 512;
constexpr unsigned WS_AB = WS_VV + 64 * 4 * VP * 16;
constexpr unsigned WS_YP = WS_AB + 40 * 128 * 1024;
constexpr unsigned WS_OA = WS_YP + 4 * 64 * 40 * 1024;
constexpr unsigned WS_OB = WS_OA + 64 * NH * 1024;
constexpr unsigned WS_WO = WS_OB + 64 * NH * 256;
constexpr unsigned WS_G = WS_WO + 40 * 20 * 1024;
constexpr unsigned WS_FLAG = WS_G + SEQ * 4;
constexpr unsigned WS_END = WS_FLAG + 264 * 128;
constexpr float QSCALE = 0.22360679774997896f * 1.4426950408889634f;
#define ST_AUX 16

#define STAMP(k, i) do {} while (0)
#define STAMP_END(k) do {} while (0)
#define STAMP_FLUSH(kk) do {} while (0)
#define MFMA32(a, b, c) __builtin_amdgcn_mfma_f32_32x32x16_bf16((a), (b), (c), 0, 0, 0)
#define MFMA16(a, b, c) __builtin_amdgcn_mfma_f32_16x16x32_bf16((a), (b), (c), 0, 0, 0)
#define SBAR() __builtin_amdgcn_sched_barrier(0)
#define PIN(x) asm volatile("" : "+v"(x))
#define KEEP(x) asm volatile("" ::"v"(x))

typedef __attribute__((ext_vector_type(2))) __bf16 bf16x2;
typedef __attribute__((ext_vector_type(2))) float f32x2;
DI unsigned pk2(float lo, float hi) {
    f32x2 v = {lo, hi};
    return __builtin_bit_cast(unsigned, __builtin_convertvector(v, bf16x2));
}
DI float bsel(unsigned m, float a, float b) { return __uint_as_float((__float_as_uint(a) & m) | (__float_as_uint(b) & ~m)); }
DI float bflo(unsigned v) { return __uint_as_float(v << 16); }
DI float bfhi(unsigned v) { return __uint_as_float(v & 0xFFFF0000u); }
DI void dma16(const void* sbase, unsigned voff, unsigned lds) {
    asm volatile("s_mov_b32 m0, %2\n\ts_nop 0\n\tglobal_load_lds_dwordx4 %0, %1" ::"v"(voff), "s"(sbase), "s"(lds) : "memory");
}
template <int N> DI void dma16_lo(const void* sbase, unsigned voff, unsigned lds) {
    unsigned long long sv;
    asm volatile("s_mov_b32 m0, %3\n\ts_mov_b64 %0, exec\n\ts_mov_b64 exec, %4\n\tglobal_load_lds_dwordx4 %1, %2\n\ts_mov_b64 exec, %0"
                 : "=&s"(sv) : "v"(voff), "s"(sbase), "s"(lds), "s"((1ull << N) - 1) : "memory");
}
DI void touch(const void* sbase, unsigned voff, unsigned lds) {
    asm volatile("s_mov_b32 m0, %2\n\ts_nop 0\n\tglobal_load_lds_dword %0, %1" ::"v"(voff), "s"(sbase), "s"(lds) : "memory");
}
template <int N> DI void wait_vm() { asm volatile("s_waitcnt vmcnt(%0)" ::"i"(N) : "memory"); }
DI void bar() { asm volatile("s_waitcnt lgkmcnt(0)\n\ts_barrier" ::: "memory"); }
DI void bar_raw() { asm volatile("s_barrier" ::: "memory"); }
DI lptr lbase(unsigned a) { asm volatile("" : "+v"(a)); return (lptr)(uintptr_t)a; }
DI bf16x8 lds128(lptr p) { return *(const LAS bf16x8*)p; }
DI u32x2 lds64(lptr p) { return *(const LAS u32x2*)p; }
DI s16x4 ldstr(lptr p) {
    typedef short v4i16_t __attribute__((ext_vector_type(4)));
    return __builtin_bit_cast(s16x4, __builtin_amdgcn_ds_read_tr16_b64_v4i16((LAS v4i16_t*)p));
}
DI u32x4 pack8(const f32x16& x, int s, float sc) {
    u32x4 p;
    p[0] = pk2(x[8 * s + 0] * sc, x[8 * s + 1] * sc);
    p[1] = pk2(x[8 * s + 2] * sc, x[8 * s + 3] * sc);
    p[2] = pk2(x[8 * s + 4] * sc, x[8 * s + 5] * sc);
    p[3] = pk2(x[8 * s + 6] * sc, x[8 * s + 7] * sc);
    return p;
}
typedef __amdgpu_buffer_rsrc_t rsrc_t;
DI rsrc_t ws_rsrc(const void* ws) { return __builtin_amdgcn_make_buffer_rsrc((void*)ws, (short)0, (int)WS_END, 0x00020000); }
DI void st128(rsrc_t r, unsigned voff, unsigned soff, u32x4 v) { __builtin_amdgcn_raw_buffer_store_b128(v, r, voff + soff, 0, ST_AUX); }
DI void st64(rsrc_t r, unsigned voff, unsigned soff, u32x2 v) { __builtin_amdgcn_raw_buffer_store_b64(v, r, voff, soff, ST_AUX); }
DI void st32(rsrc_t r, unsigned voff, unsigned soff, unsigned v) { __builtin_amdgcn_raw_buffer_store_b32(v, r, voff, soff, ST_AUX); }
#define LD_AUX 0
DI u32x4 ld128(rsrc_t r, unsigned voff) { return __builtin_amdgcn_raw_buffer_load_b128(r, voff, 0, LD_AUX); }
DI bf16x8 as_frag(u32x4 v) { return __builtin_bit_cast(bf16x8, v); }
typedef __attribute__((ext_vector_type(16))) __bf16 bf16x16;
template <int HI> DI f32x16 pv_mma(bf16x8 v, const f32x16& p, const f32x16& o, int idx) {
    const auto ph = __builtin_shufflevector(p, p, 8 * HI, 8 * HI + 1, 8 * HI + 2, 8 * HI + 3, 8 * HI + 4, 8 * HI + 5, 8 * HI + 6, 8 * HI + 7);
    return __builtin_amdgcn_smfmac_f32_32x32x32_bf16(v, __builtin_bit_cast(bf16x16, ph), o, idx, 0, 0);
}
DI u32x4 pack8n(const f32x16& x, int s, float sc) {
    const u32x4 p = pack8(x, s, sc);
    const auto r0 = __builtin_amdgcn_permlane32_swap(p[0], p[2], false, false);
    const auto r1 = __builtin_amdgcn_permlane32_swap(p[1], p[3], false, false);
    return u32x4{r0[0], r1[0], r0[1], r1[1]};
}

struct Side { u32x2 gx[5], gg[5]; u16 wv[8]; };
DI void side_load(Side& S, const u16* __restrict__ x, const u16* __restrict__ wo, const u16* __restrict__ gw, int gi) {
    const int lane = threadIdx.x & 63, r = lane & 31, h = lane >> 5;
#pragma unroll
    for (int c = 0; c < 5; ++c) {
        S.gx[c] = *(const u32x2*)(x + (size_t)(gi * 2 + h) * DM + r * 20 + c * 4);
        S.gg[c] = *(const u32x2*)(gw + r * 20 + c * 4);
    }
    const int wf = gi < 800 ? gi : 0, wnt = wf / 20, wks = wf - wnt * 20, q4 = lane >> 4;
    const int k0 = wks < 16 ? (2 * wks + (q4 >> 1)) * 20 + 4 * (q4 & 1) : (8 * (wks - 16) + 2 * q4) * 20 + 16;
    const int k1 = wks < 16 ? k0 + 8 : k0 + 20;
    const u16* wsrc = wo + wnt * 16 + (lane & 15);
#pragma unroll
    for (int k = 0; k < 4; ++k) { S.wv[k] = wsrc[(k0 + k) * DM]; S.wv[4 + k] = wsrc[(k1 + k) * DM]; }
}
DI void side_finish(const Side& S, char* __restrict__ ws, int gi) {
    const int lane = threadIdx.x & 63, r = lane & 31, h = lane >> 5;
    const rsrc_t wr = ws_rsrc(ws);
    if (gi < 800)
        st128(wr, lane * 16, WS_WO + gi * 1024, u32x4{S.wv[0] | ((unsigned)S.wv[1] << 16), S.wv[2] | ((unsigned)S.wv[3] << 16), S.wv[4] | ((unsigned)S.wv[5] << 16), S.wv[6] | ((unsigned)S.wv[7] << 16)});
    float z = 0.f;
#pragma unroll
    for (int c = 0; c < 5; ++c)
#pragma unroll
        for (int k = 0; k < 2; ++k) z += bflo(S.gx[c][k]) * bflo(S.gg[c][k]) + bfhi(S.gx[c][k]) * bfhi(S.gg[c][k]);
#pragma unroll
    for (int o = 16; o > 0; o >>= 1) z += __shfl_xor(z, o);
    if (r == 0) st32(wr, (gi * 2 + h) * 4, WS_G, __float_as_uint(1.0f / (1.0f + __expf(-z))));
}

constexpr int P_STG = 45056;
constexpr int P_RING = 3 * P_STG;

DI void store_ab(const f32x16& acc, int dt, int stile, rsrc_t ws, int lane) {
    const float sc = dt >= 20 ? -1.f : 1.f;
    const int nt = dt >= 20 ? dt - 20 : dt;
    const int ks = (dt >= 20 ? 64 : 0) + stile;
    const unsigned dst = WS_AB + (unsigned)((nt * 2 + ((lane >> 4) & 1)) * 128 + ks) * 1024u + (unsigned)((lane >> 5) * 256 + (lane & 15) * 16);
#pragma unroll
    for (int s = 0; s < 2; ++s) st128(ws, dst, s * 512, pack8n(acc, s, sc));
}

template <bool PATH_A>
DI void proj_load(const u16* __restrict__ x, const u16* __restrict__ wqkv, const u16* __restrict__ cd, const u16* __restrict__ sd,
                  const u16* __restrict__ wo, const u16* __restrict__ gw, Side& S, int rb, int grp, unsigned lds0, int wid) {
    const int lane = threadIdx.x & 63;
    constexpr int ND = PATH_A ? 1 : 7, NDMA = PATH_A ? 10 : 11;
    const int dt0 = PATH_A ? 28 + grp : 7 * grp;
    const unsigned vrow = (unsigned)(lane >> 3) * 1280u + (unsigned)(((lane & 7) ^ ((4 * (wid & 1) + (lane >> 4)) & 7)) * 16);
    const char* xb = (const char*)x + ((size_t)rb * 128 + wid * 8) * 1280;
    unsigned vw[5];
    if (PATH_A) {
#pragma unroll
        for (int j = 0; j < 5; ++j) { const int i = (wid + 4 * j) * 64 + lane; vw[j] = (unsigned)(i / 20) * 3840u + (unsigned)(i % 20) * 16u; }
    }
    const char* wb = (const char*)wqkv + grp * 320;
    const char* db[ND];
#pragma unroll
    for (int j = 0; j < ND; ++j) {
        const int dt = dt0 + j;
        db[j] = (dt >= 20 ? (const char*)sd + (size_t)(dt - 20) * 40960 : (const char*)cd + (size_t)dt * 40960) + wid * 8 * 1280;
    }
    unsigned slot = lds0 + wid * 1024;
#define PROT 3
    int st = PROT == 1 ? ((rb & 7) * 5 / 4 + (int)(blockIdx.x >> 6) * 5 / 2) % 10 : PROT == 3 ? 3 * ((rb + (int)(blockIdx.x >> 6)) % 3) : PROT == 2 ? 5 * ((rb + (int)(blockIdx.x >> 6)) & 1) : PROT == 4 ? ((rb + (int)(blockIdx.x >> 6)) & 3) * 5 / 2 : 0;
    auto issue = [&]() {
#pragma unroll
        for (int j = 0; j < 4; ++j) dma16(xb + j * 40960 + st * 128, vrow, slot + j * 4096);
#pragma unroll
        for (int j = 0; j < ND; ++j) dma16(db[j] + st * 128, vrow, slot + 16384 + j * 4096);
        if (PATH_A) {
#pragma unroll
            for (int j = 0; j < 5; ++j) dma16(wb + (size_t)st * 64 * 3840, vw[j], slot + 20480 + j * 4096);
        }
        slot = slot + P_STG == lds0 + wid * 1024 + P_RING ? lds0 + wid * 1024 : slot + P_STG;
        st = st == 9 ? 0 : st + 1;
    };
#pragma unroll 1
    for (int n = 0; n < 3; ++n) issue();
    wait_vm<2 * NDMA>(); bar_raw();
    wait_vm<NDMA>(); bar_raw(); bar_raw();
#pragma unroll 1
    for (int n = 3; n < 10; ++n) { issue(); wait_vm<NDMA>(); bar_raw(); bar_raw(); }
    wait_vm<0>();
    bar_raw();
    side_load(S, x, wo, gw, blockIdx.x * 4 + wid);
    bar_raw(); bar_raw(); bar_raw();
}

template <int PATH>
DI void proj_mma(char* __restrict__ ws, int rb, int grp, unsigned lds0, int wid ) {
    const int lane = threadIdx.x & 63, r = lane & 31, h = lane >> 5;
    constexpr bool PATH_A = PATH < 2;
    constexpr int NW = PATH_A ? 5 : 0, ND = PATH_A ? 1 : 7, NT = NW + ND;
    const int dt0 = PATH_A ? 28 + grp : 7 * grp;
    const unsigned sw = lds0 + (unsigned)r * 128u + (unsigned)((h ^ ((r >> 1) & 7)) * 16);
    const unsigned awo = lds0 + 20480u + (unsigned)(8 * h + ((lane & 15) >> 2)) * 320u + (unsigned)(((lane >> 4) & 1) * 32 + (lane & 3) * 8);
    f32x16 acc[NT];
#pragma unroll
    for (int t = 0; t < NT; ++t) acc[t] = f32x16{};
    bf16x8 fx[2], fw[2][NT];
    lptr pr, px, pw;
    auto setp = [&](unsigned so, int ks) {
        pr = lbase((sw ^ (unsigned)(ks * 32)) + so);
        px = lbase((sw ^ (unsigned)(ks * 32)) + so + wid * 4096);
        if (PATH_A && ks == 0) pw = lbase(awo + so);
    };
    auto rd = [&](int ks, int b, int t) {
        if (t == 0) fx[b] = lds128(px);
        if (t < ND) fw[b][t] = lds128(pr + 16384 + t * 4096);
        else {
            s16x4 lo = ldstr(pw + ks * 16 * 320 + (t - ND) * 64), hi = ldstr(pw + ks * 16 * 320 + 4 * 320 + (t - ND) * 64);
            fw[b][t] = __builtin_bit_cast(bf16x8, __builtin_shufflevector(lo, hi, 0, 1, 2, 3, 4, 5, 6, 7));
        }
    };
    auto mm = [&](int b, int t) {
        if (PATH == 0 && t >= ND) acc[t] = MFMA32(fw[b][t], fx[b], acc[t]);
        else acc[t] = MFMA32(fx[b], fw[b][t], acc[t]);
    };
    unsigned so = 0;
    setp(0, 0);
#pragma unroll
    for (int t = 0; t < NT; ++t) rd(0, 0, t);
    SBAR();
#pragma unroll 1
    for (int st = 0; st < 10; ++st) {
#pragma unroll
        for (int ks = 0; ks < 4; ++ks) {
            if (ks == 3) { bar_raw(); so = so == 2 * P_STG ? 0 : so + P_STG; }
            setp(so, (ks + 1) & 3);
#pragma unroll
            for (int t = 0; t < NT; ++t) { rd((ks + 1) & 3, (ks + 1) & 1, t); SBAR(); mm(ks & 1, t); SBAR(); }
        }
        bar_raw();
    }
    STAMP(0, 2);
    const rsrc_t wr = ws_rsrc(ws);
    const int stile = rb * 4 + wid;
    if (PATH == 0) {
        const float sc = grp < 4 ? QSCALE : 1.f;
        const unsigned t0 = (unsigned)(grp * 8 * 64 + stile);
        const unsigned va = WS_QA + t0 * 1024 + lane * 16, vb = WS_QB + (t0 + h * 64) * 512 + r * 16;
#pragma unroll
        for (int m = 0; m < 4; ++m) {
            u32x2 q[5];
#pragma unroll
            for (int i = 0; i < 5; ++i) {
                const int p = 5 * m + i, t = ND + (p >> 2), g = p & 3;
                q[i][0] = pk2(acc[t][4 * g] * sc, acc[t][4 * g + 1] * sc);
                q[i][1] = pk2(acc[t][4 * g + 2] * sc, acc[t][4 * g + 3] * sc);
            }
            st128(wr, va, (2 * m) * 64 * 1024, u32x4{q[0][0], q[0][1], q[1][0], q[1][1]});
            st128(wr, va, (2 * m + 1) * 64 * 1024, u32x4{q[3][0], q[3][1], q[4][0], q[4][1]});
            st128(wr, vb, (2 * m) * 64 * 512, u32x4{q[2][0], q[2][1], 0x00003F80u, 0u});
        }
    } else if (PATH == 1) {
        const unsigned vv = WS_VV + (unsigned)((stile * 4 + h) * VP + (grp - 8) * 160 + r) * 16;
#pragma unroll
        for (int t = 0; t < 5; ++t) {
            st128(wr, vv, t * 512, pack8n(acc[ND + t], 0, 1.f));
            st128(wr, vv, t * 512 + 2 * VP * 16, pack8n(acc[ND + t], 1, 1.f));
        }
    }
#pragma unroll
    for (int t = 0; t < ND; ++t) store_ab(acc[t], dt0 + t, stile, wr, lane);
    STAMP(0, 3);
}
constexpr int F_STG = 36864;
constexpr int F_RING = 4 * F_STG;
DI void yg_issue(const u16* __restrict__ cs, const u16* __restrict__ ss, const char* __restrict__ ws, unsigned lds0, int lw, int st) {
    const int lane = threadIdx.x & 63;
    const int b = blockIdx.x, xcd = b & 7, i = b >> 3;
    const int tb = (xcd >> 2) * 8 + (i & 7), eg = i >> 3, sp = xcd & 3;
    const unsigned vrow = (unsigned)(lane >> 3) * 4096u + (unsigned)(((lane & 7) ^ ((4 * (lw & 1) + (lane >> 4)) & 7)) * 16);
    const char* rp = (const char*)(sp >= 2 ? ss : cs) + (size_t)(tb * 128 + lw * 8) * 4096 + (sp & 1) * 2048;
    const char* ab = ws + WS_AB + (size_t)((eg * 10 + (lw >> 1)) * 128 + sp * 32 + (lw & 1)) * 1024;
    const unsigned d = lds0 + lw * 1024 + (st & 3) * F_STG;
#pragma unroll
    for (int j = 0; j < 4; ++j) dma16(rp + j * 32 * 4096 + st * 128, vrow, d + j * 4096);
#pragma unroll
    for (int j = 0; j < 5; ++j) dma16(ab + (j * 256 + st * 2) * 1024, lane * 16, d + 16384 + j * 4096);
}

DI float xhalf_max(float v) {
    auto rr = __builtin_amdgcn_permlane32_swap(__float_as_uint(v), __float_as_uint(v), false, false);
    return fmaxf(__uint_as_float(rr[0]), __uint_as_float(rr[1]));
}
DI float max16(const f32x16& s) {
    float a = fmaxf(fmaxf(s[0], s[1]), s[2]), b = fmaxf(fmaxf(s[3], s[4]), s[5]);
    a = fmaxf(fmaxf(a, s[6]), s[7]); b = fmaxf(fmaxf(b, s[8]), s[9]);
    a = fmaxf(fmaxf(a, s[10]), s[11]); b = fmaxf(fmaxf(b, s[12]), s[13]);
    a = fmaxf(fmaxf(a, s[14]), s[15]);
    return fmaxf(a, b);
}
#define EX2(v) __builtin_amdgcn_exp2f(v)
constexpr unsigned VV_T = 4 * VP * 16, VV_S = 2 * VP * 16;
constexpr unsigned ONES = 0x3F803F80u;

DI u32x4 fix_v(u32x4 v, int r) {
#pragma unroll
    for (int i = 0; i < 4; ++i) v[i] = r < 20 ? v[i] : (r == 20 ? ONES : 0u);
    return v;
}
DI f32x16 attn_safe(rsrc_t wr, int head, u32x4 qa, u32x4 qb, int lane) {
    const int r = lane & 31, h = lane >> 5;
    qb[2] = 0u;
    f32x16 o = f32x16{};
    float m = -INFINITY;
#pragma unroll 1
    for (int t = 0; t < 64; ++t) {
        const u32x4 ka = ld128(wr, WS_KA + (unsigned)(head * 64 + t) * 1024 + lane * 16);
        const u32x4 kb = ld128(wr, WS_KB + (unsigned)(head * 64 + t) * 512 + r * 16);
        const unsigned vp = WS_VV + (unsigned)((t * 4 + h) * VP + head * 20 + (r < 20 ? r : 0)) * 16;
        const u32x4 v0 = fix_v(ld128(wr, vp), r), v1 = fix_v(ld128(wr, vp + VV_S), r);
        f32x16 s = MFMA32(as_frag(ka), as_frag(qa), f32x16{});
        s = MFMA32(as_frag(kb), as_frag(qb), s);
        const float mn = fmaxf(m, xhalf_max(max16(s)));
        const float al = EX2(m - mn);
        m = mn;
#pragma unroll
        for (int i = 0; i < 16; ++i) { o[i] *= al; s[i] = EX2(s[i] - mn); }
        o = pv_mma<0>(as_frag(v0), s, o, (int)0xDDDDDDDD);
        o = pv_mma<1>(as_frag(v1), s, o, (int)0xDDDDDDDD);
    }
    return o;
}

#define H_PACE 8
#define H_START 2
DI void attn_finish(f32x16 o, rsrc_t wr, int head, int qt) {
    const int lane = threadIdx.x & 63, r = lane & 31, h = lane >> 5;
    auto rl = __builtin_amdgcn_permlane32_swap(__float_as_uint(o[8]), __float_as_uint(o[8]), false, false);
    float l = __uint_as_float(rl[1]);
    float z = 0.f;
#pragma unroll
    for (int k = 0; k < 12; ++k) z = __builtin_fmaf(o[k], 0.f, z);
    const bool ok = (z == 0.f) && (l >= 8.6736174e-19f) && (l <= 1.2676506e30f);
    if (__builtin_expect(!__all(ok), 0)) {
        const u32x4 q0 = ld128(wr, WS_QA + (unsigned)(head * 64 + qt) * 1024 + lane * 16);
        const u32x4 q1 = ld128(wr, h ? 0x80000000u : WS_QB + (unsigned)(head * 64 + qt) * 512 + r * 16);
        o = attn_safe(wr, head, q0, q1, lane);
        rl = __builtin_amdgcn_permlane32_swap(__float_as_uint(o[8]), __float_as_uint(o[8]), false, false);
        l = __uint_as_float(rl[1]);
    }
    const float inv = __builtin_amdgcn_rcpf(l);
    const unsigned ot = (unsigned)(qt * NH + head);
    st128(wr, lane * 16, WS_OA + ot * 1024, pack8(o, 0, inv));
    if (h == 0) st64(wr, r * 8, WS_OB + ot * 256, u32x2{pk2(o[8] * inv, o[9] * inv), pk2(o[10] * inv, o[11] * inv)});
}

constexpr int KVT_B = 2880, KVG_B = 4 * KVT_B, KV1 = 3 * F_STG;
constexpr int KV_FULL = KV1 + 4 * KVG_B;
constexpr int KV_FREE = KV_FULL + 16;
constexpr int LDS_SCR = KV_FREE + 16;
DI unsigned kv_off(unsigned g) { return g < 9 ? g * KVG_B : KV1 + ((g - 9) & 3) * KVG_B; }
DI void kv_wait_free(unsigned lds0, unsigned n) {
    for (;;) {
        const u32x4 f = *(const volatile LAS u32x4*)(uintptr_t)(lds0 + KV_FREE);
        if (__builtin_amdgcn_readfirstlane(min(min(f[0], f[1]), min(f[2], f[3]))) >= n) break;
        __builtin_amdgcn_s_sleep(8);
    }
    asm volatile("" ::: "memory");
}
DI void lds_put(unsigned addr, unsigned v) { *(volatile LAS unsigned*)(uintptr_t)addr = v; }

DI void phase_attn(char* __restrict__ ws, unsigned lds0, int wid) {
    const int lane = threadIdx.x & 63, r = lane & 31, h = lane >> 5;
    const int b = blockIdx.x, head = (b & 7) * 4 + (b >> 6), qt0 = ((b >> 3) & 7) * 8 + wid * 2;
    const rsrc_t wr = ws_rsrc(ws);
    STAMP(1, 0);
    u32x4 qa[2], qbv[2];
#pragma unroll
    for (int i = 0; i < 2; ++i) {
        qa[i] = ld128(wr, WS_QA + (unsigned)(head * 64 + qt0 + i) * 1024 + lane * 16);
        qbv[i] = ld128(wr, h ? 0x80000000u : WS_QB + (unsigned)(head * 64 + qt0 + i) * 512 + r * 16);
    }
    const int rv = r <= 20 ? r : (r < 28 ? 20 : 16);
    const unsigned oka = lds0 + lane * 16, okb = lds0 + 1024 + r * 16, ov = lds0 + 1536u + (unsigned)(h * 21 + rv) * 16u;
    unsigned afull = lds0 + KV_FULL, afree = lds0 + KV_FREE + wid * 4;
    PIN(afull); PIN(afree);
    bar_raw();
    bf16x8 ka[2], kb[2], v0[2], v1[2];
    lptr pka = lbase(oka), pkb = lbase(okb), pv = lbase(ov);
#define LDK(n, T) do { ka[n] = lds128(pka + (T) * KVT_B); kb[n] = lds128(pkb + (T) * KVT_B); SBAR(); } while (0)
#define LDV(n, T) do { v0[n] = lds128(pv + (T) * KVT_B); v1[n] = lds128(pv + (T) * KVT_B + 42 * 16); SBAR(); } while (0)
    LDK(0, 0); LDK(1, 1); LDV(0, 0);
#pragma unroll
    for (int i = 0; i < 2; ++i) {
        qbv[i][2] = 0u;
        f32x16 t = MFMA32(ka[0], as_frag(qa[i]), f32x16{});
        t = MFMA32(kb[0], as_frag(qbv[i]), t);
        const float m0 = xhalf_max(max16(t));
        qbv[i][2] = h ? 0u : (pk2(-m0, 0.f) & 0xFFFFu);
    }
    STAMP(1, 1);
    f32x16 oA = f32x16{}, oB = f32x16{}, s0, s1, s2, s3 = f32x16{};
    int idx = (int)0xDDDDDDDD;
    PIN(idx);
    v0[1] = ka[0]; v1[1] = ka[0];
    s0 = MFMA32(ka[0], as_frag(qa[0]), f32x16{});
    s0 = MFMA32(kb[0], as_frag(qbv[0]), s0);
    SBAR();
#define GRP(MF, H, S, i) do { MF; SBAR(); H; SBAR(); S[i] = EX2(S[i]); S[i + 1] = EX2(S[i + 1]); S[i + 2] = EX2(S[i + 2]); S[i + 3] = EX2(S[i + 3]); PIN(S); SBAR(); } while (0)
#define SLOT(X, N, kn, Q, P, O, vn, H0, H1, H2, H3) do { \
        GRP(N = MFMA32(ka[kn], as_frag(qa[Q]), f32x16{}), H0, X, 0); \
        GRP(N = MFMA32(kb[kn], as_frag(qbv[Q]), N), H1, X, 4); \
        GRP(O = pv_mma<0>(v0[vn], P, O, idx), H2, X, 8); \
        GRP(O = pv_mma<1>(v1[vn], P, O, idx), H3, X, 12); } while (0)
#define RKA(n, T) ka[n] = lds128(pka + (T) * KVT_B)
#define RKB(n, T) kb[n] = lds128(pkb + (T) * KVT_B)
#define RV0(n, T) v0[n] = lds128(pv + (T) * KVT_B)
#define RV1(n, T) v1[n] = lds128(pv + (T) * KVT_B + 42 * 16)
#define LGKM0 __builtin_amdgcn_s_waitcnt(0xC07F)
#define NOP (void)0
    unsigned nx = KVG_B, gv = 0;
    lptr pvn;
#pragma unroll 1
    for (int g = 0; g < 16; ++g) {
        u32x2 full;
        SLOT(s0, s1, 0, 1, s3, oB, 1, LGKM0, RKA(0, 2), RKB(0, 2), RV0(1, 1));
        SLOT(s1, s2, 1, 0, s0, oA, 0, RV1(1, 1), NOP, NOP, NOP);
        SLOT(s2, s3, 1, 1, s1, oB, 0, LGKM0, RKA(1, 3), RKB(1, 3), RV0(0, 2));
        SLOT(s3, s0, 0, 0, s2, oA, 1, RV1(0, 2), full = *(const volatile LAS u32x2*)(uintptr_t)afull, pka = lbase(oka + nx), pkb = lbase(okb + nx));
        SLOT(s0, s1, 0, 1, s3, oB, 1,
             do { if (__builtin_expect(__builtin_amdgcn_readfirstlane(min(full[0], full[1])) < (unsigned)g + 2u, 0)) {
                      do { __builtin_amdgcn_s_sleep(1); full = *(const volatile LAS u32x2*)(uintptr_t)afull; } while (__builtin_amdgcn_readfirstlane(min(full[0], full[1])) < (unsigned)g + 2u); }
                  asm volatile("" ::: "memory"); } while (0),
             RKA(0, 0), RKB(0, 0), RV0(1, 3));
        SLOT(s1, s2, 1, 0, s0, oA, 0, RV1(1, 3), pvn = lbase(ov + nx), nx = kv_off(g + 2), NOP);
        pv = pvn;
        SLOT(s2, s3, 1, 1, s1, oB, 0, LGKM0, RKA(1, 1), RKB(1, 1), RV0(0, 0));
        SLOT(s3, s0, 0, 0, s2, oA, 1, RV1(0, 0), do { gv += 1; PIN(gv); } while (0), lds_put(afree, gv)  , NOP);
    }
    oB = pv_mma<0>(v0[1], s3, oB, idx);
    oB = pv_mma<1>(v1[1], s3, oB, idx);
    STAMP(1, 2);
    attn_finish(oA, wr, head, qt0);
    attn_finish(oB, wr, head, qt0 + 1);
    STAMP(1, 3);
}
DI void attn_produce(const char* __restrict__ ws, unsigned lds0, int pw) {
    const int lane = threadIdx.x & 63;
    const int b = blockIdx.x, head = (b & 7) * 4 + (b >> 6);
    unsigned so[6], sd[6];
#pragma unroll
    for (int j = 0; j < 6; ++j) {
        const int cc = min(j * 64 + lane, 359), tl = pw * 2 + (cc >= 180), c = cc >= 180 ? cc - 180 : cc;
        const int v = c - 96, g = v / 21, d = v - g * 21;
        so[j] = c < 64 ? WS_KA + (unsigned)(head * 64 + tl) * 1024 + c * 16
              : c < 96 ? WS_KB + (unsigned)(head * 64 + tl) * 512 + (c - 64) * 16
                       : WS_VV + (unsigned)((tl * 4 + g) * VP + (d < 20 ? head * 20 + d : 640)) * 16;
        sd[j] = c < 64 ? 4096u : c < 96 ? 2048u : 4 * VV_T;
    }
    auto issue = [&](unsigned g) {
        const unsigned dst = lds0 + kv_off(g) + pw * 2 * KVT_B;
#pragma unroll
        for (int j = 0; j < 5; ++j) { dma16(ws, so[j], dst + j * 1024); so[j] += sd[j]; }
        dma16_lo<40>(ws, so[5], dst + 5 * 1024); so[5] += sd[5];
    };
    issue(0); issue(1); issue(2);
    if (pw == 0 && lane < 4) lds_put(lds0 + KV_FREE + lane * 4, 0u);
    lds_put(lds0 + KV_FULL + pw * 4, 1u);
    wait_vm<12>(); bar();
#pragma unroll 1
    for (unsigned g = 3; g < 13; ++g) { issue(g); wait_vm<12>(); lds_put(lds0 + KV_FULL + pw * 4, g - 1); }
#pragma unroll 1
    for (unsigned g = 13; g < 16; ++g) {
        wait_vm<0>(); lds_put(lds0 + KV_FULL + pw * 4, g);
        kv_wait_free(lds0, g - 3);
        issue(g);
    }
    wait_vm<0>(); lds_put(lds0 + KV_FULL + pw * 4, 17u);
}
DI void attn_helper(const u16* __restrict__ cs, const u16* __restrict__ ss, const char* __restrict__ ws, unsigned lds0, int hw) {
    const int lane = threadIdx.x & 63;
    const int b = blockIdx.x, qb = (b >> 3) & 7;
    bar_raw();
    kv_wait_free(lds0, H_START);
    const char* pfp = (const char*)((b & 2) ? ss : cs) + (size_t)((((b & 7) >> 2) * 8 + qb) * 128 + (b >> 6) * 32 + hw * 16) * 4096 + (b & 1) * 2048;
#pragma unroll 1
    for (int n = 0; n < 16; ++n) { touch(pfp + n * 128, (lane >> 2) * 4096, lds0 + LDS_SCR); __builtin_amdgcn_s_sleep(H_PACE); }
    kv_wait_free(lds0, 9);
#pragma unroll 1
    for (int n = 0; n < 6; ++n) { yg_issue(cs, ss, ws, lds0, hw * 2 + (n & 1), n >> 1); __builtin_amdgcn_s_sleep(4); }
}

typedef __attribute__((address_space(1))) unsigned gu32;
#define RLX_AGENT __ATOMIC_RELAXED, __HIP_MEMORY_SCOPE_AGENT
DI void poll1(unsigned& v, gu32* c) {
    unsigned long long sv;
    asm volatile("s_mov_b64 %1, exec\n\ts_mov_b64 exec, 1\n\tglobal_load_dword %0, %2, %3 sc1\n\ts_mov_b64 exec, %1" : "+v"(v), "=&s"(sv) : "v"(0u), "s"(c) : "memory");
}
DI void wait_count(gu32* c, unsigned seen, unsigned want) {
    for (unsigned spins = 0; (unsigned)__builtin_amdgcn_readfirstlane(seen) < want && spins < (1u << 17); ++spins) {
        __builtin_amdgcn_s_sleep(1);
        poll1(seen, c);
        asm volatile("s_waitcnt vmcnt(0)" : "+v"(seen)::"memory");
    }
    asm volatile("" ::: "memory");
}
DI u32x4 ld128c(rsrc_t r, unsigned voff) { return __builtin_amdgcn_raw_buffer_load_b128(r, voff, 0, 16); }
DI u32x2 ld64c(rsrc_t r, unsigned voff) { return __builtin_amdgcn_raw_buffer_load_b64(r, voff, 0, 16); }

DI void phase_tail(const u16* __restrict__ cs, const u16* __restrict__ ss, char* __restrict__ ws, u16* __restrict__ out, unsigned lds0, int wid) {
    const int lane = threadIdx.x & 63, c = lane & 15, q = lane >> 4;
    const int b = blockIdx.x, xcd = b & 7, i = b >> 3;
    const int tb = (xcd >> 2) * 8 + (i & 7), eg = i >> 3, sp = xcd & 3;
    const int sb = tb * 2 + (sp & 1), ng = eg * 2 + (sp >> 1);
    STAMP(3, 0);
    if (wid >= 4) {
        const int lw = wid - 4;
        wait_vm<0>(); bar();
#pragma unroll 1
        for (int st = 3; st < 16; ++st) {
            yg_issue(cs, ss, ws, lds0, lw, st);
            wait_vm<18>(); bar_raw();
            if (st == 6 && threadIdx.x == 256) __hip_atomic_fetch_add((gu32*)(ws + WS_FLAG + (256 + (i & 7)) * 128), 1u, RLX_AGENT);
        }
        const char* wb = ws + WS_WO + (size_t)(ng * 100 + lw) * 1024;
#pragma unroll 1
        for (int st = 0; st < 3; ++st) {
#pragma unroll
            for (int j = 0; j < 7; ++j) dma16(wb + st * F_STG + j * 4096, lane * 16, lds0 + lw * 1024 + st * F_STG + j * 4096);
            if (st < 2) {
#pragma unroll
                for (int j = 7; j < 9; ++j) dma16(wb + st * F_STG + j * 4096, lane * 16, lds0 + lw * 1024 + st * F_STG + j * 4096);
                wait_vm<18>(); bar_raw();
            }
        }
        wait_vm<0>(); bar_raw();
        bar_raw();
        return;
    }
    const rsrc_t wr = ws_rsrc(ws);
    const int rh = wid & 1, kk = wid >> 1;
    bf16x8 fo[5][2][2];
    {
        const int ch = wid >> 1;
        f32x4 acc[4][5];
#pragma unroll
        for (int u = 0; u < 4; ++u)
#pragma unroll
            for (int t = 0; t < 5; ++t) acc[u][t] = f32x4{};
        bf16x8 frE[2][4], fnE[2][5], frO[2][4], fnO[2][5];
        const unsigned af = lds0 + 16384 + ch * 10240 + lane * 16;
        const unsigned a0 = lds0 + (unsigned)(rh * 64 + c) * 128u + (unsigned)((q ^ (c >> 1)) * 16);
        auto step = [&](bf16x8 (&fr)[2][4], bf16x8 (&fn)[2][5], const bf16x8 (&pr)[2][4], const bf16x8 (&pn)[2][5], int st, bool ld, bool mm) {
            lptr p, r0, r1;
            if (ld) {
                bar();
                const unsigned so = (st & 3) * F_STG;
                p = lbase(af + so); r0 = lbase(a0 + so); r1 = lbase((a0 ^ 64u) + so);
            }
#pragma unroll
            for (int k = 0; k < 2; ++k)
#pragma unroll
                for (int t = 0; t < 5; ++t) {
                    if (ld) {
                        fn[k][t] = lds128(p + t * 2048 + k * 1024);
                        if (t < 4) fr[k][t] = lds128((k ? r1 : r0) + t * 2048);
                    }
                    SBAR();
                    if (mm) {
#pragma unroll
                        for (int u = 0; u < 4; ++u) acc[u][t] = MFMA16(pn[k][t], pr[k][u], acc[u][t]);
                    }
                    SBAR();
                }
        };
        gu32* ocnt = (gu32*)(ws + WS_FLAG + (256 + (sb >> 2)) * 128);
        unsigned oseen = 0;
        const int poll_at = wid == 0 ? 8 : 99;
        step(frE, fnE, frO, fnO, 0, true, false);
        STAMP(3, 1);
#pragma unroll 1
        for (int st = 0; st < 14; st += 2) {
            step(frO, fnO, frE, fnE, st + 1, true, true);
            if (st == 2) wait_vm<0>();
            if (st == 2) STAMP(3, 7);
            if (st == poll_at) poll1(oseen, ocnt);
            step(frE, fnE, frO, fnO, st + 2, true, true);
        }
        STAMP(3, 4);
        if (wid == 0) {
            asm volatile("s_waitcnt vmcnt(0)" : "+v"(oseen)::"memory");
            wait_count(ocnt, oseen, 32u);
        }
        step(frO, fnO, frE, fnE, 15, true, true);
        STAMP(3, 5);
        step(frE, fnE, frO, fnO, 16, false, true);
        STAMP(3, 2);
        const unsigned yp = WS_YP + (unsigned)(((tb * 4 + eg) * 4 + wid) * 4 + sp) * 10240u + lane * 16;
#pragma unroll
        for (int u = 0; u < 2; ++u)
#pragma unroll
            for (int t = 0; t < 5; ++t) {
                const f32x4 v0 = acc[2 * u][t], v1 = acc[2 * u + 1][t];
                st128(wr, yp, (u * 5 + t) * 1024, u32x4{pk2(v0[0], v0[1]), pk2(v0[2], v0[3]), pk2(v1[0], v1[1]), pk2(v1[2], v1[3])});
            }
        SBAR();
        STAMP(3, 3);
        const unsigned oa = WS_OA + (unsigned)((sb * 2 + rh) * NH + kk * 4) * 1024u + q * 512 + c * 16;
        const unsigned ob = WS_OB + (unsigned)((sb * 2 + rh) * NH + kk * 16) * 256u + q * 512 + c * 8;
#pragma unroll
        for (int j = 0; j < 4; ++j)
#pragma unroll
            for (int k = 0; k < 2; ++k)
#pragma unroll
                for (int u = 0; u < 2; ++u) fo[j][k][u] = as_frag(ld128c(wr, oa + j * 8192 + k * 2048 + u * 256));
#pragma unroll
        for (int k = 0; k < 2; ++k)
#pragma unroll
            for (int u = 0; u < 2; ++u) {
                const u32x2 a = ld64c(wr, ob + k * 2048 + u * 128), bq = ld64c(wr, ob + k * 2048 + u * 128 + 256);
                fo[4][k][u] = as_frag(u32x4{a[0], a[1], bq[0], bq[1]});
            }
        SBAR();
        STAMP(3, 6);
        wait_vm<24>();
        STAMP(2, 5);
        if (lane == 0) __hip_atomic_fetch_add((gu32*)(ws + WS_FLAG + ((tb * 4 + eg) * 4 + wid) * 128), 1u, RLX_AGENT);
    }
    const int s0 = sb * 64 + rh * 32 + c;
    f32x4 acc[2][5];
#pragma unroll
    for (int t = 0; t < 5; ++t) { acc[0][t] = f32x4{}; acc[1][t] = f32x4{}; }
    bar();
    STAMP(2, 0);
    {
        const lptr p0 = lbase(lds0 + kk * 2048 + lane * 16), p1 = lbase(lds0 + kk * 2048 + lane * 16 + 61440);
        bf16x8 fn[2][5];
#define WOF(n, t) lds128(((t) < 3 ? p0 + (t) * 20480 : p1 + ((t) - 3) * 20480) + ((n) >> 1) * 4096 + ((n) & 1) * 1024)
#pragma unroll
        for (int t = 0; t < 5; ++t) fn[0][t] = WOF(0, t);
#pragma unroll
        for (int n = 0; n < 10; ++n)
#pragma unroll
            for (int t = 0; t < 5; ++t) {
                if (n < 9) fn[(n + 1) & 1][t] = WOF(n + 1, t);
                SBAR();
                acc[0][t] = MFMA16(fn[n & 1][t], fo[n >> 1][n & 1][0], acc[0][t]);
                acc[1][t] = MFMA16(fn[n & 1][t], fo[n >> 1][n & 1][1], acc[1][t]);
                SBAR();
            }
    }
    STAMP(2, 1);
    gu32* ycnt = (gu32*)(ws + WS_FLAG + ((tb * 4 + eg) * 4 + sp) * 128);
    unsigned yseen = 0;
    poll1(yseen, ycnt);
    const float g0 = ((const float*)(ws + WS_G))[s0], g1 = ((const float*)(ws + WS_G))[s0 + 16];
    const int t0 = kk * 3;
    f32x4 fin[2][3];
    {
        LAS f32x4* xw = (LAS f32x4*)(uintptr_t)(lds0 + 3 * F_STG + wid * 6144 + lane * 16);
        const LAS f32x4* xr = (const LAS f32x4*)(uintptr_t)(lds0 + 3 * F_STG + (wid ^ 2) * 6144 + lane * 16);
        unsigned mk = 0u - (unsigned)kk;
        PIN(mk);
#pragma unroll
        for (int u = 0; u < 2; ++u)
#pragma unroll
            for (int t = 0; t < 3; ++t) {
                const f32x4 lo = acc[u][t], hi = acc[u][t < 2 ? 3 + t : 4];
                f32x4 snd;
#pragma unroll
                for (int e = 0; e < 4; ++e) { fin[u][t][e] = bsel(mk, hi[e], lo[e]); snd[e] = bsel(mk, lo[e], hi[e]); }
                xw[(u * 3 + t) * 64] = snd;
            }
        bar();
#pragma unroll
        for (int u = 0; u < 2; ++u)
#pragma unroll
            for (int t = 0; t < 3; ++t) fin[u][t] = (fin[u][t] + xr[(u * 3 + t) * 64]) * (u ? g1 : g0);
    }
    asm volatile("s_waitcnt vmcnt(0)" : "+v"(yseen)::"memory");
    wait_count(ycnt, yseen, 4u);
    STAMP(2, 2);
    u32x4 yv[4][3];
    const unsigned yb = WS_YP + (unsigned)(((tb * 4 + eg) * 4 + sp) * 4) * 10240u + (unsigned)(rh * 5 + t0) * 1024u + lane * 16;
#pragma unroll
    for (int t = 0; t < 3; ++t)
#pragma unroll
        for (int v = 0; v < 4; ++v) yv[v][t] = ld128c(wr, yb + v * 10240 + (t0 + t < 5 ? t : 0) * 1024);
    u16* op = out + (size_t)s0 * DM + ng * 80 + t0 * 16 + q * 4;
#pragma unroll
    for (int t = 0; t < 3; ++t) {
        if (t0 + t < 5) {
#pragma unroll
            for (int u = 0; u < 2; ++u) {
                const float gm = 1.0f - (u ? g1 : g0);
                const float y0 = (bflo(yv[0][t][2 * u]) + bflo(yv[1][t][2 * u])) + (bflo(yv[2][t][2 * u]) + bflo(yv[3][t][2 * u]));
                const float y1 = (bfhi(yv[0][t][2 * u]) + bfhi(yv[1][t][2 * u])) + (bfhi(yv[2][t][2 * u]) + bfhi(yv[3][t][2 * u]));
                const float y2 = (bflo(yv[0][t][2 * u + 1]) + bflo(yv[1][t][2 * u + 1])) + (bflo(yv[2][t][2 * u + 1]) + bflo(yv[3][t][2 * u + 1]));
                const float y3 = (bfhi(yv[0][t][2 * u + 1]) + bfhi(yv[1][t][2 * u + 1])) + (bfhi(yv[2][t][2 * u + 1]) + bfhi(yv[3][t][2 * u + 1]));
                const f32x4 a = fin[u][t];
                *(u32x2*)(op + (size_t)u * 16 * DM + t * 16) = u32x2{pk2(a[0] + gm * y0, a[1] + gm * y1), pk2(a[2] + gm * y2, a[3] + gm * y3)};
            }
        }
    }
    STAMP_END(2);
}

constexpr int LDS_B = LDS_SCR + 256;

__global__ __launch_bounds__(512) void k_proj(const u16* __restrict__ x, const u16* __restrict__ wqkv, const u16* __restrict__ cd,
                                              const u16* __restrict__ sd, const u16* __restrict__ wo, const u16* __restrict__ gw,
                                              char* __restrict__ ws) {
    extern __shared__ __attribute__((aligned(16))) char smem[];
    const unsigned lds0 = (unsigned)(uintptr_t)smem;
    const int wid = __builtin_amdgcn_readfirstlane(threadIdx.x >> 6);
    const int b = blockIdx.x, xcd = b & 7, i = b >> 3;
    const int rb = (xcd & 1) * 8 + (i & 7), cq = xcd >> 1, gl = i >> 3;
    if (wid >= 4) {
        Side S;
        if (gl == 3) proj_load<false>(x, wqkv, cd, sd, wo, gw, S, rb, cq, lds0, wid - 4);
        else proj_load<true>(x, wqkv, cd, sd, wo, gw, S, rb, cq * 3 + gl, lds0, wid - 4);
        side_finish(S, ws, b * 4 + wid - 4);
        return;
    }
    {
        const char* pc; asm volatile("s_getpc_b64 %0" : "=s"(pc));
        pc = (const char*)((uintptr_t)pc & ~(uintptr_t)127);
        touch(pc, min(threadIdx.x, K1LINES) * 128, lds0 + F_RING);
    }
    STAMP(0, 0);
    if (b == 255) {
        const unsigned tl = threadIdx.x;
        st128(ws_rsrc(ws), (tl * VP + 640) * 16, WS_VV, u32x4{ONES, ONES, ONES, ONES});
        st32(ws_rsrc(ws), tl * 128, WS_FLAG, 0u);
        if (tl < 8) st32(ws_rsrc(ws), (256 + tl) * 128, WS_FLAG, 0u);
    }
    bar_raw();
    STAMP(0, 1);
    if (wid == 0) touch((const char*)wo + b * 3200, min(threadIdx.x * 128u, 3196u), lds0 + F_RING);
    if (gl == 3) proj_mma<2>(ws, rb, cq, lds0, wid);
    else if (cq * 3 + gl < 8) proj_mma<0>(ws, rb, cq * 3 + gl, lds0, wid);
    else proj_mma<1>(ws, rb, cq * 3 + gl, lds0, wid);
    wait_vm<0>();
    STAMP(0, 4);
    STAMP_FLUSH(0);
}

__global__ __launch_bounds__(512) void k_mix(const u16* __restrict__ x, const u16* __restrict__ wo, const u16* __restrict__ gw,
                                             const u16* __restrict__ cs, const u16* __restrict__ ss, char* __restrict__ ws, u16* __restrict__ out) {
    extern __shared__ __attribute__((aligned(16))) char smem[];
    const int wid = __builtin_amdgcn_readfirstlane(threadIdx.x >> 6);
    if (wid >= 6) {
        const char* pc; asm volatile("s_getpc_b64 %0" : "=s"(pc));
        pc = (const char*)((uintptr_t)pc & ~(uintptr_t)127);
        touch(pc, min(threadIdx.x - 384, K2LINES) * 128, (unsigned)(uintptr_t)smem + LDS_SCR);
        touch(pc, min(threadIdx.x - 256, K2LINES) * 128, (unsigned)(uintptr_t)smem + LDS_SCR);
    }
    if (wid < 4) phase_attn(ws, (unsigned)(uintptr_t)smem, wid);
    else if (wid < 6) attn_produce(ws, (unsigned)(uintptr_t)smem, wid - 4);
    else attn_helper(cs, ss, ws, (unsigned)(uintptr_t)smem, wid - 6);
    phase_tail(cs, ss, ws, out, (unsigned)(uintptr_t)smem, wid);
    STAMP_FLUSH(1);
}

extern "C" void kernel_launch(void* const* d_in, const int* in_sizes, int n_in, void* d_out, int out_size, void* d_ws, size_t ws_size,
                              hipStream_t stream) {
    const u16* x = (const u16*)d_in[0];
    const u16* wqkv = (const u16*)d_in[1];
    const u16* wo = (const u16*)d_in[2];
    const u16* cs = (const u16*)d_in[4];
    const u16* ss = (const u16*)d_in[5];
    const u16* cd = (const u16*)d_in[6];
    const u16* sd = (const u16*)d_in[7];
    const u16* gw = (const u16*)d_in[8];
    char* ws = (char*)d_ws;
    (void)hipFuncSetAttribute((const void*)k_proj, hipFuncAttributeMaxDynamicSharedMemorySize, LDS_B);
    (void)hipFuncSetAttribute((const void*)k_mix, hipFuncAttributeMaxDynamicSharedMemorySize, LDS_B);
    k_proj<<<256, 512, LDS_B, stream>>>(x, wqkv, cd, sd, wo, gw, ws);
    k_mix<<<256, 512, LDS_B, stream>>>(x, wo, gw, cs, ss, ws, (u16*)d_out);
}
```

```cpp
#define SIGFIRST
#include <hip/hip_runtime.h>
#include <hip/hip_bf16.h>
#include <stdint.h>

typedef unsigned short u16;
typedef __attribute__((ext_vector_type(8))) __bf16 bf16x8;
typedef __attribute__((ext_vector_type(4))) short s16x4;
typedef __attribute__((ext_vector_type(16))) float f32x16;
typedef __attribute__((ext_vector_type(4))) float f32x4;
typedef __attribute__((ext_vector_type(4))) unsigned u32x4;
typedef __attribute__((ext_vector_type(2))) unsigned u32x2;
#define LAS __attribute__((address_space(3)))
#define DI __device__ __forceinline__
typedef const LAS char* lptr;

constexpr int SEQ = 2048, DM = 640, NH = 32;
constexpr int VP = 648;
constexpr unsigned WS_QA = 0;
constexpr unsigned WS_KA = WS_QA + NH * 64 * 1024;
constexpr unsigned WS_QB = WS_KA + NH * 64 * 1024;
constexpr unsigned WS_KB = WS_QB + NH * 64 * 512;
constexpr unsigned WS_VV = WS_KB + NH * 64 * 512;
constexpr unsigned WS_AB = WS_VV + 64 * 4 * VP * 16;
constexpr unsigned WS_YP = WS_AB + 40 * 128 * 1024;
constexpr unsigned WS_OA = WS_YP + 4 * 64 * 40 * 1024;
constexpr unsigned WS_OB = WS_OA + 64 * NH * 1024;
constexpr unsigned WS_FLAG = WS_OB + 64 * NH * 256;
constexpr unsigned WS_END = WS_FLAG + 264 * 128;
constexpr float QSCALE = 0.22360679774997896f * 1.4426950408889634f;
#define ST_AUX 16

#define STAMP(k, i) do {} while (0)
#define STAMP_END(k) do {} while (0)
#define STAMP_FLUSH(kk) do {} while (0)
#define MFMA32(a, b, c) __builtin_amdgcn_mfma_f32_32x32x16_bf16((a), (b), (c), 0, 0, 0)
#define MFMA16(a, b, c) __builtin_amdgcn_mfma_f32_16x16x32_bf16((a), (b), (c), 0, 0, 0)
#define SBAR() __builtin_amdgcn_sched_barrier(0)
#define PIN(x) asm volatile("" : "+v"(x))
#define KEEP(x) asm volatile("" ::"v"(x))

typedef __attribute__((ext_vector_type(2))) __bf16 bf16x2;
typedef __attribute__((ext_vector_type(2))) float f32x2;
DI unsigned pk2(float lo, float hi) {
    f32x2 v = {lo, hi};
    return __builtin_bit_cast(unsigned, __builtin_convertvector(v, bf16x2));
}
DI float bsel(unsigned m, float a, float b) { return __uint_as_float((__float_as_uint(a) & m) | (__float_as_uint(b) & ~m)); }
DI float bflo(unsigned v) { return __uint_as_float(v << 16); }
DI float bfhi(unsigned v) { return __uint_as_float(v & 0xFFFF0000u); }
DI void dma16(const void* sbase, unsigned voff, unsigned lds) {
    asm volatile("s_mov_b32 m0, %2\n\ts_nop 0\n\tglobal_load_lds_dwordx4 %0, %1" ::"v"(voff), "s"(sbase), "s"(lds) : "memory");
}
template <int N> DI void dma16_lo(const void* sbase, unsigned voff, unsigned lds) {
    unsigned long long sv;
    asm volatile("s_mov_b32 m0, %3\n\ts_mov_b64 %0, exec\n\ts_mov_b64 exec, %4\n\tglobal_load_lds_dwordx4 %1, %2\n\ts_mov_b64 exec, %0"
                 : "=&s"(sv) : "v"(voff), "s"(sbase), "s"(lds), "s"((1ull << N) - 1) : "memory");
}
DI void touch(const void* sbase, unsigned voff, unsigned lds) {
    asm volatile("s_mov_b32 m0, %2\n\ts_nop 0\n\tglobal_load_lds_dword %0, %1" ::"v"(voff), "s"(sbase), "s"(lds) : "memory");
}
template <int N> DI void wait_vm() { asm volatile("s_waitcnt vmcnt(%0)" ::"i"(N) : "memory"); }
DI void bar() { asm volatile("s_waitcnt lgkmcnt(0)\n\ts_barrier" ::: "memory"); }
DI void bar_raw() { asm volatile("s_barrier" ::: "memory"); }
DI lptr lbase(unsigned a) { asm volatile("" : "+v"(a)); return (lptr)(uintptr_t)a; }
DI bf16x8 lds128(lptr p) { return *(const LAS bf16x8*)p; }
DI u32x2 lds64(lptr p) { return *(const LAS u32x2*)p; }
DI s16x4 ldstr(lptr p) {
    typedef short v4i16_t __attribute__((ext_vector_type(4)));
    return __builtin_bit_cast(s16x4, __builtin_amdgcn_ds_read_tr16_b64_v4i16((LAS v4i16_t*)p));
}
DI u32x4 pack8(const f32x16& x, int s, float sc) {
    u32x4 p;
    p[0] = pk2(x[8 * s + 0] * sc, x[8 * s + 1] * sc);
    p[1] = pk2(x[8 * s + 2] * sc, x[8 * s + 3] * sc);
    p[2] = pk2(x[8 * s + 4] * sc, x[8 * s + 5] * sc);
    p[3] = pk2(x[8 * s + 6] * sc, x[8 * s + 7] * sc);
    return p;
}
typedef __amdgpu_buffer_rsrc_t rsrc_t;
DI rsrc_t ws_rsrc(const void* ws) { return __builtin_amdgcn_make_buffer_rsrc((void*)ws, (short)0, (int)WS_END, 0x00020000); }
#define K1_AUX 16
DI void st128p(rsrc_t r, unsigned voff, unsigned soff, u32x4 v) { __builtin_amdgcn_raw_buffer_store_b128(v, r, voff + soff, 0, K1_AUX); }
DI void st128(rsrc_t r, unsigned voff, unsigned soff, u32x4 v) { __builtin_amdgcn_raw_buffer_store_b128(v, r, voff + soff, 0, ST_AUX); }
DI void st64(rsrc_t r, unsigned voff, unsigned soff, u32x2 v) { __builtin_amdgcn_raw_buffer_store_b64(v, r, voff, soff, ST_AUX); }
DI void st32(rsrc_t r, unsigned voff, unsigned soff, unsigned v) { __builtin_amdgcn_raw_buffer_store_b32(v, r, voff, soff, ST_AUX); }
#define LD_AUX 0
DI u32x4 ld128(rsrc_t r, unsigned voff) { return __builtin_amdgcn_raw_buffer_load_b128(r, voff, 0, LD_AUX); }
DI bf16x8 as_frag(u32x4 v) { return __builtin_bit_cast(bf16x8, v); }
typedef __attribute__((ext_vector_type(16))) __bf16 bf16x16;
template <int HI> DI f32x16 pv_mma(bf16x8 v, const f32x16& p, const f32x16& o, int idx) {
    const auto ph = __builtin_shufflevector(p, p, 8 * HI, 8 * HI + 1, 8 * HI + 2, 8 * HI + 3, 8 * HI + 4, 8 * HI + 5, 8 * HI + 6, 8 * HI + 7);
    return __builtin_amdgcn_smfmac_f32_32x32x32_bf16(v, __builtin_bit_cast(bf16x16, ph), o, idx, 0, 0);
}
DI u32x4 pack8n(const f32x16& x, int s, float sc) {
    const u32x4 p = pack8(x, s, sc);
    const auto r0 = __builtin_amdgcn_permlane32_swap(p[0], p[2], false, false);
    const auto r1 = __builtin_amdgcn_permlane32_swap(p[1], p[3], false, false);
    return u32x4{r0[0], r1[0], r0[1], r1[1]};
}

constexpr int P_STG = 45056;
constexpr int P_RING = 3 * P_STG;
#define EB0 1

DI void store_ab(const f32x16& acc, int dt, int stile, rsrc_t ws, int lane) {
    const float sc = dt >= 20 ? -1.f : 1.f;
    const int nt = dt >= 20 ? dt - 20 : dt;
    const int ks = (dt >= 20 ? 64 : 0) + stile;
    const unsigned dst = WS_AB + (unsigned)((nt * 2 + ((lane >> 4) & 1)) * 128 + ks) * 1024u + (unsigned)((lane >> 5) * 256 + (lane & 15) * 16);
#pragma unroll
    for (int s = 0; s < 2; ++s) st128p(ws, dst, s * 512, pack8n(acc, s, sc));
}

DI void proj_load(const u16* __restrict__ x, const u16* __restrict__ wqkv, const u16* __restrict__ cd, const u16* __restrict__ sd,
                  int rb, int cq, int gl, unsigned lds0, int wid) {
    const int lane = threadIdx.x & 63;
    constexpr int NDMA = 10;
    const bool pa = gl != 3;
    const unsigned vrow = (unsigned)(lane >> 3) * 1280u + (unsigned)(((lane & 7) ^ ((4 * (wid & 1) + (lane >> 4)) & 7)) * 16);
    const char* xb = (const char*)x + ((size_t)rb * 128 + wid * 8) * 1280;
    int st = 3 * ((rb + gl) % 3);
    unsigned slot = lds0 + wid * 1024;
#pragma unroll
    for (int j = 0; j < 4; ++j) dma16(xb + j * 40960 + st * 128, vrow, slot + j * 4096);
    const char* wb[7];
    unsigned vo[5];
    const int dt0 = pa ? 28 + cq * 3 + gl : 7 * cq;
#pragma unroll
    for (int j = 0; j < 7; ++j) {
        const int dt = dt0 + j;
        const char* d = (const char*)(dt >= 20 ? sd : cd) + ((dt >= 20 ? dt - 20 : dt) * 32 + wid * 8) * 1280;
        wb[j] = pa && j ? (const char*)wqkv + (cq * 3 + gl) * 320 : d;
    }
#pragma unroll
    for (int j = 0; j < 5; ++j) { const unsigned i = (wid + 4 * j) * 64 + lane, q = (i * 3277u) >> 16; vo[j] = pa ? q * 3840u + (i - q * 20u) * 16u : vrow; }
    const int wstep = pa ? 64 * 3840 : 128;
    auto issue = [&](bool with_x) {
        if (with_x) {
#pragma unroll
            for (int j = 0; j < 4; ++j) dma16(xb + j * 40960 + st * 128, vrow, slot + j * 4096);
        }
        if (!pa) dma16(wb[6] + st * 128, vrow, slot + 40960);
        dma16(wb[0] + st * 128, vrow, slot + 16384);
#pragma unroll
        for (int j = 0; j < 5; ++j) dma16(wb[1 + j] + st * wstep, vo[j], slot + 20480 + j * 4096);
        slot = slot + P_STG == lds0 + wid * 1024 + P_RING ? lds0 + wid * 1024 : slot + P_STG;
        st = st == 9 ? 0 : st + 1;
    };
    STAMP(0, 5);
#pragma unroll 1
    for (int n = 0; n < 3; ++n) {
        issue(n > 0);
        if (n == 0) STAMP(0, 6);
        if (n == EB0) { wait_vm<EB0 * NDMA>(); bar_raw(); }
    }
    STAMP(0, 7);
    wait_vm<NDMA>(); bar_raw(); bar_raw();
#pragma unroll 1
    for (int n = 3; n < 10; ++n) { issue(true); wait_vm<NDMA>(); bar_raw(); bar_raw(); }
    wait_vm<0>();
    bar_raw();
    bar_raw(); bar_raw(); bar_raw();
}

template <int PATH>
DI void proj_mma(char* __restrict__ ws, int rb, int grp, unsigned lds0, int wid ) {
    const int lane = threadIdx.x & 63, r = lane & 31, h = lane >> 5;
    constexpr bool PATH_A = PATH < 2;
    constexpr int NW = PATH_A ? 5 : 0, ND = PATH_A ? 1 : 7, NT = NW + ND;
    const int dt0 = PATH_A ? 28 + grp : 7 * grp;
    const unsigned sw = lds0 + (unsigned)r * 128u + (unsigned)((h ^ ((r >> 1) & 7)) * 16);
    const unsigned awo = lds0 + 20480u + (unsigned)(8 * h + ((lane & 15) >> 2)) * 320u + (unsigned)(((lane >> 4) & 1) * 32 + (lane & 3) * 8);
    f32x16 acc[NT];
#pragma unroll
    for (int t = 0; t < NT; ++t) acc[t] = f32x16{};
    bf16x8 fx[2], fw[2][NT];
    lptr pr, px, pw;
    auto setp = [&](unsigned so, int ks) {
        pr = lbase((sw ^ (unsigned)(ks * 32)) + so);
        px = lbase((sw ^ (unsigned)(ks * 32)) + so + wid * 4096);
        if (PATH_A && ks == 0) pw = lbase(awo + so);
    };
    auto rd = [&](int ks, int b, int t) {
        if (t == 0) fx[b] = lds128(px);
        if (t < ND) fw[b][t] = lds128(pr + 16384 + t * 4096);
        else {
            s16x4 lo = ldstr(pw + ks * 16 * 320 + (t - ND) * 64), hi = ldstr(pw + ks * 16 * 320 + 4 * 320 + (t - ND) * 64);
            fw[b][t] = __builtin_bit_cast(bf16x8, __builtin_shufflevector(lo, hi, 0, 1, 2, 3, 4, 5, 6, 7));
        }
    };
    auto mm = [&](int b, int t) {
        if (PATH == 0 && t >= ND) acc[t] = MFMA32(fw[b][t], fx[b], acc[t]);
        else acc[t] = MFMA32(fx[b], fw[b][t], acc[t]);
    };
    unsigned so = 0;
    setp(0, 0);
#pragma unroll
    for (int t = 0; t < NT; ++t) PIN(acc[t]);
    bar_raw();
    STAMP(0, 1);
#pragma unroll
    for (int t = 0; t < NT; ++t) rd(0, 0, t);
    SBAR();
#pragma unroll 1
    for (int st = 0; st < 10; ++st) {
#pragma unroll
        for (int ks = 0; ks < 4; ++ks) {
            if (ks == 3) { bar_raw(); so = so == 2 * P_STG ? 0 : so + P_STG; }
            setp(so, (ks + 1) & 3);
#pragma unroll
            for (int t = 0; t < NT; ++t) { rd((ks + 1) & 3, (ks + 1) & 1, t); SBAR(); mm(ks & 1, t); SBAR(); }
        }
        bar_raw();
    }
    STAMP(0, 2);
    const rsrc_t wr = ws_rsrc(ws);
    const int stile = rb * 4 + wid;
    if (PATH == 0) {
        const float sc = grp < 4 ? QSCALE : 1.f;
        const unsigned t0 = (unsigned)(grp * 8 * 64 + stile);
        const unsigned va = WS_QA + t0 * 1024 + lane * 16, vb = WS_QB + (t0 + h * 64) * 512 + r * 16;
#pragma unroll
        for (int m = 0; m < 4; ++m) {
            u32x2 q[5];
#pragma unroll
            for (int i = 0; i < 5; ++i) {
                const int p = 5 * m + i, t = ND + (p >> 2), g = p & 3;
                q[i][0] = pk2(acc[t][4 * g] * sc, acc[t][4 * g + 1] * sc);
                q[i][1] = pk2(acc[t][4 * g + 2] * sc, acc[t][4 * g + 3] * sc);
            }
            st128p(wr, va, (2 * m) * 64 * 1024, u32x4{q[0][0], q[0][1], q[1][0], q[1][1]});
            st128p(wr, va, (2 * m + 1) * 64 * 1024, u32x4{q[3][0], q[3][1], q[4][0], q[4][1]});
            st128p(wr, vb, (2 * m) * 64 * 512, u32x4{q[2][0], q[2][1], 0x00003F80u, 0u});
        }
    } else if (PATH == 1) {
        const unsigned vv = WS_VV + (unsigned)((stile * 4 + h) * VP + (grp - 8) * 160 + r) * 16;
#pragma unroll
        for (int t = 0; t < 5; ++t) {
            st128p(wr, vv, t * 512, pack8n(acc[ND + t], 0, 1.f));
            st128p(wr, vv, t * 512 + 2 * VP * 16, pack8n(acc[ND + t], 1, 1.f));
        }
    }
#pragma unroll
    for (int t = 0; t < ND; ++t) store_ab(acc[t], dt0 + t, stile, wr, lane);
    STAMP(0, 3);
}
#define YG_MAP const int tb = (xcd >> 2) * 8 + (i & 7), eg = i >> 3, sp = xcd & 3
constexpr int F_STG = 36864;
constexpr int F_RING = 4 * F_STG;
DI void yg_issue(const u16* __restrict__ cs, const u16* __restrict__ ss, const char* __restrict__ ws, unsigned lds0, int lw, int st) {
    const int lane = threadIdx.x & 63;
    const int b = blockIdx.x, xcd = b & 7, i = b >> 3;
    YG_MAP;
    const unsigned vrow = (unsigned)(lane >> 3) * 4096u + (unsigned)(((lane & 7) ^ ((4 * (lw & 1) + (lane >> 4)) & 7)) * 16);
    const char* rp = (const char*)(sp >= 2 ? ss : cs) + (size_t)(tb * 128 + lw * 8) * 4096 + (sp & 1) * 2048;
    const char* ab = ws + WS_AB + (size_t)((eg * 10 + (lw >> 1)) * 128 + sp * 32 + (lw & 1)) * 1024;
    const unsigned d = lds0 + lw * 1024 + (st & 3) * F_STG;
#pragma unroll
    for (int j = 0; j < 4; ++j) dma16(rp + j * 32 * 4096 + st * 128, vrow, d + j * 4096);
#pragma unroll
    for (int j = 0; j < 5; ++j) dma16(ab + (j * 256 + st * 2) * 1024, lane * 16, d + 16384 + j * 4096);
}

DI float xhalf_max(float v) {
    auto rr = __builtin_amdgcn_permlane32_swap(__float_as_uint(v), __float_as_uint(v), false, false);
    return fmaxf(__uint_as_float(rr[0]), __uint_as_float(rr[1]));
}
DI float max16(const f32x16& s) {
    float a = fmaxf(fmaxf(s[0], s[1]), s[2]), b = fmaxf(fmaxf(s[3], s[4]), s[5]);
    a = fmaxf(fmaxf(a, s[6]), s[7]); b = fmaxf(fmaxf(b, s[8]), s[9]);
    a = fmaxf(fmaxf(a, s[10]), s[11]); b = fmaxf(fmaxf(b, s[12]), s[13]);
    a = fmaxf(fmaxf(a, s[14]), s[15]);
    return fmaxf(a, b);
}
#define EX2(v) __builtin_amdgcn_exp2f(v)
constexpr unsigned VV_T = 4 * VP * 16, VV_S = 2 * VP * 16;
constexpr unsigned ONES = 0x3F803F80u;

DI u32x4 fix_v(u32x4 v, int r) {
#pragma unroll
    for (int i = 0; i < 4; ++i) v[i] = r < 20 ? v[i] : (r == 20 ? ONES : 0u);
    return v;
}
DI f32x16 attn_safe(rsrc_t wr, int head, u32x4 qa, u32x4 qb, int lane) {
    const int r = lane & 31, h = lane >> 5;
    qb[2] = 0u;
    f32x16 o = f32x16{};
    float m = -INFINITY;
#pragma unroll 1
    for (int t = 0; t < 64; ++t) {
        const u32x4 ka = ld128(wr, WS_KA + (unsigned)(head * 64 + t) * 1024 + lane * 16);
        const u32x4 kb = ld128(wr, WS_KB + (unsigned)(head * 64 + t) * 512 + r * 16);
        const unsigned vp = WS_VV + (unsigned)((t * 4 + h) * VP + head * 20 + (r < 20 ? r : 0)) * 16;
        const u32x4 v0 = fix_v(ld128(wr, vp), r), v1 = fix_v(ld128(wr, vp + VV_S), r);
        f32x16 s = MFMA32(as_frag(ka), as_frag(qa), f32x16{});
        s = MFMA32(as_frag(kb), as_frag(qb), s);
        const float mn = fmaxf(m, xhalf_max(max16(s)));
        const float al = EX2(m - mn);
        m = mn;
#pragma unroll
        for (int i = 0; i < 16; ++i) { o[i] *= al; s[i] = EX2(s[i] - mn); }
        o = pv_mma<0>(as_frag(v0), s, o, (int)0xDDDDDDDD);
        o = pv_mma<1>(as_frag(v1), s, o, (int)0xDDDDDDDD);
    }
    return o;
}

#define H_PACE 8
#define H_START 2
DI void attn_finish(f32x16 o, rsrc_t wr, int head, int qt) {
    const int lane = threadIdx.x & 63, r = lane & 31, h = lane >> 5;
    auto rl = __builtin_amdgcn_permlane32_swap(__float_as_uint(o[8]), __float_as_uint(o[8]), false, false);
    float l = __uint_as_float(rl[1]);
    float z = 0.f;
#pragma unroll
    for (int k = 0; k < 12; ++k) z = __builtin_fmaf(o[k], 0.f, z);
    const bool ok = (z == 0.f) && (l >= 8.6736174e-19f) && (l <= 1.2676506e30f);
    if (__builtin_expect(!__all(ok), 0)) {
        const u32x4 q0 = ld128(wr, WS_QA + (unsigned)(head * 64 + qt) * 1024 + lane * 16);
        const u32x4 q1 = ld128(wr, h ? 0x80000000u : WS_QB + (unsigned)(head * 64 + qt) * 512 + r * 16);
        o = attn_safe(wr, head, q0, q1, lane);
        rl = __builtin_amdgcn_permlane32_swap(__float_as_uint(o[8]), __float_as_uint(o[8]), false, false);
        l = __uint_as_float(rl[1]);
    }
    const float inv = __builtin_amdgcn_rcpf(l);
    const unsigned ot = (unsigned)(qt * NH + head);
    st128(wr, lane * 16, WS_OA + ot * 1024, pack8(o, 0, inv));
    if (h == 0) st64(wr, r * 8, WS_OB + ot * 256, u32x2{pk2(o[8] * inv, o[9] * inv), pk2(o[10] * inv, o[11] * inv)});
}

#define HPRIO 3
#define KVSLEEP 8
constexpr int KVT_B = 2880, KVG_B = 4 * KVT_B, KV1 = 3 * F_STG;
constexpr int KV_FULL = KV1 + 4 * KVG_B;
constexpr int KV_FREE = KV_FULL + 16;
constexpr int LDS_G = KV_FREE + 16;
constexpr int LDS_SCR = LDS_G + 256;
DI unsigned kv_off(unsigned g) { return g < 9 ? g * KVG_B : KV1 + ((g - 9) & 3) * KVG_B; }
DI void kv_wait_free(unsigned lds0, unsigned n) {
    for (;;) {
        const u32x4 f = *(const volatile LAS u32x4*)(uintptr_t)(lds0 + KV_FREE);
        if (__builtin_amdgcn_readfirstlane(min(min(f[0], f[1]), min(f[2], f[3]))) >= n) break;
        __builtin_amdgcn_s_sleep(KVSLEEP);
    }
    asm volatile("" ::: "memory");
}
DI void lds_put(unsigned addr, unsigned v) { *(volatile LAS unsigned*)(uintptr_t)addr = v; }

DI void phase_attn(char* __restrict__ ws, unsigned lds0, int wid) {
    const int lane = threadIdx.x & 63, r = lane & 31, h = lane >> 5;
    const int b = blockIdx.x, head = (b & 7) * 4 + (b >> 6), qt0 = ((b >> 3) & 7) * 8 + wid * 2;
    const rsrc_t wr = ws_rsrc(ws);
    STAMP(1, 0);
    u32x4 qa[2], qbv[2];
#pragma unroll
    for (int i = 0; i < 2; ++i) {
        qa[i] = ld128(wr, WS_QA + (unsigned)(head * 64 + qt0 + i) * 1024 + lane * 16);
        qbv[i] = ld128(wr, h ? 0x80000000u : WS_QB + (unsigned)(head * 64 + qt0 + i) * 512 + r * 16);
    }
    const int rv = r <= 20 ? r : (r < 28 ? 20 : 16);
    const unsigned oka = lds0 + lane * 16, ov = lds0 + 1536u + (unsigned)(h * 21 + rv) * 16u;
    unsigned afull = lds0 + KV_FULL, afree = lds0 + KV_FREE + wid * 4;
    PIN(afull); PIN(afree);
    __builtin_amdgcn_s_setprio(2);
    bar_raw();
    bf16x8 ka[2], kb[2], v0[2], v1[2];
    lptr pka = lbase(oka), pv = lbase(ov);
#define LDK(n, T) do { ka[n] = lds128(pka + (T) * KVT_B); kb[n] = lds128(pka + (T) * KVT_B + 1024); SBAR(); } while (0)
#define LDV(n, T) do { v0[n] = lds128(pv + (T) * KVT_B); v1[n] = lds128(pv + (T) * KVT_B + 42 * 16); SBAR(); } while (0)
    LDK(0, 0); LDK(1, 1); LDV(0, 0);
#pragma unroll
    for (int i = 0; i < 2; ++i) {
        qbv[i][2] = 0u;
        f32x16 t = MFMA32(ka[0], as_frag(qa[i]), f32x16{});
        t = MFMA32(kb[0], as_frag(qbv[i]), t);
        const float m0 = xhalf_max(max16(t));
        qbv[i][2] = h ? 0u : (pk2(-m0, 0.f) & 0xFFFFu);
    }
    STAMP(1, 1);
    f32x16 oA = f32x16{}, oB = f32x16{}, s0, s1, s2, s3 = f32x16{};
    int idx = (int)0xDDDDDDDD;
    PIN(idx);
    v0[1] = ka[0]; v1[1] = ka[0];
    s0 = MFMA32(ka[0], as_frag(qa[0]), f32x16{});
    s0 = MFMA32(kb[0], as_frag(qbv[0]), s0);
    SBAR();
#define GRP(MF, H, S, i) do { MF; SBAR(); H; SBAR(); S[i] = EX2(S[i]); S[i + 1] = EX2(S[i + 1]); S[i + 2] = EX2(S[i + 2]); S[i + 3] = EX2(S[i + 3]); PIN(S); SBAR(); } while (0)
#define SLOT(X, N, kn, Q, P, O, vn, H0, H1, H2, H3) do { \
        GRP(N = MFMA32(ka[kn], as_frag(qa[Q]), f32x16{}), H0, X, 0); \
        GRP(N = MFMA32(kb[kn], as_frag(qbv[Q]), N), H1, X, 4); \
        GRP(O = pv_mma<0>(v0[vn], P, O, idx), H2, X, 8); \
        GRP(O = pv_mma<1>(v1[vn], P, O, idx), H3, X, 12); } while (0)
#define RKA(n, T) ka[n] = lds128(pka + (T) * KVT_B)
#define RKB(n, T) kb[n] = lds128(pka + (T) * KVT_B + 1024)
#define RV0(n, T) v0[n] = lds128(pv + (T) * KVT_B)
#define RV1(n, T) v1[n] = lds128(pv + (T) * KVT_B + 42 * 16)
#define LGKM0 __builtin_amdgcn_s_waitcnt(0xC07F)
#define NOP (void)0
    unsigned nx = KVG_B, gv = 0, need = 2;
    lptr pvn;
#define SPIN(x) asm volatile("" : "+s"(x))
#define GROUP(WRAP) do { \
        u32x2 full; unsigned fm, sfull; \
        SLOT(s0, s1, 0, 1, s3, oB, 1, LGKM0, RKA(0, 2), RKB(0, 2), RV0(1, 1)); \
        SLOT(s1, s2, 1, 0, s0, oA, 0, RV1(1, 1), full = *(const volatile LAS u32x2*)(uintptr_t)afull, do { gv += 1; PIN(gv); } while (0), NOP); \
        SLOT(s2, s3, 1, 1, s1, oB, 0, LGKM0, RKA(1, 3), RKB(1, 3), RV0(0, 2)); \
        SLOT(s3, s0, 0, 0, s2, oA, 1, RV1(0, 2), do { fm = min(full[0], full[1]); PIN(fm); } while (0), do { sfull = __builtin_amdgcn_readfirstlane(fm); SPIN(sfull); } while (0), pka = lbase(oka + nx)); \
          \
        SLOT(s0, s1, 0, 1, s3, oB, 1, \
             do { if (__builtin_expect(sfull < need, 0)) { \
                      do { __builtin_amdgcn_s_sleep(1); full = *(const volatile LAS u32x2*)(uintptr_t)afull; } while ((unsigned)__builtin_amdgcn_readfirstlane(min(full[0], full[1])) < need); } \
                  asm volatile("" ::: "memory"); LGKM0; } while (0), \
             RKA(0, 0), RKB(0, 0), RV0(1, 3)); \
        SLOT(s1, s2, 1, 0, s0, oA, 0, RV1(1, 3), pvn = lbase(ov + nx), do { nx += KVG_B; SPIN(nx); } while (0), \
             do { if (WRAP) { nx = nx == 9 * KVG_B ? KV1 : nx; SPIN(nx); } } while (0)); \
        pv = pvn; \
        SLOT(s2, s3, 1, 1, s1, oB, 0, LGKM0, RKA(1, 1), RKB(1, 1), RV0(0, 0)); \
        SLOT(s3, s0, 0, 0, s2, oA, 1, RV1(0, 0), lds_put(afree, gv)  , do { need += 1; SPIN(need); } while (0), \
             do { if (WRAP) { nx = nx == KV1 + 4 * KVG_B ? KV1 : nx; SPIN(nx); } } while (0)); } while (0)
#pragma unroll 1
    do { GROUP(false); GROUP(true); } while (need < 18);
    oB = pv_mma<0>(v0[1], s3, oB, idx);
    oB = pv_mma<1>(v1[1], s3, oB, idx);
    __builtin_amdgcn_s_setprio(0);
    STAMP(1, 2);
    attn_finish(oA, wr, head, qt0);
    attn_finish(oB, wr, head, qt0 + 1);
    STAMP(1, 3);
}
DI void gate16(const u16* __restrict__ x, const u16* __restrict__ gw, unsigned lds0, int n) {
    const int lane = threadIdx.x & 63, c = lane & 15, q = lane >> 4, b = blockIdx.x, xcd = b & 7, i = b >> 3;
    YG_MAP;
    (void)eg;
    __builtin_amdgcn_s_setprio(3);
    const u16* xr = x + (size_t)((tb * 2 + (sp & 1)) * 64 + n * 16 + c) * DM + q * 8;
    f32x4 z = f32x4{};
#pragma unroll
    for (int ks = 0; ks < 20; ++ks) z = MFMA16(*(const bf16x8*)(gw + ks * 32 + q * 8), *(const bf16x8*)(xr + ks * 32), z);
    if (lane < 16) *(LAS float*)(uintptr_t)(lds0 + LDS_G + (n * 16 + lane) * 4) = 1.0f / (1.0f + __expf(-z[0]));
    __builtin_amdgcn_s_setprio(HPRIO);
}
DI void attn_produce(const u16* __restrict__ x, const u16* __restrict__ gw, const char* __restrict__ ws, unsigned lds0, int pw) {
    const int lane = threadIdx.x & 63;
    const int b = blockIdx.x, head = (b & 7) * 4 + (b >> 6);
    unsigned so[6], sd[6];
#pragma unroll
    for (int j = 0; j < 6; ++j) {
        const int cc = min(j * 64 + lane, 359), tl = pw * 2 + (cc >= 180), c = cc >= 180 ? cc - 180 : cc;
        const int v = c - 96, g = v / 21, d = v - g * 21;
        so[j] = c < 64 ? WS_KA + (unsigned)(head * 64 + tl) * 1024 + c * 16
              : c < 96 ? WS_KB + (unsigned)(head * 64 + tl) * 512 + (c - 64) * 16
                       : WS_VV + (unsigned)((tl * 4 + g) * VP + (d < 20 ? head * 20 + d : 640)) * 16;
        sd[j] = c < 64 ? 4096u : c < 96 ? 2048u : 4 * VV_T;
    }
    auto issue = [&](unsigned g) {
        const unsigned dst = lds0 + kv_off(g) + pw * 2 * KVT_B;
#pragma unroll
        for (int j = 0; j < 5; ++j) { dma16(ws, so[j], dst + j * 1024); so[j] += sd[j]; }
        dma16_lo<40>(ws, so[5], dst + 5 * 1024); so[5] += sd[5];
    };
    issue(0); issue(1); issue(2);
    if (pw == 0 && lane < 4) lds_put(lds0 + KV_FREE + lane * 4, 0u);
    lds_put(lds0 + KV_FULL + pw * 4, 1u);
    wait_vm<12>(); bar();
#pragma unroll 1
    for (unsigned g = 3; g < 13; ++g) { issue(g); wait_vm<12>(); lds_put(lds0 + KV_FULL + pw * 4, g - 1); }
    wait_vm<0>(); lds_put(lds0 + KV_FULL + pw * 4, 13u);
    gate16(x, gw, lds0, pw);
#pragma unroll 1
    for (unsigned g = 13; g < 16; ++g) {
        wait_vm<0>(); lds_put(lds0 + KV_FULL + pw * 4, g);
        kv_wait_free(lds0, g - 3);
        issue(g);
    }
    wait_vm<0>(); lds_put(lds0 + KV_FULL + pw * 4, 17u);
}
DI void attn_helper(const u16* __restrict__ cs, const u16* __restrict__ ss, const u16* __restrict__ wo,
                    const u16* __restrict__ x, const u16* __restrict__ gw, const char* __restrict__ ws, unsigned lds0, int hw) {
    const int lane = threadIdx.x & 63;
    const int b = blockIdx.x, xcd = b & 7, i = b >> 3;
    YG_MAP;
    bar_raw();
    gate16(x, gw, lds0, 2 + hw);
    kv_wait_free(lds0, H_START);
    const char* pfp = (const char*)(sp >= 2 ? ss : cs) + (size_t)(tb * 128 + eg * 32 + hw * 16) * 4096 + (sp & 1) * 2048;
#pragma unroll 1
    for (int n = 0; n < 16; ++n) { touch(pfp + n * 128, (lane >> 2) * 4096, lds0 + LDS_SCR); __builtin_amdgcn_s_sleep(H_PACE); }
    touch((const char*)wo + ((tb & 7) * 80 + hw * 40) * 1280 + (eg * 2 + (sp >> 1)) * 160, min(lane >> 1, 39) * 1280 + (lane & 1) * 156, lds0 + LDS_SCR);
    touch((const char*)wo + ((tb & 7) * 80 + hw * 40 + 32) * 1280 + (eg * 2 + (sp >> 1)) * 160, ((lane >> 1) & 7) * 1280 + (lane & 1) * 156, lds0 + LDS_SCR);
    kv_wait_free(lds0, 9);
#pragma unroll 1
    for (int n = 0; n < 6; ++n) { yg_issue(cs, ss, ws, lds0, hw * 2 + (n & 1), n >> 1); __builtin_amdgcn_s_sleep(4); }
}

typedef __attribute__((address_space(1))) unsigned gu32;
#define RLX_AGENT __ATOMIC_RELAXED, __HIP_MEMORY_SCOPE_AGENT
DI void poll1(unsigned& v, gu32* c) {
    unsigned long long sv;
    asm volatile("s_mov_b64 %1, exec\n\ts_mov_b64 exec, 1\n\tglobal_load_dword %0, %2, %3 sc1\n\ts_mov_b64 exec, %1" : "+v"(v), "=&s"(sv) : "v"(0u), "s"(c) : "memory");
}
DI void wait_count(gu32* c, unsigned seen, unsigned want) {
    for (unsigned spins = 0; (unsigned)__builtin_amdgcn_readfirstlane(seen) < want && spins < (1u << 17); ++spins) {
        __builtin_amdgcn_s_sleep(1);
        poll1(seen, c);
        asm volatile("s_waitcnt vmcnt(0)" : "+v"(seen)::"memory");
    }
    asm volatile("" ::: "memory");
}
DI u32x4 ld128c(rsrc_t r, unsigned voff) { return __builtin_amdgcn_raw_buffer_load_b128(r, voff, 0, 16); }
DI u32x2 ld64c(rsrc_t r, unsigned voff) { return __builtin_amdgcn_raw_buffer_load_b64(r, voff, 0, 16); }

DI void phase_tail(const u16* __restrict__ cs, const u16* __restrict__ ss, const u16* __restrict__ wo, char* __restrict__ ws, u16* __restrict__ out, unsigned lds0, int wid) {
    const int lane = threadIdx.x & 63, c = lane & 15, q = lane >> 4;
    const int b = blockIdx.x, xcd = b & 7, i = b >> 3;
    YG_MAP;
    const int sb = tb * 2 + (sp & 1), ng = eg * 2 + (sp >> 1);
    STAMP(3, 0);
    __builtin_amdgcn_s_setprio(0);
    if (wid >= 4) {
        const int lw = wid - 4;
        unsigned wv[5];
#pragma unroll
        for (int j = 0; j < 5; ++j) { const unsigned ci = (unsigned)(lw + 4 * j) * 64u + lane, row = (ci * 6554u) >> 16; wv[j] = row * 1280u + (ci - row * 10u) * 16u; PIN(wv[j]); }
        wait_vm<0>(); bar();
#pragma unroll 1
        for (int st = 3; st < 16; ++st) {
            yg_issue(cs, ss, ws, lds0, lw, st);
            wait_vm<18>(); bar_raw();
            if (st == 6 && threadIdx.x == 256) __hip_atomic_fetch_add((gu32*)(ws + WS_FLAG + (256 + (i & 7)) * 128), 1u, RLX_AGENT);
        }
#pragma unroll
        for (int j = 0; j < 25; ++j) {
            dma16((const char*)wo + ng * 160 + (j / 5) * 128 * 1280, wv[j % 5], lds0 + lw * 1024 + j * 4096);
            if (j == 8 || j == 17) { wait_vm<18>(); bar_raw(); }
        }
        wait_vm<0>(); bar_raw();
        wait_count((gu32*)(ws + WS_FLAG + ((tb * 4 + eg) * 4 + sp) * 128), 0u, 3u);
        const int t0 = (lw >> 1) * 3;
        const unsigned yb = WS_YP + (unsigned)(((tb * 4 + eg) * 4 + sp) * 4) * 10240u + (unsigned)((lw & 1) * 5 + t0) * 1024u + lane * 16;
        u32x4 yv[3][3];
#pragma unroll
        for (int t = 0; t < 3; ++t)
#pragma unroll
            for (int v = 0; v < 3; ++v) yv[v][t] = ld128c(ws_rsrc(ws), yb + ((sp + 1 + v) & 3) * 10240 + (t0 + t < 5 ? t : 0) * 1024);
        SBAR();
        bar_raw();
        LAS f32x4* yw = (LAS f32x4*)(uintptr_t)(lds0 + lw * 6144 + lane * 16);
#pragma unroll
        for (int t = 0; t < 3; ++t)
#pragma unroll
            for (int u = 0; u < 2; ++u)
                yw[(u * 3 + t) * 64] = f32x4{(bflo(yv[0][t][2 * u]) + bflo(yv[1][t][2 * u])) + bflo(yv[2][t][2 * u]),
                                             (bfhi(yv[0][t][2 * u]) + bfhi(yv[1][t][2 * u])) + bfhi(yv[2][t][2 * u]),
                                             (bflo(yv[0][t][2 * u + 1]) + bflo(yv[1][t][2 * u + 1])) + bflo(yv[2][t][2 * u + 1]),
                                             (bfhi(yv[0][t][2 * u + 1]) + bfhi(yv[1][t][2 * u + 1])) + bfhi(yv[2][t][2 * u + 1])};
        bar();
        return;
    }
    const rsrc_t wr = ws_rsrc(ws);
    const int rh = wid & 1, kk = wid >> 1;
    bf16x8 fo[5][2][2];
    {
        const int ch = wid >> 1;
        f32x4 acc[4][5];
#pragma unroll
        for (int u = 0; u < 4; ++u)
#pragma unroll
            for (int t = 0; t < 5; ++t) acc[u][t] = f32x4{};
        bf16x8 frE[2][4], fnE[2][5], frO[2][4], fnO[2][5];
        const unsigned af = lds0 + 16384 + ch * 10240 + lane * 16;
        const unsigned a0 = lds0 + (unsigned)(rh * 64 + c) * 128u + (unsigned)((q ^ (c >> 1)) * 16);
        auto step = [&](bf16x8 (&fr)[2][4], bf16x8 (&fn)[2][5], const bf16x8 (&pr)[2][4], const bf16x8 (&pn)[2][5], int st, bool ld, bool mm) {
            lptr p, r0, r1;
            if (ld) {
                bar();
                const unsigned so = (st & 3) * F_STG;
                p = lbase(af + so); r0 = lbase(a0 + so); r1 = lbase((a0 ^ 64u) + so);
            }
#pragma unroll
            for (int k = 0; k < 2; ++k)
#pragma unroll
                for (int t = 0; t < 5; ++t) {
                    if (ld) {
                        fn[k][t] = lds128(p + t * 2048 + k * 1024);
                        if (t < 4) fr[k][t] = lds128((k ? r1 : r0) + t * 2048);
                    }
                    SBAR();
                    if (mm) {
#pragma unroll
                        for (int u = 0; u < 4; ++u) acc[u][t] = MFMA16(pn[k][t], pr[k][u], acc[u][t]);
                    }
                    SBAR();
                }
        };
        gu32* ocnt = (gu32*)(ws + WS_FLAG + (256 + (sb >> 2)) * 128);
        unsigned oseen = 0;
        const int poll_at = wid == 0 ? 8 : 99;
        step(frE, fnE, frO, fnO, 0, true, false);
        STAMP(3, 1);
#pragma unroll 1
        for (int st = 0; st < 14; st += 2) {
            step(frO, fnO, frE, fnE, st + 1, true, true);
            if (st == 2) wait_vm<0>();
            if (st == 2) STAMP(3, 7);
            if (st == poll_at) poll1(oseen, ocnt);
            step(frE, fnE, frO, fnO, st + 2, true, true);
        }
        STAMP(3, 4);
        if (wid == 0) {
            asm volatile("s_waitcnt vmcnt(0)" : "+v"(oseen)::"memory");
            wait_count(ocnt, oseen, 32u);
        }
        step(frO, fnO, frE, fnE, 15, true, true);
        STAMP(3, 5);
        step(frE, fnE, frO, fnO, 16, false, true);
        STAMP(3, 2);
        const unsigned yp = WS_YP + (unsigned)(((tb * 4 + eg) * 4 + wid) * 4 + sp) * 10240u + lane * 16;
        u32x4 pk[2][5];
#pragma unroll
        for (int u = 0; u < 2; ++u)
#pragma unroll
            for (int t = 0; t < 5; ++t) {
                const f32x4 v0 = acc[2 * u][t], v1 = acc[2 * u + 1][t];
                pk[u][t] = u32x4{pk2(v0[0], v0[1]), pk2(v0[2], v0[3]), pk2(v1[0], v1[1]), pk2(v1[2], v1[3])};
            }
        if (wid != sp) {
#pragma unroll
            for (int u = 0; u < 2; ++u)
#pragma unroll
                for (int t = 0; t < 5; ++t) st128(wr, yp, (u * 5 + t) * 1024, pk[u][t]);
        } else {
#pragma unroll
            for (int u = 0; u < 2; ++u)
#pragma unroll
                for (int t = 0; t < 5; ++t) *(LAS u32x4*)(uintptr_t)(lds0 + (u ? F_RING : 102400) + t * 1024 + lane * 16) = pk[u][t];
        }
        SBAR();
        STAMP(3, 3);
        bar();
        wait_vm<0>();
        STAMP(2, 5);
        if (lane == 0 && wid != sp) __hip_atomic_fetch_add((gu32*)(ws + WS_FLAG + ((tb * 4 + eg) * 4 + wid) * 128), 1u, RLX_AGENT);
        const unsigned oa = WS_OA + (unsigned)((sb * 2 + rh) * NH + kk * 4) * 1024u + q * 512 + c * 16;
        const unsigned ob = WS_OB + (unsigned)((sb * 2 + rh) * NH + kk * 16) * 256u + q * 512 + c * 8;
#pragma unroll
        for (int j = 0; j < 4; ++j)
#pragma unroll
            for (int k = 0; k < 2; ++k)
#pragma unroll
                for (int u = 0; u < 2; ++u) fo[j][k][u] = as_frag(ld128c(wr, oa + j * 8192 + k * 2048 + u * 256));
#pragma unroll
        for (int k = 0; k < 2; ++k)
#pragma unroll
            for (int u = 0; u < 2; ++u) {
                const u32x2 a = ld64c(wr, ob + k * 2048 + u * 128), bq = ld64c(wr, ob + k * 2048 + u * 128 + 256);
                fo[4][k][u] = as_frag(u32x4{a[0], a[1], bq[0], bq[1]});
            }
        SBAR();
        STAMP(3, 6);
    }
    const int s0 = sb * 64 + rh * 32 + c;
    f32x4 acc[2][5];
#pragma unroll
    for (int t = 0; t < 5; ++t) { acc[0][t] = f32x4{}; acc[1][t] = f32x4{}; }
    STAMP(2, 0);
    {
        const unsigned wl = lds0 + (unsigned)(c >> 2) * 160u + (c & 3) * 8;
        const lptr p0 = lbase(wl + (unsigned)(kk * 80 + (q >> 1) * 20 + (q & 1) * 4) * 160u), p1 = lbase(wl + (unsigned)(kk * 80 + (q >> 1) * 20 + (q & 1) * 4) * 160u + 51200);
        const lptr p2 = lbase(wl + (unsigned)(kk * 320 + q * 40 + 16) * 160u);
        bf16x8 fn[2][5];
        auto wof = [&](int n, int t) {
            const lptr pp = n < 4 ? p0 + (n >> 1) * 25600 + (n & 1) * 6400 : n < 8 ? p1 + ((n - 4) >> 1) * 25600 + (n & 1) * 6400 : p2 + (n & 1) * 25600;
            const s16x4 lo = ldstr(pp + t * 32), hi = ldstr(pp + t * 32 + (n < 8 ? 8 : 20) * 160);
            return __builtin_bit_cast(bf16x8, __builtin_shufflevector(lo, hi, 0, 1, 2, 3, 4, 5, 6, 7));
        };
#define WOF(n, t) wof(n, t)
#pragma unroll
        for (int t = 0; t < 5; ++t) fn[0][t] = WOF(0, t);
#pragma unroll
        for (int n = 0; n < 10; ++n)
#pragma unroll
            for (int t = 0; t < 5; ++t) {
                if (n < 9) fn[(n + 1) & 1][t] = WOF(n + 1, t);
                SBAR();
                acc[0][t] = MFMA16(fn[n & 1][t], fo[n >> 1][n & 1][0], acc[0][t]);
                acc[1][t] = MFMA16(fn[n & 1][t], fo[n >> 1][n & 1][1], acc[1][t]);
                SBAR();
            }
    }
    STAMP(2, 1);
    const float g0 = *(const LAS float*)(uintptr_t)(lds0 + LDS_G + (rh * 32 + c) * 4), g1 = *(const LAS float*)(uintptr_t)(lds0 + LDS_G + (rh * 32 + 16 + c) * 4);
    const int t0 = kk * 3;
    f32x4 fin[2][3];
    {
        LAS f32x4* xw = (LAS f32x4*)(uintptr_t)(lds0 + 3 * F_STG + wid * 6144 + lane * 16);
        const LAS f32x4* xr = (const LAS f32x4*)(uintptr_t)(lds0 + 3 * F_STG + (wid ^ 2) * 6144 + lane * 16);
        unsigned mk = 0u - (unsigned)kk;
        PIN(mk);
#pragma unroll
        for (int u = 0; u < 2; ++u)
#pragma unroll
            for (int t = 0; t < 3; ++t) {
                const f32x4 lo = acc[u][t], hi = acc[u][t < 2 ? 3 + t : 4];
                f32x4 snd;
#pragma unroll
                for (int e = 0; e < 4; ++e) { fin[u][t][e] = bsel(mk, hi[e], lo[e]); snd[e] = bsel(mk, lo[e], hi[e]); }
                xw[(u * 3 + t) * 64] = snd;
            }
        bar();
#pragma unroll
        for (int u = 0; u < 2; ++u)
#pragma unroll
            for (int t = 0; t < 3; ++t) fin[u][t] = (fin[u][t] + xr[(u * 3 + t) * 64]) * (u ? g1 : g0);
    }
#pragma unroll
    for (int t = 0; t < 3; ++t) {
        const u32x4 y = *(const LAS u32x4*)(uintptr_t)(lds0 + (rh ? F_RING : 102400) + (t0 + t < 5 ? t0 + t : 0) * 1024 + lane * 16);
#pragma unroll
        for (int u = 0; u < 2; ++u) fin[u][t] += f32x4{bflo(y[2 * u]), bfhi(y[2 * u]), bflo(y[2 * u + 1]), bfhi(y[2 * u + 1])} * (1.0f - (u ? g1 : g0));
    }
    bar_raw();
    STAMP(2, 2);
    const LAS f32x4* yr = (const LAS f32x4*)(uintptr_t)(lds0 + wid * 6144 + lane * 16);
    u16* op = out + (size_t)s0 * DM + ng * 80 + t0 * 16 + q * 4;
#pragma unroll
    for (int t = 0; t < 3; ++t) {
        if (t0 + t < 5) {
#pragma unroll
            for (int u = 0; u < 2; ++u) {
                const f32x4 v = fin[u][t] + yr[(u * 3 + t) * 64] * (1.0f - (u ? g1 : g0));
                *(u32x2*)(op + (size_t)u * 16 * DM + t * 16) = u32x2{pk2(v[0], v[1]), pk2(v[2], v[3])};
            }
        }
    }
    STAMP_END(2);
}

constexpr int LDS_B = LDS_SCR + 256;

__global__ __launch_bounds__(512) void k_proj(const u16* __restrict__ x, const u16* __restrict__ wqkv, const u16* __restrict__ cd,
                                              const u16* __restrict__ sd, char* __restrict__ ws) {
    extern __shared__ __attribute__((aligned(16))) char smem[];
    const unsigned lds0 = (unsigned)(uintptr_t)smem;
    const int wid = __builtin_amdgcn_readfirstlane(threadIdx.x >> 6);
    const int b = blockIdx.x, xcd = b & 7, i = b >> 3;
    const int rb = (xcd & 1) * 8 + (i & 7), cq = xcd >> 1, gl = i >> 3;
    asm volatile("" ::"s"(x), "s"(wqkv), "s"(cd), "s"(sd));
    if (wid < 4) {
        STAMP(0, 0);
        if (b == 255) {
            const unsigned tl = threadIdx.x;
            st128(ws_rsrc(ws), (tl * VP + 640) * 16, WS_VV, u32x4{ONES, ONES, ONES, ONES});
            st32(ws_rsrc(ws), tl * 128, WS_FLAG, 0u);
            if (tl < 8) st32(ws_rsrc(ws), (256 + tl) * 128, WS_FLAG, 0u);
        }
        if (gl == 3) proj_mma<2>(ws, rb, cq, lds0, wid);
        else if (cq * 3 + gl < 8) proj_mma<0>(ws, rb, cq * 3 + gl, lds0, wid);
        else proj_mma<1>(ws, rb, cq * 3 + gl, lds0, wid);
        wait_vm<0>();
        STAMP(0, 4);
        STAMP_FLUSH(0);
        return;
    }
    proj_load(x, wqkv, cd, sd, rb, cq, gl, lds0, wid - 4);
    STAMP(0, 4);
    STAMP_FLUSH(0);
}

__global__ __launch_bounds__(512) void k_mix(const u16* __restrict__ x, const u16* __restrict__ wo, const u16* __restrict__ gw,
                                             const u16* __restrict__ cs, const u16* __restrict__ ss, char* __restrict__ ws, u16* __restrict__ out) {
    extern __shared__ __attribute__((aligned(16))) char smem[];
    const int wid = __builtin_amdgcn_readfirstlane(threadIdx.x >> 6);
    if (wid < 4) phase_attn(ws, (unsigned)(uintptr_t)smem, wid);
    else if (wid < 6) attn_produce(x, gw, ws, (unsigned)(uintptr_t)smem, wid - 4);
    else attn_helper(cs, ss, wo, x, gw, ws, (unsigned)(uintptr_t)smem, wid - 6);
    phase_tail(cs, ss, wo, ws, out, (unsigned)(uintptr_t)smem, wid);
    STAMP_FLUSH(1);
}

extern "C" void kernel_launch(void* const* d_in, const int* in_sizes, int n_in, void* d_out, int out_size, void* d_ws, size_t ws_size,
                              hipStream_t stream) {
    const u16* x = (const u16*)d_in[0];
    const u16* wqkv = (const u16*)d_in[1];
    const u16* wo = (const u16*)d_in[2];
    const u16* cs = (const u16*)d_in[4];
    const u16* ss = (const u16*)d_in[5];
    const u16* cd = (const u16*)d_in[6];
    const u16* sd = (const u16*)d_in[7];
    const u16* gw = (const u16*)d_in[8];
    char* ws = (char*)d_ws;
    (void)hipFuncSetAttribute((const void*)k_proj, hipFuncAttributeMaxDynamicSharedMemorySize, LDS_B);
    (void)hipFuncSetAttribute((const void*)k_mix, hipFuncAttributeMaxDynamicSharedMemorySize, LDS_B);
    k_proj<<<256, 512, LDS_B, stream>>>(x, wqkv, cd, sd, ws);
    k_mix<<<256, 512, LDS_B, stream>>>(x, wo, gw, cs, ss, ws, (u16*)d_out);
}
```

```cpp
#define SIGFIRST
#include <hip/hip_runtime.h>
#include <hip/hip_bf16.h>
#include <stdint.h>

typedef unsigned short u16;
typedef __attribute__((ext_vector_type(8))) __bf16 bf16x8;
typedef __attribute__((ext_vector_type(4))) short s16x4;
typedef __attribute__((ext_vector_type(16))) float f32x16;
typedef __attribute__((ext_vector_type(4))) float f32x4;
typedef __attribute__((ext_vector_type(4))) unsigned u32x4;
typedef __attribute__((ext_vector_type(2))) unsigned u32x2;
#define LAS __attribute__((address_space(3)))
#define DI __device__ __forceinline__
typedef const LAS char* lptr;

constexpr int SEQ = 2048, DM = 640, NH = 32;
constexpr int VP = 648;
constexpr unsigned WS_QA = 0;
constexpr unsigned WS_KA = WS_QA + NH * 64 * 1024;
constexpr unsigned WS_QB = WS_KA + NH * 64 * 1024;
constexpr unsigned WS_KB = WS_QB + NH * 64 * 512;
constexpr unsigned WS_VV = WS_KB + NH * 64 * 512;
constexpr unsigned WS_AB = WS_VV + 64 * 4 * VP * 16;
constexpr unsigned WS_YP = WS_AB + 40 * 128 * 1024;
constexpr unsigned WS_OA = WS_YP + 4 * 64 * 40 * 1024;
constexpr unsigned WS_OB = WS_OA + 64 * NH * 1024;
constexpr unsigned WS_FLAG = WS_OB + 64 * NH * 256;
constexpr unsigned WS_REQ = WS_FLAG + 274 * 128;
constexpr unsigned WS_RSP = WS_REQ + 1024;
constexpr unsigned WS_ACK = WS_RSP + 1024;
constexpr unsigned WS_END = WS_ACK + 1024;
constexpr float QSCALE = 0.22360679774997896f * 1.4426950408889634f;
#define ST_AUX 16

#define STAMP(k, i) do {} while (0)
#define STAMP_END(k) do {} while (0)
#define STAMP_FLUSH(kk) do {} while (0)
#define MFMA32(a, b, c) __builtin_amdgcn_mfma_f32_32x32x16_bf16((a), (b), (c), 0, 0, 0)
#define MFMA16(a, b, c) __builtin_amdgcn_mfma_f32_16x16x32_bf16((a), (b), (c), 0, 0, 0)
#define SBAR() __builtin_amdgcn_sched_barrier(0)
#define PIN(x) asm volatile("" : "+v"(x))
#define KEEP(x) asm volatile("" ::"v"(x))

typedef __attribute__((ext_vector_type(2))) __bf16 bf16x2;
typedef __attribute__((ext_vector_type(2))) float f32x2;
DI unsigned pk2(float lo, float hi) {
    f32x2 v = {lo, hi};
    return __builtin_bit_cast(unsigned, __builtin_convertvector(v, bf16x2));
}
DI float bsel(unsigned m, float a, float b) { return __uint_as_float((__float_as_uint(a) & m) | (__float_as_uint(b) & ~m)); }
DI float bflo(unsigned v) { return __uint_as_float(v << 16); }
DI float bfhi(unsigned v) { return __uint_as_float(v & 0xFFFF0000u); }
DI void dma16(const void* sbase, unsigned voff, unsigned lds) {
    asm volatile("s_mov_b32 m0, %2\n\ts_nop 0\n\tglobal_load_lds_dwordx4 %0, %1" ::"v"(voff), "s"(sbase), "s"(lds) : "memory");
}
DI void dma16c(const void* sbase, unsigned voff, unsigned lds) {
    asm volatile("s_mov_b32 m0, %2\n\ts_nop 0\n\tglobal_load_lds_dwordx4 %0, %1 sc1" ::"v"(voff), "s"(sbase), "s"(lds) : "memory");
}
template <int N> DI void dma16_lo(const void* sbase, unsigned voff, unsigned lds) {
    unsigned long long sv;
    asm volatile("s_mov_b32 m0, %3\n\ts_mov_b64 %0, exec\n\ts_mov_b64 exec, %4\n\tglobal_load_lds_dwordx4 %1, %2 sc1\n\ts_mov_b64 exec, %0"
                 : "=&s"(sv) : "v"(voff), "s"(sbase), "s"(lds), "s"((1ull << N) - 1) : "memory");
}
DI void touch(const void* sbase, unsigned voff, unsigned lds) {
    asm volatile("s_mov_b32 m0, %2\n\ts_nop 0\n\tglobal_load_lds_dword %0, %1" ::"v"(voff), "s"(sbase), "s"(lds) : "memory");
}
template <int N> DI void wait_vm() { asm volatile("s_waitcnt vmcnt(%0)" ::"i"(N) : "memory"); }
DI void bar() { asm volatile("s_waitcnt lgkmcnt(0)\n\ts_barrier" ::: "memory"); }
DI void bar_raw() { asm volatile("s_barrier" ::: "memory"); }
DI lptr lbase(unsigned a) { asm volatile("" : "+v"(a)); return (lptr)(uintptr_t)a; }
DI bf16x8 lds128(lptr p) { return *(const LAS bf16x8*)p; }
DI u32x2 lds64(lptr p) { return *(const LAS u32x2*)p; }
DI s16x4 ldstr(lptr p) {
    typedef short v4i16_t __attribute__((ext_vector_type(4)));
    return __builtin_bit_cast(s16x4, __builtin_amdgcn_ds_read_tr16_b64_v4i16((LAS v4i16_t*)p));
}
DI u32x4 pack8(const f32x16& x, int s, float sc) {
    u32x4 p;
    p[0] = pk2(x[8 * s + 0] * sc, x[8 * s + 1] * sc);
    p[1] = pk2(x[8 * s + 2] * sc, x[8 * s + 3] * sc);
    p[2] = pk2(x[8 * s + 4] * sc, x[8 * s + 5] * sc);
    p[3] = pk2(x[8 * s + 6] * sc, x[8 * s + 7] * sc);
    return p;
}
typedef __amdgpu_buffer_rsrc_t rsrc_t;
DI rsrc_t ws_rsrc(const void* ws) { return __builtin_amdgcn_make_buffer_rsrc((void*)ws, (short)0, (int)WS_END, 0x00020000); }
#define K1_AUX 16
DI void st128p(rsrc_t r, unsigned voff, unsigned soff, u32x4 v) { __builtin_amdgcn_raw_buffer_store_b128(v, r, voff + soff, 0, K1_AUX); }
DI void st128(rsrc_t r, unsigned voff, unsigned soff, u32x4 v) { __builtin_amdgcn_raw_buffer_store_b128(v, r, voff + soff, 0, ST_AUX); }
DI void st64(rsrc_t r, unsigned voff, unsigned soff, u32x2 v) { __builtin_amdgcn_raw_buffer_store_b64(v, r, voff, soff, ST_AUX); }
DI void st32(rsrc_t r, unsigned voff, unsigned soff, unsigned v) { __builtin_amdgcn_raw_buffer_store_b32(v, r, voff, soff, ST_AUX); }
#define LD_AUX 16
DI u32x4 ld128(rsrc_t r, unsigned voff) { return __builtin_amdgcn_raw_buffer_load_b128(r, voff, 0, LD_AUX); }
DI bf16x8 as_frag(u32x4 v) { return __builtin_bit_cast(bf16x8, v); }
typedef __attribute__((ext_vector_type(16))) __bf16 bf16x16;
template <int HI> DI f32x16 pv_mma(bf16x8 v, const f32x16& p, const f32x16& o, int idx) {
    const auto ph = __builtin_shufflevector(p, p, 8 * HI, 8 * HI + 1, 8 * HI + 2, 8 * HI + 3, 8 * HI + 4, 8 * HI + 5, 8 * HI + 6, 8 * HI + 7);
    return __builtin_amdgcn_smfmac_f32_32x32x32_bf16(v, __builtin_bit_cast(bf16x16, ph), o, idx, 0, 0);
}
DI u32x4 pack8n(const f32x16& x, int s, float sc) {
    const u32x4 p = pack8(x, s, sc);
    const auto r0 = __builtin_amdgcn_permlane32_swap(p[0], p[2], false, false);
    const auto r1 = __builtin_amdgcn_permlane32_swap(p[1], p[3], false, false);
    return u32x4{r0[0], r1[0], r0[1], r1[1]};
}

#define POLL_SLEEP 4
typedef __attribute__((address_space(1))) unsigned gu32;
#define RLX_AGENT __ATOMIC_RELAXED, __HIP_MEMORY_SCOPE_AGENT
DI void poll1(unsigned& v, gu32* c) {
    unsigned long long sv;
    asm volatile("s_mov_b64 %1, exec\n\ts_mov_b64 exec, 1\n\tglobal_load_dword %0, %2, %3 sc1\n\ts_mov_b64 exec, %1" : "+v"(v), "=&s"(sv) : "v"(0u), "s"(c) : "memory");
}
DI void wait_count(gu32* c, unsigned seen, unsigned want) {
    for (unsigned spins = 0; (unsigned)__builtin_amdgcn_readfirstlane(seen) < want && spins < (1u << 17); ++spins) {
        __builtin_amdgcn_s_sleep(POLL_SLEEP);
        poll1(seen, c);
        asm volatile("s_waitcnt vmcnt(0)" : "+v"(seen)::"memory");
    }
    asm volatile("" ::: "memory");
}

constexpr int LEADER = 255;
DI void echo_round(u32x4& v, const char* ws, bool put) {
    const unsigned o = (threadIdx.x & 63) * 16;
    if (put) asm volatile("global_store_dwordx4 %0, %1, %2 sc1\n\ts_nop 1" ::"v"(o), "v"(v), "s"(ws + WS_RSP) : "memory");
    asm volatile("global_load_dwordx4 %0, %1, %2 sc1" : "+v"(v) : "v"(o), "s"(ws + WS_REQ) : "memory");
}

constexpr int P_STG = 45056;
constexpr int P_RING = 3 * P_STG;
#define EB0 1

DI void store_ab(const f32x16& acc, int dt, int stile, rsrc_t ws, int lane) {
    const float sc = dt >= 20 ? -1.f : 1.f;
    const int nt = dt >= 20 ? dt - 20 : dt;
    const int ks = (dt >= 20 ? 64 : 0) + stile;
    const unsigned dst = WS_AB + (unsigned)((nt * 2 + ((lane >> 4) & 1)) * 128 + ks) * 1024u + (unsigned)((lane >> 5) * 256 + (lane & 15) * 16);
#pragma unroll
    for (int s = 0; s < 2; ++s) st128p(ws, dst, s * 512, pack8n(acc, s, sc));
}

DI void proj_load(const u16* __restrict__ x, const u16* __restrict__ wqkv, const u16* __restrict__ cd, const u16* __restrict__ sd,
                  const char* __restrict__ ws, int rb, int cq, int gl, unsigned lds0, int wid) {
    const int lane = threadIdx.x & 63;
    constexpr int NDMA = 10;
    const bool pa = gl != 3;
    const unsigned vrow = (unsigned)(lane >> 3) * 1280u + (unsigned)(((lane & 7) ^ ((4 * (wid & 1) + (lane >> 4)) & 7)) * 16);
    const char* xb = (const char*)x + ((size_t)rb * 128 + wid * 8) * 1280;
    int st = 3 * ((rb + gl) % 3);
    unsigned slot = lds0 + wid * 1024;
#pragma unroll
    for (int j = 0; j < 4; ++j) dma16(xb + j * 40960 + st * 128, vrow, slot + j * 4096);
    const char* wb[7];
    unsigned vo[5];
    const int dt0 = pa ? 28 + cq * 3 + gl : 7 * cq;
#pragma unroll
    for (int j = 0; j < 7; ++j) {
        const int dt = dt0 + j;
        const char* d = (const char*)(dt >= 20 ? sd : cd) + ((dt >= 20 ? dt - 20 : dt) * 32 + wid * 8) * 1280;
        wb[j] = pa && j ? (const char*)wqkv + (cq * 3 + gl) * 320 : d;
    }
#pragma unroll
    for (int j = 0; j < 5; ++j) { const unsigned i = (wid + 4 * j) * 64 + lane, q = (i * 3277u) >> 16; vo[j] = pa ? q * 3840u + (i - q * 20u) * 16u : vrow; }
    const int wstep = pa ? 64 * 3840 : 128;
    auto issue = [&](bool with_x) {
        if (with_x) {
#pragma unroll
            for (int j = 0; j < 4; ++j) dma16(xb + j * 40960 + st * 128, vrow, slot + j * 4096);
        }
        if (!pa) dma16(wb[6] + st * 128, vrow, slot + 40960);
        dma16(wb[0] + st * 128, vrow, slot + 16384);
#pragma unroll
        for (int j = 0; j < 5; ++j) dma16(wb[1 + j] + st * wstep, vo[j], slot + 20480 + j * 4096);
        slot = slot + P_STG == lds0 + wid * 1024 + P_RING ? lds0 + wid * 1024 : slot + P_STG;
        st = st == 9 ? 0 : st + 1;
    };
    const bool leader = blockIdx.x == LEADER && wid == 3;
    u32x4 rq = {0u, 0u, 0u, 0u};
    unsigned back = 0;
    STAMP(0, 5);
#pragma unroll 1
    for (int n = 0; n < 3; ++n) {
        issue(n > 0);
        if (n == 0) STAMP(0, 6);
        if (n == EB0) { wait_vm<EB0 * NDMA>(); bar_raw(); }
    }
    STAMP(0, 7);
    wait_vm<NDMA>(); bar_raw(); bar_raw();
#pragma unroll 1
    for (int n = 3; n < 10; ++n) {
        if (leader) echo_round(rq, ws, n > 3);
        if (wid == 0 && n == 9) poll1(back, (gu32*)(ws + WS_RSP + blockIdx.x * 4));
        issue(true); wait_vm<NDMA>(); bar_raw(); bar_raw();
    }
    wait_vm<0>();
    bar_raw();
    bool acked = wid != 0;
    const unsigned want = __builtin_amdgcn_readfirstlane(*(const volatile LAS unsigned*)(uintptr_t)(lds0 + P_RING));
    PIN(back);
    if (!acked && (unsigned)__builtin_amdgcn_readfirstlane(back) == want) {
        if (lane == 0) __hip_atomic_store((gu32*)(ws + WS_ACK + blockIdx.x * 4), 1u, RLX_AGENT);
        acked = true;
    }
    bar_raw(); bar_raw(); bar_raw();
    if (!acked) {
        for (unsigned spins = 0; spins < (1u << 17) && (unsigned)__builtin_amdgcn_readfirstlane(back) != want; ++spins) {
            __builtin_amdgcn_s_sleep(4);
            poll1(back, (gu32*)(ws + WS_RSP + blockIdx.x * 4));
            asm volatile("s_waitcnt vmcnt(0)" : "+v"(back)::"memory");
        }
        if (lane == 0) __hip_atomic_store((gu32*)(ws + WS_ACK + blockIdx.x * 4), 1u, RLX_AGENT);
    }
    if (leader) {
        for (unsigned spins = 0; spins < (1u << 17); ++spins) {
            echo_round(rq, ws, true);
            u32x4 ak;
            asm volatile("global_load_dwordx4 %0, %2, %3 sc1\n\ts_waitcnt vmcnt(0)" : "=&v"(ak), "+v"(rq) : "v"(lane * 16), "s"(ws + WS_ACK) : "memory");
            if (__all((ak[0] & ak[1] & ak[2] & ak[3]) == 1u)) break;
            __builtin_amdgcn_s_sleep(4);
        }
    }
}

template <int PATH>
DI void proj_mma(char* __restrict__ ws, int rb, int grp, unsigned lds0, int wid ) {
    const int lane = threadIdx.x & 63, r = lane & 31, h = lane >> 5;
    constexpr bool PATH_A = PATH < 2;
    constexpr int NW = PATH_A ? 5 : 0, ND = PATH_A ? 1 : 7, NT = NW + ND;
    const int dt0 = PATH_A ? 28 + grp : 7 * grp;
    const unsigned sw = lds0 + (unsigned)r * 128u + (unsigned)((h ^ ((r >> 1) & 7)) * 16);
    const unsigned awo = lds0 + 20480u + (unsigned)(8 * h + ((lane & 15) >> 2)) * 320u + (unsigned)(((lane >> 4) & 1) * 32 + (lane & 3) * 8);
    f32x16 acc[NT];
#pragma unroll
    for (int t = 0; t < NT; ++t) acc[t] = f32x16{};
    bf16x8 fx[2], fw[2][NT];
    lptr pr, px, pw;
    auto setp = [&](unsigned so, int ks) {
        pr = lbase((sw ^ (unsigned)(ks * 32)) + so);
        px = lbase((sw ^ (unsigned)(ks * 32)) + so + wid * 4096);
        if (PATH_A && ks == 0) pw = lbase(awo + so);
    };
    auto rd = [&](int ks, int b, int t) {
        if (t == 0) fx[b] = lds128(px);
        if (t < ND) fw[b][t] = lds128(pr + 16384 + t * 4096);
        else {
            s16x4 lo = ldstr(pw + ks * 16 * 320 + (t - ND) * 64), hi = ldstr(pw + ks * 16 * 320 + 4 * 320 + (t - ND) * 64);
            fw[b][t] = __builtin_bit_cast(bf16x8, __builtin_shufflevector(lo, hi, 0, 1, 2, 3, 4, 5, 6, 7));
        }
    };
    auto mm = [&](int b, int t) {
        if (PATH == 0 && t >= ND) acc[t] = MFMA32(fw[b][t], fx[b], acc[t]);
        else acc[t] = MFMA32(fx[b], fw[b][t], acc[t]);
    };
    unsigned so = 0;
    setp(0, 0);
#pragma unroll
    for (int t = 0; t < NT; ++t) PIN(acc[t]);
    bar_raw();
    STAMP(0, 1);
#pragma unroll
    for (int t = 0; t < NT; ++t) rd(0, 0, t);
    SBAR();
#pragma unroll 1
    for (int st = 0; st < 10; ++st) {
#pragma unroll
        for (int ks = 0; ks < 4; ++ks) {
            if (ks == 3) { bar_raw(); so = so == 2 * P_STG ? 0 : so + P_STG; }
            setp(so, (ks + 1) & 3);
#pragma unroll
            for (int t = 0; t < NT; ++t) { rd((ks + 1) & 3, (ks + 1) & 1, t); SBAR(); mm(ks & 1, t); SBAR(); }
        }
        bar_raw();
    }
    STAMP(0, 2);
    const rsrc_t wr = ws_rsrc(ws);
    const int stile = rb * 4 + wid;
    if (PATH == 0) {
        const float sc = grp < 4 ? QSCALE : 1.f;
        const unsigned t0 = (unsigned)(grp * 8 * 64 + stile);
        const unsigned va = WS_QA + t0 * 1024 + lane * 16, vb = WS_QB + (t0 + h * 64) * 512 + r * 16;
#pragma unroll
        for (int m = 0; m < 4; ++m) {
            u32x2 q[5];
#pragma unroll
            for (int i = 0; i < 5; ++i) {
                const int p = 5 * m + i, t = ND + (p >> 2), g = p & 3;
                q[i][0] = pk2(acc[t][4 * g] * sc, acc[t][4 * g + 1] * sc);
                q[i][1] = pk2(acc[t][4 * g + 2] * sc, acc[t][4 * g + 3] * sc);
            }
            st128p(wr, va, (2 * m) * 64 * 1024, u32x4{q[0][0], q[0][1], q[1][0], q[1][1]});
            st128p(wr, va, (2 * m + 1) * 64 * 1024, u32x4{q[3][0], q[3][1], q[4][0], q[4][1]});
            st128p(wr, vb, (2 * m) * 64 * 512, u32x4{q[2][0], q[2][1], 0x00003F80u, 0u});
        }
    } else if (PATH == 1) {
        const unsigned vv = WS_VV + (unsigned)((stile * 4 + h) * VP + (grp - 8) * 160 + r) * 16;
#pragma unroll
        for (int t = 0; t < 5; ++t) {
            st128p(wr, vv, t * 512, pack8n(acc[ND + t], 0, 1.f));
            st128p(wr, vv, t * 512 + 2 * VP * 16, pack8n(acc[ND + t], 1, 1.f));
        }
    }
#pragma unroll
    for (int t = 0; t < ND; ++t) store_ab(acc[t], dt0 + t, stile, wr, lane);
    STAMP(0, 3);
}
#define YG_MAP const int tb = (xcd >> 2) * 8 + (i & 7), eg = i >> 3, sp = xcd & 3
constexpr int F_STG = 36864;
constexpr int F_RING = 4 * F_STG;
DI void yg_issue(const u16* __restrict__ cs, const u16* __restrict__ ss, const char* __restrict__ ws, unsigned lds0, int lw, int st) {
    const int lane = threadIdx.x & 63;
    const int b = blockIdx.x, xcd = b & 7, i = b >> 3;
    YG_MAP;
    const unsigned vrow = (unsigned)(lane >> 3) * 4096u + (unsigned)(((lane & 7) ^ ((4 * (lw & 1) + (lane >> 4)) & 7)) * 16);
    const char* rp = (const char*)(sp >= 2 ? ss : cs) + (size_t)(tb * 128 + lw * 8) * 4096 + (sp & 1) * 2048;
    const char* ab = ws + WS_AB + (size_t)((eg * 10 + (lw >> 1)) * 128 + sp * 32 + (lw & 1)) * 1024;
    const unsigned d = lds0 + lw * 1024 + (st & 3) * F_STG;
#pragma unroll
    for (int j = 0; j < 4; ++j) dma16(rp + j * 32 * 4096 + st * 128, vrow, d + j * 4096);
#pragma unroll
    for (int j = 0; j < 5; ++j) dma16c(ab + (j * 256 + st * 2) * 1024, lane * 16, d + 16384 + j * 4096);
}

DI float xhalf_max(float v) {
    auto rr = __builtin_amdgcn_permlane32_swap(__float_as_uint(v), __float_as_uint(v), false, false);
    return fmaxf(__uint_as_float(rr[0]), __uint_as_float(rr[1]));
}
DI float max16(const f32x16& s) {
    float a = fmaxf(fmaxf(s[0], s[1]), s[2]), b = fmaxf(fmaxf(s[3], s[4]), s[5]);
    a = fmaxf(fmaxf(a, s[6]), s[7]); b = fmaxf(fmaxf(b, s[8]), s[9]);
    a = fmaxf(fmaxf(a, s[10]), s[11]); b = fmaxf(fmaxf(b, s[12]), s[13]);
    a = fmaxf(fmaxf(a, s[14]), s[15]);
    return fmaxf(a, b);
}
#define EX2(v) __builtin_amdgcn_exp2f(v)
constexpr unsigned VV_T = 4 * VP * 16, VV_S = 2 * VP * 16;
constexpr unsigned ONES = 0x3F803F80u;

DI u32x4 fix_v(u32x4 v, int r) {
#pragma unroll
    for (int i = 0; i < 4; ++i) v[i] = r < 20 ? v[i] : (r == 20 ? ONES : 0u);
    return v;
}
DI f32x16 attn_safe(rsrc_t wr, int head, u32x4 qa, u32x4 qb, int lane) {
    const int r = lane & 31, h = lane >> 5;
    qb[2] = 0u;
    f32x16 o = f32x16{};
    float m = -INFINITY;
#pragma unroll 1
    for (int t = 0; t < 64; ++t) {
        const u32x4 ka = ld128(wr, WS_KA + (unsigned)(head * 64 + t) * 1024 + lane * 16);
        const u32x4 kb = ld128(wr, WS_KB + (unsigned)(head * 64 + t) * 512 + r * 16);
        const unsigned vp = WS_VV + (unsigned)((t * 4 + h) * VP + head * 20 + (r < 20 ? r : 0)) * 16;
        const u32x4 v0 = fix_v(ld128(wr, vp), r), v1 = fix_v(ld128(wr, vp + VV_S), r);
        f32x16 s = MFMA32(as_frag(ka), as_frag(qa), f32x16{});
        s = MFMA32(as_frag(kb), as_frag(qb), s);
        const float mn = fmaxf(m, xhalf_max(max16(s)));
        const float al = EX2(m - mn);
        m = mn;
#pragma unroll
        for (int i = 0; i < 16; ++i) { o[i] *= al; s[i] = EX2(s[i] - mn); }
        o = pv_mma<0>(as_frag(v0), s, o, (int)0xDDDDDDDD);
        o = pv_mma<1>(as_frag(v1), s, o, (int)0xDDDDDDDD);
    }
    return o;
}

#define H_PACE 8
#define H_START 2
DI void attn_finish(f32x16 o, rsrc_t wr, int head, int qt) {
    const int lane = threadIdx.x & 63, r = lane & 31, h = lane >> 5;
    auto rl = __builtin_amdgcn_permlane32_swap(__float_as_uint(o[8]), __float_as_uint(o[8]), false, false);
    float l = __uint_as_float(rl[1]);
    float z = 0.f;
#pragma unroll
    for (int k = 0; k < 12; ++k) z = __builtin_fmaf(o[k], 0.f, z);
    const bool ok = (z == 0.f) && (l >= 8.6736174e-19f) && (l <= 1.2676506e30f);
    if (__builtin_expect(!__all(ok), 0)) {
        const u32x4 q0 = ld128(wr, WS_QA + (unsigned)(head * 64 + qt) * 1024 + lane * 16);
        const u32x4 q1 = ld128(wr, h ? 0x80000000u : WS_QB + (unsigned)(head * 64 + qt) * 512 + r * 16);
        o = attn_safe(wr, head, q0, q1, lane);
        rl = __builtin_amdgcn_permlane32_swap(__float_as_uint(o[8]), __float_as_uint(o[8]), false, false);
        l = __uint_as_float(rl[1]);
    }
    const float inv = __builtin_amdgcn_rcpf(l);
    const unsigned ot = (unsigned)(qt * NH + head);
    st128(wr, lane * 16, WS_OA + ot * 1024, pack8(o, 0, inv));
    if (h == 0) st64(wr, r * 8, WS_OB + ot * 256, u32x2{pk2(o[8] * inv, o[9] * inv), pk2(o[10] * inv, o[11] * inv)});
}

#define HPRIO 3
#define KVSLEEP 8
constexpr int KVT_B = 2880, KVG_B = 4 * KVT_B, KV1 = 3 * F_STG;
constexpr int KV_FULL = KV1 + 4 * KVG_B;
constexpr int KV_FREE = KV_FULL + 16;
constexpr int LDS_G = KV_FREE + 16;
constexpr int LDS_SCR = LDS_G + 256;
DI unsigned kv_off(unsigned g) { return g < 9 ? g * KVG_B : KV1 + ((g - 9) & 3) * KVG_B; }
DI void kv_wait_free(unsigned lds0, unsigned n) {
    for (;;) {
        const u32x4 f = *(const volatile LAS u32x4*)(uintptr_t)(lds0 + KV_FREE);
        if (__builtin_amdgcn_readfirstlane(min(min(f[0], f[1]), min(f[2], f[3]))) >= n) break;
        __builtin_amdgcn_s_sleep(KVSLEEP);
    }
    asm volatile("" ::: "memory");
}
DI void lds_put(unsigned addr, unsigned v) { *(volatile LAS unsigned*)(uintptr_t)addr = v; }

DI void phase_attn(char* __restrict__ ws, unsigned lds0, int wid) {
    const int lane = threadIdx.x & 63, r = lane & 31, h = lane >> 5;
    const int b = blockIdx.x, head = (b & 7) * 4 + (b >> 6), qt0 = ((b >> 3) & 7) * 8 + wid * 2;
    const rsrc_t wr = ws_rsrc(ws);
    bar(); bar_raw();
    STAMP(1, 0);
    u32x4 qa[2], qbv[2];
#pragma unroll
    for (int i = 0; i < 2; ++i) {
        qa[i] = ld128(wr, WS_QA + (unsigned)(head * 64 + qt0 + i) * 1024 + lane * 16);
        qbv[i] = ld128(wr, h ? 0x80000000u : WS_QB + (unsigned)(head * 64 + qt0 + i) * 512 + r * 16);
    }
    const int rv = r <= 20 ? r : (r < 28 ? 20 : 16);
    const unsigned oka = lds0 + lane * 16, ov = lds0 + 1536u + (unsigned)(h * 21 + rv) * 16u;
    unsigned afull = lds0 + KV_FULL, afree = lds0 + KV_FREE + wid * 4;
    PIN(afull); PIN(afree);
    __builtin_amdgcn_s_setprio(2);
    bar_raw();
    bf16x8 ka[2], kb[2], v0[2], v1[2];
    lptr pka = lbase(oka), pv = lbase(ov);
#define LDK(n, T) do { ka[n] = lds128(pka + (T) * KVT_B); kb[n] = lds128(pka + (T) * KVT_B + 1024); SBAR(); } while (0)
#define LDV(n, T) do { v0[n] = lds128(pv + (T) * KVT_B); v1[n] = lds128(pv + (T) * KVT_B + 42 * 16); SBAR(); } while (0)
    LDK(0, 0); LDK(1, 1); LDV(0, 0);
#pragma unroll
    for (int i = 0; i < 2; ++i) {
        qbv[i][2] = 0u;
        f32x16 t = MFMA32(ka[0], as_frag(qa[i]), f32x16{});
        t = MFMA32(kb[0], as_frag(qbv[i]), t);
        const float m0 = xhalf_max(max16(t));
        qbv[i][2] = h ? 0u : (pk2(-m0, 0.f) & 0xFFFFu);
    }
    STAMP(1, 1);
    f32x16 oA = f32x16{}, oB = f32x16{}, s0, s1, s2, s3 = f32x16{};
    int idx = (int)0xDDDDDDDD;
    PIN(idx);
    v0[1] = ka[0]; v1[1] = ka[0];
    s0 = MFMA32(ka[0], as_frag(qa[0]), f32x16{});
    s0 = MFMA32(kb[0], as_frag(qbv[0]), s0);
    SBAR();
#define GRP(MF, H, S, i) do { MF; SBAR(); H; SBAR(); S[i] = EX2(S[i]); S[i + 1] = EX2(S[i + 1]); S[i + 2] = EX2(S[i + 2]); S[i + 3] = EX2(S[i + 3]); PIN(S); SBAR(); } while (0)
#define SLOT(X, N, kn, Q, P, O, vn, H0, H1, H2, H3) do { \
        GRP(N = MFMA32(ka[kn], as_frag(qa[Q]), f32x16{}), H0, X, 0); \
        GRP(N = MFMA32(kb[kn], as_frag(qbv[Q]), N), H1, X, 4); \
        GRP(O = pv_mma<0>(v0[vn], P, O, idx), H2, X, 8); \
        GRP(O = pv_mma<1>(v1[vn], P, O, idx), H3, X, 12); } while (0)
#define RKA(n, T) ka[n] = lds128(pka + (T) * KVT_B)
#define RKB(n, T) kb[n] = lds128(pka + (T) * KVT_B + 1024)
#define RV0(n, T) v0[n] = lds128(pv + (T) * KVT_B)
#define RV1(n, T) v1[n] = lds128(pv + (T) * KVT_B + 42 * 16)
#define LGKM0 __builtin_amdgcn_s_waitcnt(0xC07F)
#define NOP (void)0
    unsigned nx = KVG_B, gv = 0, need = 2;
    lptr pvn;
#define SPIN(x) asm volatile("" : "+s"(x))
#define GROUP(WRAP) do { \
        u32x2 full; unsigned fm, sfull; \
        SLOT(s0, s1, 0, 1, s3, oB, 1, LGKM0, RKA(0, 2), RKB(0, 2), RV0(1, 1)); \
        SLOT(s1, s2, 1, 0, s0, oA, 0, RV1(1, 1), full = *(const volatile LAS u32x2*)(uintptr_t)afull, do { gv += 1; PIN(gv); } while (0), NOP); \
        SLOT(s2, s3, 1, 1, s1, oB, 0, LGKM0, RKA(1, 3), RKB(1, 3), RV0(0, 2)); \
        SLOT(s3, s0, 0, 0, s2, oA, 1, RV1(0, 2), do { fm = min(full[0], full[1]); PIN(fm); } while (0), do { sfull = __builtin_amdgcn_readfirstlane(fm); SPIN(sfull); } while (0), pka = lbase(oka + nx)); \
          \
        SLOT(s0, s1, 0, 1, s3, oB, 1, \
             do { if (__builtin_expect(sfull < need, 0)) { \
                      do { __builtin_amdgcn_s_sleep(1); full = *(const volatile LAS u32x2*)(uintptr_t)afull; } while ((unsigned)__builtin_amdgcn_readfirstlane(min(full[0], full[1])) < need); } \
                  asm volatile("" ::: "memory"); LGKM0; } while (0), \
             RKA(0, 0), RKB(0, 0), RV0(1, 3)); \
        SLOT(s1, s2, 1, 0, s0, oA, 0, RV1(1, 3), pvn = lbase(ov + nx), do { nx += KVG_B; SPIN(nx); } while (0), \
             do { if (WRAP) { nx = nx == 9 * KVG_B ? KV1 : nx; SPIN(nx); } } while (0)); \
        pv = pvn; \
        SLOT(s2, s3, 1, 1, s1, oB, 0, LGKM0, RKA(1, 1), RKB(1, 1), RV0(0, 0)); \
        SLOT(s3, s0, 0, 0, s2, oA, 1, RV1(0, 0), lds_put(afree, gv)  , do { need += 1; SPIN(need); } while (0), \
             do { if (WRAP) { nx = nx == KV1 + 4 * KVG_B ? KV1 : nx; SPIN(nx); } } while (0)); } while (0)
#pragma unroll 1
    do { GROUP(false); GROUP(true); } while (need < 18);
    oB = pv_mma<0>(v0[1], s3, oB, idx);
    oB = pv_mma<1>(v1[1], s3, oB, idx);
    __builtin_amdgcn_s_setprio(0);
    STAMP(1, 2);
    attn_finish(oA, wr, head, qt0);
    attn_finish(oB, wr, head, qt0 + 1);
    STAMP(1, 3);
}
DI void gate16(const u16* __restrict__ x, const u16* __restrict__ gw, unsigned lds0, int n) {
    const int lane = threadIdx.x & 63, c = lane & 15, q = lane >> 4, b = blockIdx.x, xcd = b & 7, i = b >> 3;
    YG_MAP;
    (void)eg;
    __builtin_amdgcn_s_setprio(3);
    const u16* xr = x + (size_t)((tb * 2 + (sp & 1)) * 64 + n * 16 + c) * DM + q * 8;
    f32x4 z = f32x4{};
#pragma unroll
    for (int ks = 0; ks < 20; ++ks) z = MFMA16(*(const bf16x8*)(gw + ks * 32 + q * 8), *(const bf16x8*)(xr + ks * 32), z);
    if (lane < 16) *(LAS float*)(uintptr_t)(lds0 + LDS_G + (n * 16 + lane) * 4) = 1.0f / (1.0f + __expf(-z[0]));
    __builtin_amdgcn_s_setprio(HPRIO);
}
DI void attn_produce(const u16* __restrict__ x, const u16* __restrict__ gw, const char* __restrict__ ws, unsigned lds0, int pw) {
    const int lane = threadIdx.x & 63;
    const int b = blockIdx.x, head = (b & 7) * 4 + (b >> 6);
    unsigned so[6], sd[6];
#pragma unroll
    for (int j = 0; j < 6; ++j) {
        const int cc = min(j * 64 + lane, 359), tl = pw * 2 + (cc >= 180), c = cc >= 180 ? cc - 180 : cc;
        const int v = c - 96, g = v / 21, d = v - g * 21;
        so[j] = c < 64 ? WS_KA + (unsigned)(head * 64 + tl) * 1024 + c * 16
              : c < 96 ? WS_KB + (unsigned)(head * 64 + tl) * 512 + (c - 64) * 16
                       : WS_VV + (unsigned)((tl * 4 + g) * VP + (d < 20 ? head * 20 + d : 640)) * 16;
        sd[j] = c < 64 ? 4096u : c < 96 ? 2048u : 4 * VV_T;
    }
    auto issue = [&](unsigned g) {
        const unsigned dst = lds0 + kv_off(g) + pw * 2 * KVT_B;
#pragma unroll
        for (int j = 0; j < 5; ++j) { dma16c(ws, so[j], dst + j * 1024); so[j] += sd[j]; }
        dma16_lo<40>(ws, so[5], dst + 5 * 1024); so[5] += sd[5];
    };
#pragma unroll
    for (int j = 0; j < 6; ++j) { PIN(so[j]); PIN(sd[j]); }
    bar();
    if (pw == 0) {
        const int xcd = b & 7, gl = b >> 6, grp = (xcd >> 1) * 3 + gl;
        if (lane < (gl != 3 ? 3 : 1)) __hip_atomic_fetch_add((gu32*)(ws + WS_FLAG + (lane ? 263 + (grp & 3) * 2 + lane : 272 + (xcd & 1)) * 128), 1u, RLX_AGENT);
        STAMP(1, 4);
        wait_count((gu32*)(ws + WS_FLAG + (264 + xcd) * 128), 0u, 48u);
    }
    bar_raw();
    issue(0); issue(1); issue(2);
    if (pw == 0 && lane < 4) lds_put(lds0 + KV_FREE + lane * 4, 0u);
    lds_put(lds0 + KV_FULL + pw * 4, 1u);
    wait_vm<12>(); bar();
#pragma unroll 1
    for (unsigned g = 3; g < 13; ++g) { issue(g); wait_vm<12>(); lds_put(lds0 + KV_FULL + pw * 4, g - 1); }
    wait_vm<0>(); lds_put(lds0 + KV_FULL + pw * 4, 13u);
    gate16(x, gw, lds0, pw);
#pragma unroll 1
    for (unsigned g = 13; g < 16; ++g) {
        wait_vm<0>(); lds_put(lds0 + KV_FULL + pw * 4, g);
        kv_wait_free(lds0, g - 3);
        issue(g);
    }
    wait_vm<0>(); lds_put(lds0 + KV_FULL + pw * 4, 17u);
}
DI void attn_helper(const u16* __restrict__ cs, const u16* __restrict__ ss, const u16* __restrict__ wo,
                    const u16* __restrict__ x, const u16* __restrict__ gw, const char* __restrict__ ws, unsigned lds0, int hw) {
    const int lane = threadIdx.x & 63;
    const int b = blockIdx.x, xcd = b & 7, i = b >> 3;
    YG_MAP;
    bar(); bar_raw(); bar_raw();
    gate16(x, gw, lds0, 2 + hw);
    kv_wait_free(lds0, H_START);
    const char* pfp = (const char*)(sp >= 2 ? ss : cs) + (size_t)(tb * 128 + eg * 32 + hw * 16) * 4096 + (sp & 1) * 2048;
#pragma unroll 1
    for (int n = 0; n < 16; ++n) { touch(pfp + n * 128, (lane >> 2) * 4096, lds0 + LDS_SCR); __builtin_amdgcn_s_sleep(H_PACE); }
    touch((const char*)wo + ((tb & 7) * 80 + hw * 40) * 1280 + (eg * 2 + (sp >> 1)) * 160, min(lane >> 1, 39) * 1280 + (lane & 1) * 156, lds0 + LDS_SCR);
    touch((const char*)wo + ((tb & 7) * 80 + hw * 40 + 32) * 1280 + (eg * 2 + (sp >> 1)) * 160, ((lane >> 1) & 7) * 1280 + (lane & 1) * 156, lds0 + LDS_SCR);
    wait_count((gu32*)(ws + WS_FLAG + (272 + (sp & 1)) * 128), 0u, 128u);
    kv_wait_free(lds0, 9);
#pragma unroll 1
    for (int n = 0; n < 6; ++n) { yg_issue(cs, ss, ws, lds0, hw * 2 + (n & 1), n >> 1); __builtin_amdgcn_s_sleep(4); }
}

DI u32x4 ld128c(rsrc_t r, unsigned voff) { return __builtin_amdgcn_raw_buffer_load_b128(r, voff, 0, 16); }
DI u32x2 ld64c(rsrc_t r, unsigned voff) { return __builtin_amdgcn_raw_buffer_load_b64(r, voff, 0, 16); }

DI void phase_tail(const u16* __restrict__ cs, const u16* __restrict__ ss, const u16* __restrict__ wo, char* __restrict__ ws, u16* __restrict__ out, unsigned lds0, int wid) {
    const int lane = threadIdx.x & 63, c = lane & 15, q = lane >> 4;
    const int b = blockIdx.x, xcd = b & 7, i = b >> 3;
    YG_MAP;
    const int sb = tb * 2 + (sp & 1), ng = eg * 2 + (sp >> 1);
    STAMP(3, 0);
    __builtin_amdgcn_s_setprio(0);
    if (wid >= 4) {
        const int lw = wid - 4;
        unsigned wv[5];
#pragma unroll
        for (int j = 0; j < 5; ++j) { const unsigned ci = (unsigned)(lw + 4 * j) * 64u + lane, row = (ci * 6554u) >> 16; wv[j] = row * 1280u + (ci - row * 10u) * 16u; PIN(wv[j]); }
        wait_vm<0>(); bar();
#pragma unroll 1
        for (int st = 3; st < 16; ++st) {
            yg_issue(cs, ss, ws, lds0, lw, st);
            wait_vm<18>(); bar_raw();
            if (st == 6 && threadIdx.x == 256) __hip_atomic_fetch_add((gu32*)(ws + WS_FLAG + (256 + (i & 7)) * 128), 1u, RLX_AGENT);
        }
#pragma unroll
        for (int j = 0; j < 25; ++j) {
            dma16((const char*)wo + ng * 160 + (j / 5) * 128 * 1280, wv[j % 5], lds0 + lw * 1024 + j * 4096);
            if (j == 8 || j == 17) { wait_vm<18>(); bar_raw(); }
        }
        wait_vm<0>(); bar_raw();
        wait_count((gu32*)(ws + WS_FLAG + ((tb * 4 + eg) * 4 + sp) * 128), 0u, 3u);
        const int t0 = (lw >> 1) * 3;
        const unsigned yb = WS_YP + (unsigned)(((tb * 4 + eg) * 4 + sp) * 4) * 10240u + (unsigned)((lw & 1) * 5 + t0) * 1024u + lane * 16;
        u32x4 yv[3][3];
#pragma unroll
        for (int t = 0; t < 3; ++t)
#pragma unroll
            for (int v = 0; v < 3; ++v) yv[v][t] = ld128c(ws_rsrc(ws), yb + ((sp + 1 + v) & 3) * 10240 + (t0 + t < 5 ? t : 0) * 1024);
        SBAR();
        bar_raw();
        LAS f32x4* yw = (LAS f32x4*)(uintptr_t)(lds0 + lw * 6144 + lane * 16);
#pragma unroll
        for (int t = 0; t < 3; ++t)
#pragma unroll
            for (int u = 0; u < 2; ++u)
                yw[(u * 3 + t) * 64] = f32x4{(bflo(yv[0][t][2 * u]) + bflo(yv[1][t][2 * u])) + bflo(yv[2][t][2 * u]),
                                             (bfhi(yv[0][t][2 * u]) + bfhi(yv[1][t][2 * u])) + bfhi(yv[2][t][2 * u]),
                                             (bflo(yv[0][t][2 * u + 1]) + bflo(yv[1][t][2 * u + 1])) + bflo(yv[2][t][2 * u + 1]),
                                             (bfhi(yv[0][t][2 * u + 1]) + bfhi(yv[1][t][2 * u + 1])) + bfhi(yv[2][t][2 * u + 1])};
        bar();
        return;
    }
    const rsrc_t wr = ws_rsrc(ws);
    const int rh = wid & 1, kk = wid >> 1;
    bf16x8 fo[5][2][2];
    {
        const int ch = wid >> 1;
        f32x4 acc[4][5];
#pragma unroll
        for (int u = 0; u < 4; ++u)
#pragma unroll
            for (int t = 0; t < 5; ++t) acc[u][t] = f32x4{};
        bf16x8 frE[2][4], fnE[2][5], frO[2][4], fnO[2][5];
        const unsigned af = lds0 + 16384 + ch * 10240 + lane * 16;
        const unsigned a0 = lds0 + (unsigned)(rh * 64 + c) * 128u + (unsigned)((q ^ (c >> 1)) * 16);
        auto step = [&](bf16x8 (&fr)[2][4], bf16x8 (&fn)[2][5], const bf16x8 (&pr)[2][4], const bf16x8 (&pn)[2][5], int st, bool ld, bool mm) {
            lptr p, r0, r1;
            if (ld) {
                bar();
                const unsigned so = (st & 3) * F_STG;
                p = lbase(af + so); r0 = lbase(a0 + so); r1 = lbase((a0 ^ 64u) + so);
            }
#pragma unroll
            for (int k = 0; k < 2; ++k)
#pragma unroll
                for (int t = 0; t < 5; ++t) {
                    if (ld) {
                        fn[k][t] = lds128(p + t * 2048 + k * 1024);
                        if (t < 4) fr[k][t] = lds128((k ? r1 : r0) + t * 2048);
                    }
                    SBAR();
                    if (mm) {
#pragma unroll
                        for (int u = 0; u < 4; ++u) acc[u][t] = MFMA16(pn[k][t], pr[k][u], acc[u][t]);
                    }
                    SBAR();
                }
        };
        gu32* ocnt = (gu32*)(ws + WS_FLAG + (256 + (sb >> 2)) * 128);
        unsigned oseen = 0;
        const int poll_at = wid == 0 ? 8 : 99;
        step(frE, fnE, frO, fnO, 0, true, false);
        STAMP(3, 1);
#pragma unroll 1
        for (int st = 0; st < 14; st += 2) {
            step(frO, fnO, frE, fnE, st + 1, true, true);
            if (st == 2) wait_vm<0>();
            if (st == 2) STAMP(3, 7);
            if (st == poll_at) poll1(oseen, ocnt);
            step(frE, fnE, frO, fnO, st + 2, true, true);
        }
        STAMP(3, 4);
        if (wid == 0) {
            asm volatile("s_waitcnt vmcnt(0)" : "+v"(oseen)::"memory");
            wait_count(ocnt, oseen, 32u);
        }
        step(frO, fnO, frE, fnE, 15, true, true);
        STAMP(3, 5);
        step(frE, fnE, frO, fnO, 16, false, true);
        STAMP(3, 2);
        const unsigned yp = WS_YP + (unsigned)(((tb * 4 + eg) * 4 + wid) * 4 + sp) * 10240u + lane * 16;
        u32x4 pk[2][5];
#pragma unroll
        for (int u = 0; u < 2; ++u)
#pragma unroll
            for (int t = 0; t < 5; ++t) {
                const f32x4 v0 = acc[2 * u][t], v1 = acc[2 * u + 1][t];
                pk[u][t] = u32x4{pk2(v0[0], v0[1]), pk2(v0[2], v0[3]), pk2(v1[0], v1[1]), pk2(v1[2], v1[3])};
            }
        if (wid != sp) {
#pragma unroll
            for (int u = 0; u < 2; ++u)
#pragma unroll
                for (int t = 0; t < 5; ++t) st128(wr, yp, (u * 5 + t) * 1024, pk[u][t]);
        } else {
#pragma unroll
            for (int u = 0; u < 2; ++u)
#pragma unroll
                for (int t = 0; t < 5; ++t) *(LAS u32x4*)(uintptr_t)(lds0 + (u ? F_RING : 102400) + t * 1024 + lane * 16) = pk[u][t];
        }
        SBAR();
        STAMP(3, 3);
        bar();
        wait_vm<0>();
        STAMP(2, 5);
        if (lane == 0 && wid != sp) __hip_atomic_fetch_add((gu32*)(ws + WS_FLAG + ((tb * 4 + eg) * 4 + wid) * 128), 1u, RLX_AGENT);
        const unsigned oa = WS_OA + (unsigned)((sb * 2 + rh) * NH + kk * 4) * 1024u + q * 512 + c * 16;
        const unsigned ob = WS_OB + (unsigned)((sb * 2 + rh) * NH + kk * 16) * 256u + q * 512 + c * 8;
#pragma unroll
        for (int j = 0; j < 4; ++j)
#pragma unroll
            for (int k = 0; k < 2; ++k)
#pragma unroll
                for (int u = 0; u < 2; ++u) fo[j][k][u] = as_frag(ld128c(wr, oa + j * 8192 + k * 2048 + u * 256));
#pragma unroll
        for (int k = 0; k < 2; ++k)
#pragma unroll
            for (int u = 0; u < 2; ++u) {
                const u32x2 a = ld64c(wr, ob + k * 2048 + u * 128), bq = ld64c(wr, ob + k * 2048 + u * 128 + 256);
                fo[4][k][u] = as_frag(u32x4{a[0], a[1], bq[0], bq[1]});
            }
        SBAR();
        STAMP(3, 6);
    }
    const int s0 = sb * 64 + rh * 32 + c;
    f32x4 acc[2][5];
#pragma unroll
    for (int t = 0; t < 5; ++t) { acc[0][t] = f32x4{}; acc[1][t] = f32x4{}; }
    STAMP(2, 0);
    {
        const unsigned wl = lds0 + (unsigned)(c >> 2) * 160u + (c & 3) * 8;
        const lptr p0 = lbase(wl + (unsigned)(kk * 80 + (q >> 1) * 20 + (q & 1) * 4) * 160u), p1 = lbase(wl + (unsigned)(kk * 80 + (q >> 1) * 20 + (q & 1) * 4) * 160u + 51200);
        const lptr p2 = lbase(wl + (unsigned)(kk * 320 + q * 40 + 16) * 160u);
        bf16x8 fn[2][5];
        auto wof = [&](int n, int t) {
            const lptr pp = n < 4 ? p0 + (n >> 1) * 25600 + (n & 1) * 6400 : n < 8 ? p1 + ((n - 4) >> 1) * 25600 + (n & 1) * 6400 : p2 + (n & 1) * 25600;
            const s16x4 lo = ldstr(pp + t * 32), hi = ldstr(pp + t * 32 + (n < 8 ? 8 : 20) * 160);
            return __builtin_bit_cast(bf16x8, __builtin_shufflevector(lo, hi, 0, 1, 2, 3, 4, 5, 6, 7));
        };
#define WOF(n, t) wof(n, t)
#pragma unroll
        for (int t = 0; t < 5; ++t) fn[0][t] = WOF(0, t);
#pragma unroll
        for (int n = 0; n < 10; ++n)
#pragma unroll
            for (int t = 0; t < 5; ++t) {
                if (n < 9) fn[(n + 1) & 1][t] = WOF(n + 1, t);
                SBAR();
                acc[0][t] = MFMA16(fn[n & 1][t], fo[n >> 1][n & 1][0], acc[0][t]);
                acc[1][t] = MFMA16(fn[n & 1][t], fo[n >> 1][n & 1][1], acc[1][t]);
                SBAR();
            }
    }
    STAMP(2, 1);
    const float g0 = *(const LAS float*)(uintptr_t)(lds0 + LDS_G + (rh * 32 + c) * 4), g1 = *(const LAS float*)(uintptr_t)(lds0 + LDS_G + (rh * 32 + 16 + c) * 4);
    const int t0 = kk * 3;
    f32x4 fin[2][3];
    {
        LAS f32x4* xw = (LAS f32x4*)(uintptr_t)(lds0 + 3 * F_STG + wid * 6144 + lane * 16);
        const LAS f32x4* xr = (const LAS f32x4*)(uintptr_t)(lds0 + 3 * F_STG + (wid ^ 2) * 6144 + lane * 16);
        unsigned mk = 0u - (unsigned)kk;
        PIN(mk);
#pragma unroll
        for (int u = 0; u < 2; ++u)
#pragma unroll
            for (int t = 0; t < 3; ++t) {
                const f32x4 lo = acc[u][t], hi = acc[u][t < 2 ? 3 + t : 4];
                f32x4 snd;
#pragma unroll
                for (int e = 0; e < 4; ++e) { fin[u][t][e] = bsel(mk, hi[e], lo[e]); snd[e] = bsel(mk, lo[e], hi[e]); }
                xw[(u * 3 + t) * 64] = snd;
            }
        bar();
#pragma unroll
        for (int u = 0; u < 2; ++u)
#pragma unroll
            for (int t = 0; t < 3; ++t) fin[u][t] = (fin[u][t] + xr[(u * 3 + t) * 64]) * (u ? g1 : g0);
    }
#pragma unroll
    for (int t = 0; t < 3; ++t) {
        const u32x4 y = *(const LAS u32x4*)(uintptr_t)(lds0 + (rh ? F_RING : 102400) + (t0 + t < 5 ? t0 + t : 0) * 1024 + lane * 16);
#pragma unroll
        for (int u = 0; u < 2; ++u) fin[u][t] += f32x4{bflo(y[2 * u]), bfhi(y[2 * u]), bflo(y[2 * u + 1]), bfhi(y[2 * u + 1])} * (1.0f - (u ? g1 : g0));
    }
    bar_raw();
    STAMP(2, 2);
    const LAS f32x4* yr = (const LAS f32x4*)(uintptr_t)(lds0 + wid * 6144 + lane * 16);
    u16* op = out + (size_t)s0 * DM + ng * 80 + t0 * 16 + q * 4;
#pragma unroll
    for (int t = 0; t < 3; ++t) {
        if (t0 + t < 5) {
#pragma unroll
            for (int u = 0; u < 2; ++u) {
                const f32x4 v = fin[u][t] + yr[(u * 3 + t) * 64] * (1.0f - (u ? g1 : g0));
                *(u32x2*)(op + (size_t)u * 16 * DM + t * 16) = u32x2{pk2(v[0], v[1]), pk2(v[2], v[3])};
            }
        }
    }
    STAMP_END(2);
}

constexpr int LDS_B = LDS_SCR + 256;

__global__ __launch_bounds__(512) void k_all(const u16* __restrict__ x, const u16* __restrict__ wqkv, const u16* __restrict__ cd,
                                             const u16* __restrict__ sd, char* __restrict__ ws, const u16* __restrict__ wo, const u16* __restrict__ gw,
                                             const u16* __restrict__ cs, const u16* __restrict__ ss, u16* __restrict__ out) {
    extern __shared__ __attribute__((aligned(16))) char smem[];
    const unsigned lds0 = (unsigned)(uintptr_t)smem;
    const int wid = __builtin_amdgcn_readfirstlane(threadIdx.x >> 6);
    const int b = blockIdx.x, xcd = b & 7, i = b >> 3;
    const int rb = (xcd & 1) * 8 + (i & 7), cq = xcd >> 1, gl = i >> 3;
    asm volatile("" ::"s"(x), "s"(wqkv), "s"(cd), "s"(sd));
    if (wid < 4) {
        STAMP(0, 0);
        if (threadIdx.x == 64) {
            const unsigned n = __hip_atomic_load((gu32*)(ws + WS_RSP + b * 4), RLX_AGENT) + 1u;
            __hip_atomic_store((gu32*)(ws + WS_REQ + b * 4), n, RLX_AGENT);
            *(volatile LAS unsigned*)(uintptr_t)(lds0 + P_RING) = n;
        }
        if (b == LEADER) {
            const unsigned tl = threadIdx.x;
            st128(ws_rsrc(ws), (tl * VP + 640) * 16, WS_VV, u32x4{ONES, ONES, ONES, ONES});
            st32(ws_rsrc(ws), tl * 128, WS_FLAG, 0u);
            if (tl < 18) st32(ws_rsrc(ws), (256 + tl) * 128, WS_FLAG, 0u);
            st32(ws_rsrc(ws), tl * 4, WS_ACK, 0u);
            wait_vm<0>();
        }
        asm volatile("s_waitcnt lgkmcnt(0)" ::: "memory");
        if (gl == 3) proj_mma<2>(ws, rb, cq, lds0, wid);
        else if (cq * 3 + gl < 8) proj_mma<0>(ws, rb, cq * 3 + gl, lds0, wid);
        else proj_mma<1>(ws, rb, cq * 3 + gl, lds0, wid);
        wait_vm<0>();
        STAMP(0, 4);
    } else proj_load(x, wqkv, cd, sd, ws, rb, cq, gl, lds0, wid - 4);
    if (wid < 4) phase_attn(ws, lds0, wid);
    else if (wid < 6) attn_produce(x, gw, ws, lds0, wid - 4);
    else attn_helper(cs, ss, wo, x, gw, ws, lds0, wid - 6);
    phase_tail(cs, ss, wo, ws, out, lds0, wid);
    STAMP_FLUSH(0); STAMP_FLUSH(1);
}

extern "C" void kernel_launch(void* const* d_in, const int* in_sizes, int n_in, void* d_out, int out_size, void* d_ws, size_t ws_size,
                              hipStream_t stream) {
    const u16* x = (const u16*)d_in[0];
    const u16* wqkv = (const u16*)d_in[1];
    const u16* wo = (const u16*)d_in[2];
    const u16* cs = (const u16*)d_in[4];
    const u16* ss = (const u16*)d_in[5];
    const u16* cd = (const u16*)d_in[6];
    const u16* sd = (const u16*)d_in[7];
    const u16* gw = (const u16*)d_in[8];
    char* ws = (char*)d_ws;
    (void)hipFuncSetAttribute((const void*)k_all, hipFuncAttributeMaxDynamicSharedMemorySize, LDS_B);
    k_all<<<256, 512, LDS_B, stream>>>(x, wqkv, cd, sd, ws, wo, gw, cs, ss, (u16*)d_out);
}
```

```cpp
#define SIGFIRST
#include <hip/hip_runtime.h>
#include <hip/hip_bf16.h>
#include <stdint.h>

typedef unsigned short u16;
typedef __attribute__((ext_vector_type(8))) __bf16 bf16x8;
typedef __attribute__((ext_vector_type(4))) short s16x4;
typedef __attribute__((ext_vector_type(16))) float f32x16;
typedef __attribute__((ext_vector_type(4))) float f32x4;
typedef __attribute__((ext_vector_type(4))) unsigned u32x4;
typedef __attribute__((ext_vector_type(2))) unsigned u32x2;
#define LAS __attribute__((address_space(3)))
#define DI __device__ __forceinline__
typedef const LAS char* lptr;

constexpr int SEQ = 2048, DM = 640, NH = 32;
constexpr int VP = 648;
constexpr unsigned WS_QA = 0;
constexpr unsigned WS_KA = WS_QA + NH * 64 * 1024;
constexpr unsigned WS_QB = WS_KA + NH * 64 * 1024;
constexpr unsigned WS_KB = WS_QB + NH * 64 * 512;
constexpr unsigned WS_VV = WS_KB + NH * 64 * 512;
constexpr unsigned WS_AB = WS_VV + 64 * 4 * VP * 16;
constexpr unsigned WS_YP = WS_AB + 40 * 128 * 1024;
constexpr unsigned WS_OA = WS_YP + 4 * 64 * 40 * 1024;
constexpr unsigned WS_OB = WS_OA + 64 * NH * 1024;
constexpr unsigned WS_FLAG = WS_OB + 64 * NH * 256;
constexpr unsigned WS_REQ = WS_FLAG + 274 * 128;
constexpr unsigned WS_RSP = WS_REQ + 1024;
constexpr unsigned WS_ACK = WS_RSP + 1024;
constexpr unsigned WS_END = WS_ACK + 1024;
constexpr float QSCALE = 0.22360679774997896f * 1.4426950408889634f;
#define ST_AUX 16

#define STAMP(k, i) do {} while (0)
#define STAMP_END(k) do {} while (0)
#define STAMP_FLUSH(kk) do {} while (0)
#define MFMA32(a, b, c) __builtin_amdgcn_mfma_f32_32x32x16_bf16((a), (b), (c), 0, 0, 0)
#define MFMA16(a, b, c) __builtin_amdgcn_mfma_f32_16x16x32_bf16((a), (b), (c), 0, 0, 0)
#define SBAR() __builtin_amdgcn_sched_barrier(0)
#define PIN(x) asm volatile("" : "+v"(x))
#define KEEP(x) asm volatile("" ::"v"(x))

typedef __attribute__((ext_vector_type(2))) __bf16 bf16x2;
typedef __attribute__((ext_vector_type(2))) float f32x2;
DI unsigned pk2(float lo, float hi) {
    f32x2 v = {lo, hi};
    return __builtin_bit_cast(unsigned, __builtin_convertvector(v, bf16x2));
}
DI float bsel(unsigned m, float a, float b) { return __uint_as_float((__float_as_uint(a) & m) | (__float_as_uint(b) & ~m)); }
DI float bflo(unsigned v) { return __uint_as_float(v << 16); }
DI float bfhi(unsigned v) { return __uint_as_float(v & 0xFFFF0000u); }
DI void dma16(const void* sbase, unsigned voff, unsigned lds) {
    asm volatile("s_mov_b32 m0, %2\n\ts_nop 0\n\tglobal_load_lds_dwordx4 %0, %1" ::"v"(voff), "s"(sbase), "s"(lds) : "memory");
}
DI void dma16c(const void* sbase, unsigned voff, unsigned lds) {
    asm volatile("s_mov_b32 m0, %2\n\ts_nop 0\n\tglobal_load_lds_dwordx4 %0, %1 sc1" ::"v"(voff), "s"(sbase), "s"(lds) : "memory");
}
template <int N> DI void dma16_lo(const void* sbase, unsigned voff, unsigned lds) {
    unsigned long long sv;
    asm volatile("s_mov_b32 m0, %3\n\ts_mov_b64 %0, exec\n\ts_mov_b64 exec, %4\n\tglobal_load_lds_dwordx4 %1, %2 sc1\n\ts_mov_b64 exec, %0"
                 : "=&s"(sv) : "v"(voff), "s"(sbase), "s"(lds), "s"((1ull << N) - 1) : "memory");
}
DI void touch(const void* sbase, unsigned voff, unsigned lds) {
    asm volatile("s_mov_b32 m0, %2\n\ts_nop 0\n\tglobal_load_lds_dword %0, %1" ::"v"(voff), "s"(sbase), "s"(lds) : "memory");
}
template <int N> DI void wait_vm() { asm volatile("s_waitcnt vmcnt(%0)" ::"i"(N) : "memory"); }
DI void bar() { asm volatile("s_waitcnt lgkmcnt(0)\n\ts_barrier" ::: "memory"); }
DI void bar_raw() { asm volatile("s_barrier" ::: "memory"); }
DI lptr lbase(unsigned a) { asm volatile("" : "+v"(a)); return (lptr)(uintptr_t)a; }
DI bf16x8 lds128(lptr p) { return *(const LAS bf16x8*)p; }
DI u32x2 lds64(lptr p) { return *(const LAS u32x2*)p; }
DI s16x4 ldstr(lptr p) {
    typedef short v4i16_t __attribute__((ext_vector_type(4)));
    return __builtin_bit_cast(s16x4, __builtin_amdgcn_ds_read_tr16_b64_v4i16((LAS v4i16_t*)p));
}
DI u32x4 pack8(const f32x16& x, int s, float sc) {
    u32x4 p;
    p[0] = pk2(x[8 * s + 0] * sc, x[8 * s + 1] * sc);
    p[1] = pk2(x[8 * s + 2] * sc, x[8 * s + 3] * sc);
    p[2] = pk2(x[8 * s + 4] * sc, x[8 * s + 5] * sc);
    p[3] = pk2(x[8 * s + 6] * sc, x[8 * s + 7] * sc);
    return p;
}
typedef __amdgpu_buffer_rsrc_t rsrc_t;
DI rsrc_t ws_rsrc(const void* ws) { return __builtin_amdgcn_make_buffer_rsrc((void*)ws, (short)0, (int)WS_END, 0x00020000); }
#define K1_AUX 16
DI void st128p(rsrc_t r, unsigned voff, unsigned soff, u32x4 v) { __builtin_amdgcn_raw_buffer_store_b128(v, r, voff + soff, 0, K1_AUX); }
DI void st128(rsrc_t r, unsigned voff, unsigned soff, u32x4 v) { __builtin_amdgcn_raw_buffer_store_b128(v, r, voff + soff, 0, ST_AUX); }
DI void st64(rsrc_t r, unsigned voff, unsigned soff, u32x2 v) { __builtin_amdgcn_raw_buffer_store_b64(v, r, voff, soff, ST_AUX); }
DI void st32(rsrc_t r, unsigned voff, unsigned soff, unsigned v) { __builtin_amdgcn_raw_buffer_store_b32(v, r, voff, soff, ST_AUX); }
#define LD_AUX 16
DI u32x4 ld128(rsrc_t r, unsigned voff) { return __builtin_amdgcn_raw_buffer_load_b128(r, voff, 0, LD_AUX); }
DI bf16x8 as_frag(u32x4 v) { return __builtin_bit_cast(bf16x8, v); }
typedef __attribute__((ext_vector_type(16))) __bf16 bf16x16;
template <int HI> DI f32x16 pv_mma(bf16x8 v, const f32x16& p, const f32x16& o, int idx) {
    const auto ph = __builtin_shufflevector(p, p, 8 * HI, 8 * HI + 1, 8 * HI + 2, 8 * HI + 3, 8 * HI + 4, 8 * HI + 5, 8 * HI + 6, 8 * HI + 7);
    return __builtin_amdgcn_smfmac_f32_32x32x32_bf16(v, __builtin_bit_cast(bf16x16, ph), o, idx, 0, 0);
}
DI u32x4 pack8n(const f32x16& x, int s, float sc) {
    const u32x4 p = pack8(x, s, sc);
    const auto r0 = __builtin_amdgcn_permlane32_swap(p[0], p[2], false, false);
    const auto r1 = __builtin_amdgcn_permlane32_swap(p[1], p[3], false, false);
    return u32x4{r0[0], r1[0], r0[1], r1[1]};
}

#define POLL_SLEEP 4
typedef __attribute__((address_space(1))) unsigned gu32;
#define RLX_AGENT __ATOMIC_RELAXED, __HIP_MEMORY_SCOPE_AGENT
DI void poll1(unsigned& v, gu32* c) {
    unsigned long long sv;
    asm volatile("s_mov_b64 %1, exec\n\ts_mov_b64 exec, 1\n\tglobal_load_dword %0, %2, %3 sc1\n\ts_mov_b64 exec, %1" : "+v"(v), "=&s"(sv) : "v"(0u), "s"(c) : "memory");
}
DI void wait_count(gu32* c, unsigned seen, unsigned want) {
    for (unsigned spins = 0; (unsigned)__builtin_amdgcn_readfirstlane(seen) < want && spins < (1u << 17); ++spins) {
        __builtin_amdgcn_s_sleep(POLL_SLEEP);
        poll1(seen, c);
        asm volatile("s_waitcnt vmcnt(0)" : "+v"(seen)::"memory");
    }
    asm volatile("" ::: "memory");
}

constexpr int LEADER = 255;
DI void echo_round(u32x4& v, const char* ws, bool put) {
    const unsigned o = (threadIdx.x & 63) * 16;
    if (put) asm volatile("global_store_dwordx4 %0, %1, %2 sc1\n\ts_nop 1" ::"v"(o), "v"(v), "s"(ws + WS_RSP) : "memory");
    asm volatile("global_load_dwordx4 %0, %1, %2 sc1" : "+v"(v) : "v"(o), "s"(ws + WS_REQ) : "memory");
}

constexpr int P_STG = 45056;
constexpr int P_RING = 3 * P_STG;
#define EB0 1

DI void store_ab(const f32x16& acc, int dt, int stile, rsrc_t ws, int lane) {
    const float sc = dt >= 20 ? -1.f : 1.f;
    const int nt = dt >= 20 ? dt - 20 : dt;
    const int ks = (dt >= 20 ? 64 : 0) + stile;
    const unsigned dst = WS_AB + (unsigned)((nt * 2 + ((lane >> 4) & 1)) * 128 + ks) * 1024u + (unsigned)((lane >> 5) * 256 + (lane & 15) * 16);
#pragma unroll
    for (int s = 0; s < 2; ++s) st128p(ws, dst, s * 512, pack8n(acc, s, sc));
}

DI void proj_load(const u16* __restrict__ x, const u16* __restrict__ wqkv, const u16* __restrict__ cd, const u16* __restrict__ sd,
                  const char* __restrict__ ws, int rb, int cq, int gl, unsigned lds0, int wid) {
    const int lane = threadIdx.x & 63;
    constexpr int NDMA = 10;
    const bool pa = gl != 3;
    const unsigned vrow = (unsigned)(lane >> 3) * 1280u + (unsigned)(((lane & 7) ^ ((4 * (wid & 1) + (lane >> 4)) & 7)) * 16);
    const char* xb = (const char*)x + ((size_t)rb * 128 + wid * 8) * 1280;
    int st = 3 * ((rb + gl) % 3);
    unsigned slot = lds0 + wid * 1024;
#pragma unroll
    for (int j = 0; j < 4; ++j) dma16(xb + j * 40960 + st * 128, vrow, slot + j * 4096);
    const char* wb[7];
    unsigned vo[5];
    const int dt0 = pa ? 28 + cq * 3 + gl : 7 * cq;
#pragma unroll
    for (int j = 0; j < 7; ++j) {
        const int dt = dt0 + j;
        const char* d = (const char*)(dt >= 20 ? sd : cd) + ((dt >= 20 ? dt - 20 : dt) * 32 + wid * 8) * 1280;
        wb[j] = pa && j ? (const char*)wqkv + (cq * 3 + gl) * 320 : d;
    }
#pragma unroll
    for (int j = 0; j < 5; ++j) { const unsigned i = (wid + 4 * j) * 64 + lane, q = (i * 3277u) >> 16; vo[j] = pa ? q * 3840u + (i - q * 20u) * 16u : vrow; }
    const int wstep = pa ? 64 * 3840 : 128;
    auto issue = [&](bool with_x) {
        if (with_x) {
#pragma unroll
            for (int j = 0; j < 4; ++j) dma16(xb + j * 40960 + st * 128, vrow, slot + j * 4096);
        }
        if (!pa) dma16(wb[6] + st * 128, vrow, slot + 40960);
        dma16(wb[0] + st * 128, vrow, slot + 16384);
#pragma unroll
        for (int j = 0; j < 5; ++j) dma16(wb[1 + j] + st * wstep, vo[j], slot + 20480 + j * 4096);
        slot = slot + P_STG == lds0 + wid * 1024 + P_RING ? lds0 + wid * 1024 : slot + P_STG;
        st = st == 9 ? 0 : st + 1;
    };
    const bool leader = blockIdx.x == LEADER && wid == 3;
    u32x4 rq = {0u, 0u, 0u, 0u};
    unsigned back = 0;
    STAMP(0, 5);
#pragma unroll 1
    for (int n = 0; n < 3; ++n) {
        issue(n > 0);
        if (n == 0) STAMP(0, 6);
        if (n == EB0) { wait_vm<EB0 * NDMA>(); bar_raw(); }
    }
    STAMP(0, 7);
    wait_vm<NDMA>(); bar_raw(); bar_raw();
#pragma unroll 1
    for (int n = 3; n < 10; ++n) {
        if (leader) echo_round(rq, ws, n > 3);
        if (wid == 0 && n == 9) poll1(back, (gu32*)(ws + WS_RSP + blockIdx.x * 4));
        issue(true); wait_vm<NDMA>(); bar_raw(); bar_raw();
    }
    wait_vm<0>();
    bar_raw();
    bool acked = wid != 0;
    const unsigned want = __builtin_amdgcn_readfirstlane(*(const volatile LAS unsigned*)(uintptr_t)(lds0 + P_RING));
    PIN(back);
    if (!acked && (unsigned)__builtin_amdgcn_readfirstlane(back) == want) {
        if (lane == 0) __hip_atomic_store((gu32*)(ws + WS_ACK + blockIdx.x * 4), 1u, RLX_AGENT);
        acked = true;
    }
    bar_raw(); bar_raw(); bar_raw();
    if (!acked) {
        for (unsigned spins = 0; spins < (1u << 17) && (unsigned)__builtin_amdgcn_readfirstlane(back) != want; ++spins) {
            __builtin_amdgcn_s_sleep(4);
            poll1(back, (gu32*)(ws + WS_RSP + blockIdx.x * 4));
            asm volatile("s_waitcnt vmcnt(0)" : "+v"(back)::"memory");
        }
        if (lane == 0) __hip_atomic_store((gu32*)(ws + WS_ACK + blockIdx.x * 4), 1u, RLX_AGENT);
    }
    if (leader) {
        for (unsigned spins = 0; spins < (1u << 17); ++spins) {
            echo_round(rq, ws, true);
            u32x4 ak;
            asm volatile("global_load_dwordx4 %0, %2, %3 sc1\n\ts_waitcnt vmcnt(0)" : "=&v"(ak), "+v"(rq) : "v"(lane * 16), "s"(ws + WS_ACK) : "memory");
            if (__all((ak[0] & ak[1] & ak[2] & ak[3]) == 1u)) break;
            __builtin_amdgcn_s_sleep(4);
        }
    }
}

template <int PATH>
DI void proj_mma(char* __restrict__ ws, int rb, int grp, unsigned lds0, int wid ) {
    const int lane = threadIdx.x & 63, r = lane & 31, h = lane >> 5;
    constexpr bool PATH_A = PATH < 2;
    constexpr int NW = PATH_A ? 5 : 0, ND = PATH_A ? 1 : 7, NT = NW + ND;
    const int dt0 = PATH_A ? 28 + grp : 7 * grp;
    const unsigned sw = lds0 + (unsigned)r * 128u + (unsigned)((h ^ ((r >> 1) & 7)) * 16);
    const unsigned awo = lds0 + 20480u + (unsigned)(8 * h + ((lane & 15) >> 2)) * 320u + (unsigned)(((lane >> 4) & 1) * 32 + (lane & 3) * 8);
    f32x16 acc[NT];
#pragma unroll
    for (int t = 0; t < NT; ++t) acc[t] = f32x16{};
    bf16x8 fx[2], fw[2][NT];
    lptr pr, px, pw;
    auto setp = [&](unsigned so, int ks) {
        pr = lbase((sw ^ (unsigned)(ks * 32)) + so);
        px = lbase((sw ^ (unsigned)(ks * 32)) + so + wid * 4096);
        if (PATH_A && ks == 0) pw = lbase(awo + so);
    };
    auto rd = [&](int ks, int b, int t) {
        if (t == 0) fx[b] = lds128(px);
        if (t < ND) fw[b][t] = lds128(pr + 16384 + t * 4096);
        else {
            s16x4 lo = ldstr(pw + ks * 16 * 320 + (t - ND) * 64), hi = ldstr(pw + ks * 16 * 320 + 4 * 320 + (t - ND) * 64);
            fw[b][t] = __builtin_bit_cast(bf16x8, __builtin_shufflevector(lo, hi, 0, 1, 2, 3, 4, 5, 6, 7));
        }
    };
    auto mm = [&](int b, int t) {
        if (PATH == 0 && t >= ND) acc[t] = MFMA32(fw[b][t], fx[b], acc[t]);
        else acc[t] = MFMA32(fx[b], fw[b][t], acc[t]);
    };
    unsigned so = 0;
    setp(0, 0);
#pragma unroll
    for (int t = 0; t < NT; ++t) PIN(acc[t]);
    bar_raw();
    STAMP(0, 1);
#pragma unroll
    for (int t = 0; t < NT; ++t) rd(0, 0, t);
    SBAR();
#pragma unroll 1
    for (int st = 0; st < 10; ++st) {
#pragma unroll
        for (int ks = 0; ks < 4; ++ks) {
            if (ks == 3) { bar_raw(); so = so == 2 * P_STG ? 0 : so + P_STG; }
            setp(so, (ks + 1) & 3);
#pragma unroll
            for (int t = 0; t < NT; ++t) { rd((ks + 1) & 3, (ks + 1) & 1, t); SBAR(); mm(ks & 1, t); SBAR(); }
        }
        bar_raw();
    }
    STAMP(0, 2);
    const rsrc_t wr = ws_rsrc(ws);
    const int stile = rb * 4 + wid;
    if (PATH == 0) {
        const float sc = grp < 4 ? QSCALE : 1.f;
        const unsigned t0 = (unsigned)(grp * 8 * 64 + stile);
        const unsigned va = WS_QA + t0 * 1024 + lane * 16, vb = WS_QB + (t0 + h * 64) * 512 + r * 16;
#pragma unroll
        for (int m = 0; m < 4; ++m) {
            u32x2 q[5];
#pragma unroll
            for (int i = 0; i < 5; ++i) {
                const int p = 5 * m + i, t = ND + (p >> 2), g = p & 3;
                q[i][0] = pk2(acc[t][4 * g] * sc, acc[t][4 * g + 1] * sc);
                q[i][1] = pk2(acc[t][4 * g + 2] * sc, acc[t][4 * g + 3] * sc);
            }
            st128p(wr, va, (2 * m) * 64 * 1024, u32x4{q[0][0], q[0][1], q[1][0], q[1][1]});
            st128p(wr, va, (2 * m + 1) * 64 * 1024, u32x4{q[3][0], q[3][1], q[4][0], q[4][1]});
            st128p(wr, vb, (2 * m) * 64 * 512, u32x4{q[2][0], q[2][1], 0x00003F80u, 0u});
        }
    } else if (PATH == 1) {
        const unsigned vv = WS_VV + (unsigned)((stile * 4 + h) * VP + (grp - 8) * 160 + r) * 16;
#pragma unroll
        for (int t = 0; t < 5; ++t) {
            st128p(wr, vv, t * 512, pack8n(acc[ND + t], 0, 1.f));
            st128p(wr, vv, t * 512 + 2 * VP * 16, pack8n(acc[ND + t], 1, 1.f));
        }
    }
#pragma unroll
    for (int t = 0; t < ND; ++t) store_ab(acc[t], dt0 + t, stile, wr, lane);
    STAMP(0, 3);
}
#define YG_MAP const int tb = (xcd >> 2) * 8 + (i & 7), eg = i >> 3, sp = xcd & 3
constexpr int F_STG = 36864;
constexpr int F_RING = 4 * F_STG;
DI void yg_issue(const u16* __restrict__ cs, const u16* __restrict__ ss, const char* __restrict__ ws, unsigned lds0, int lw, int st) {
    const int lane = threadIdx.x & 63;
    const int b = blockIdx.x, xcd = b & 7, i = b >> 3;
    YG_MAP;
    const unsigned vrow = (unsigned)(lane >> 3) * 4096u + (unsigned)(((lane & 7) ^ ((4 * (lw & 1) + (lane >> 4)) & 7)) * 16);
    const char* rp = (const char*)(sp >= 2 ? ss : cs) + (size_t)(tb * 128 + lw * 8) * 4096 + (sp & 1) * 2048;
    const char* ab = ws + WS_AB + (size_t)((eg * 10 + (lw >> 1)) * 128 + sp * 32 + (lw & 1)) * 1024;
    const unsigned d = lds0 + lw * 1024 + (st & 3) * F_STG;
#pragma unroll
    for (int j = 0; j < 4; ++j) dma16(rp + j * 32 * 4096 + st * 128, vrow, d + j * 4096);
#pragma unroll
    for (int j = 0; j < 5; ++j) dma16c(ab + (j * 256 + st * 2) * 1024, lane * 16, d + 16384 + j * 4096);
}

DI float xhalf_max(float v) {
    auto rr = __builtin_amdgcn_permlane32_swap(__float_as_uint(v), __float_as_uint(v), false, false);
    return fmaxf(__uint_as_float(rr[0]), __uint_as_float(rr[1]));
}
DI float max16(const f32x16& s) {
    float a = fmaxf(fmaxf(s[0], s[1]), s[2]), b = fmaxf(fmaxf(s[3], s[4]), s[5]);
    a = fmaxf(fmaxf(a, s[6]), s[7]); b = fmaxf(fmaxf(b, s[8]), s[9]);
    a = fmaxf(fmaxf(a, s[10]), s[11]); b = fmaxf(fmaxf(b, s[12]), s[13]);
    a = fmaxf(fmaxf(a, s[14]), s[15]);
    return fmaxf(a, b);
}
#define EX2(v) __builtin_amdgcn_exp2f(v)
constexpr unsigned VV_T = 4 * VP * 16, VV_S = 2 * VP * 16;
constexpr unsigned ONES = 0x3F803F80u;

DI u32x4 fix_v(u32x4 v, int r) {
#pragma unroll
    for (int i = 0; i < 4; ++i) v[i] = r < 20 ? v[i] : (r == 20 ? ONES : 0u);
    return v;
}
DI f32x16 attn_safe(rsrc_t wr, int head, u32x4 qa, u32x4 qb, int lane) {
    const int r = lane & 31, h = lane >> 5;
    qb[2] = 0u;
    f32x16 o = f32x16{};
    float m = -INFINITY;
#pragma unroll 1
    for (int t = 0; t < 64; ++t) {
        const u32x4 ka = ld128(wr, WS_KA + (unsigned)(head * 64 + t) * 1024 + lane * 16);
        const u32x4 kb = ld128(wr, WS_KB + (unsigned)(head * 64 + t) * 512 + r * 16);
        const unsigned vp = WS_VV + (unsigned)((t * 4 + h) * VP + head * 20 + (r < 20 ? r : 0)) * 16;
        const u32x4 v0 = fix_v(ld128(wr, vp), r), v1 = fix_v(ld128(wr, vp + VV_S), r);
        f32x16 s = MFMA32(as_frag(ka), as_frag(qa), f32x16{});
        s = MFMA32(as_frag(kb), as_frag(qb), s);
        const float mn = fmaxf(m, xhalf_max(max16(s)));
        const float al = EX2(m - mn);
        m = mn;
#pragma unroll
        for (int i = 0; i < 16; ++i) { o[i] *= al; s[i] = EX2(s[i] - mn); }
        o = pv_mma<0>(as_frag(v0), s, o, (int)0xDDDDDDDD);
        o = pv_mma<1>(as_frag(v1), s, o, (int)0xDDDDDDDD);
    }
    return o;
}

#define H_PACE 8
#define H_START 2
DI void attn_finish(f32x16 o, rsrc_t wr, int head, int qt) {
    const int lane = threadIdx.x & 63, r = lane & 31, h = lane >> 5;
    auto rl = __builtin_amdgcn_permlane32_swap(__float_as_uint(o[8]), __float_as_uint(o[8]), false, false);
    float l = __uint_as_float(rl[1]);
    float z = 0.f;
#pragma unroll
    for (int k = 0; k < 12; ++k) z = __builtin_fmaf(o[k], 0.f, z);
    const bool ok = (z == 0.f) && (l >= 8.6736174e-19f) && (l <= 1.2676506e30f);
    if (__builtin_expect(!__all(ok), 0)) {
        const u32x4 q0 = ld128(wr, WS_QA + (unsigned)(head * 64 + qt) * 1024 + lane * 16);
        const u32x4 q1 = ld128(wr, h ? 0x80000000u : WS_QB + (unsigned)(head * 64 + qt) * 512 + r * 16);
        o = attn_safe(wr, head, q0, q1, lane);
        rl = __builtin_amdgcn_permlane32_swap(__float_as_uint(o[8]), __float_as_uint(o[8]), false, false);
        l = __uint_as_float(rl[1]);
    }
    const float inv = __builtin_amdgcn_rcpf(l);
    const unsigned ot = (unsigned)(qt * NH + head);
    st128(wr, lane * 16, WS_OA + ot * 1024, pack8(o, 0, inv));
    if (h == 0) st64(wr, r * 8, WS_OB + ot * 256, u32x2{pk2(o[8] * inv, o[9] * inv), pk2(o[10] * inv, o[11] * inv)});
}

#define HPRIO 3
#define KVSLEEP 8
constexpr int KVT_B = 2880, KVG_B = 4 * KVT_B, KV1 = 3 * F_STG;
constexpr int KV_FULL = KV1 + 4 * KVG_B;
constexpr int KV_FREE = KV_FULL + 16;
constexpr int LDS_G = KV_FREE + 16;
constexpr int LDS_SCR = LDS_G + 256;
DI unsigned kv_off(unsigned g) { return g < 9 ? g * KVG_B : KV1 + ((g - 9) & 3) * KVG_B; }
DI void kv_wait_free(unsigned lds0, unsigned n) {
    for (;;) {
        const u32x4 f = *(const volatile LAS u32x4*)(uintptr_t)(lds0 + KV_FREE);
        if (__builtin_amdgcn_readfirstlane(min(min(f[0], f[1]), min(f[2], f[3]))) >= n) break;
        __builtin_amdgcn_s_sleep(KVSLEEP);
    }
    asm volatile("" ::: "memory");
}
DI void lds_put(unsigned addr, unsigned v) { *(volatile LAS unsigned*)(uintptr_t)addr = v; }

DI void phase_attn(char* __restrict__ ws, unsigned lds0, int wid) {
    const int lane = threadIdx.x & 63, r = lane & 31, h = lane >> 5;
    const int b = blockIdx.x, head = (b & 7) * 4 + (b >> 6), qt0 = ((b >> 3) & 7) * 8 + wid * 2;
    const rsrc_t wr = ws_rsrc(ws);
    bar_raw();
    STAMP(1, 0);
    u32x4 qa[2], qbv[2];
#pragma unroll
    for (int i = 0; i < 2; ++i) {
        qa[i] = ld128(wr, WS_QA + (unsigned)(head * 64 + qt0 + i) * 1024 + lane * 16);
        qbv[i] = ld128(wr, h ? 0x80000000u : WS_QB + (unsigned)(head * 64 + qt0 + i) * 512 + r * 16);
    }
    const int rv = r <= 20 ? r : (r < 28 ? 20 : 16);
    const unsigned oka = lds0 + lane * 16, ov = lds0 + 1536u + (unsigned)(h * 21 + rv) * 16u;
    unsigned afull = lds0 + KV_FULL, afree = lds0 + KV_FREE + wid * 4;
    PIN(afull); PIN(afree);
    __builtin_amdgcn_s_setprio(2);
    bar_raw();
    bf16x8 ka[2], kb[2], v0[2], v1[2];
    lptr pka = lbase(oka), pv = lbase(ov);
#define LDK(n, T) do { ka[n] = lds128(pka + (T) * KVT_B); kb[n] = lds128(pka + (T) * KVT_B + 1024); SBAR(); } while (0)
#define LDV(n, T) do { v0[n] = lds128(pv + (T) * KVT_B); v1[n] = lds128(pv + (T) * KVT_B + 42 * 16); SBAR(); } while (0)
    LDK(0, 0); LDK(1, 1); LDV(0, 0);
#pragma unroll
    for (int i = 0; i < 2; ++i) {
        qbv[i][2] = 0u;
        f32x16 t = MFMA32(ka[0], as_frag(qa[i]), f32x16{});
        t = MFMA32(kb[0], as_frag(qbv[i]), t);
        const float m0 = xhalf_max(max16(t));
        qbv[i][2] = h ? 0u : (pk2(-m0, 0.f) & 0xFFFFu);
    }
    STAMP(1, 1);
    f32x16 oA = f32x16{}, oB = f32x16{}, s0, s1, s2, s3 = f32x16{};
    int idx = (int)0xDDDDDDDD;
    PIN(idx);
    v0[1] = ka[0]; v1[1] = ka[0];
    s0 = MFMA32(ka[0], as_frag(qa[0]), f32x16{});
    s0 = MFMA32(kb[0], as_frag(qbv[0]), s0);
    SBAR();
#define GRP(MF, H, S, i) do { MF; SBAR(); H; SBAR(); S[i] = EX2(S[i]); S[i + 1] = EX2(S[i + 1]); S[i + 2] = EX2(S[i + 2]); S[i + 3] = EX2(S[i + 3]); PIN(S); SBAR(); } while (0)
#define SLOT(X, N, kn, Q, P, O, vn, H0, H1, H2, H3) do { \
        GRP(N = MFMA32(ka[kn], as_frag(qa[Q]), f32x16{}), H0, X, 0); \
        GRP(N = MFMA32(kb[kn], as_frag(qbv[Q]), N), H1, X, 4); \
        GRP(O = pv_mma<0>(v0[vn], P, O, idx), H2, X, 8); \
        GRP(O = pv_mma<1>(v1[vn], P, O, idx), H3, X, 12); } while (0)
#define RKA(n, T) ka[n] = lds128(pka + (T) * KVT_B)
#define RKB(n, T) kb[n] = lds128(pka + (T) * KVT_B + 1024)
#define RV0(n, T) v0[n] = lds128(pv + (T) * KVT_B)
#define RV1(n, T) v1[n] = lds128(pv + (T) * KVT_B + 42 * 16)
#define LGKM0 __builtin_amdgcn_s_waitcnt(0xC07F)
#define NOP (void)0
    unsigned nx = KVG_B, gv = 0, need = 2;
    lptr pvn;
#define SPIN(x) asm volatile("" : "+s"(x))
#define GROUP(WRAP) do { \
        u32x2 full; unsigned fm, sfull; \
        SLOT(s0, s1, 0, 1, s3, oB, 1, LGKM0, RKA(0, 2), RKB(0, 2), RV0(1, 1)); \
        SLOT(s1, s2, 1, 0, s0, oA, 0, RV1(1, 1), full = *(const volatile LAS u32x2*)(uintptr_t)afull, do { gv += 1; PIN(gv); } while (0), NOP); \
        SLOT(s2, s3, 1, 1, s1, oB, 0, LGKM0, RKA(1, 3), RKB(1, 3), RV0(0, 2)); \
        SLOT(s3, s0, 0, 0, s2, oA, 1, RV1(0, 2), do { fm = min(full[0], full[1]); PIN(fm); } while (0), do { sfull = __builtin_amdgcn_readfirstlane(fm); SPIN(sfull); } while (0), pka = lbase(oka + nx)); \
          \
        SLOT(s0, s1, 0, 1, s3, oB, 1, \
             do { if (__builtin_expect(sfull < need, 0)) { \
                      do { __builtin_amdgcn_s_sleep(1); full = *(const volatile LAS u32x2*)(uintptr_t)afull; } while ((unsigned)__builtin_amdgcn_readfirstlane(min(full[0], full[1])) < need); } \
                  asm volatile("" ::: "memory"); LGKM0; } while (0), \
             RKA(0, 0), RKB(0, 0), RV0(1, 3)); \
        SLOT(s1, s2, 1, 0, s0, oA, 0, RV1(1, 3), pvn = lbase(ov + nx), do { nx += KVG_B; SPIN(nx); } while (0), \
             do { if (WRAP) { nx = nx == 9 * KVG_B ? KV1 : nx; SPIN(nx); } } while (0)); \
        pv = pvn; \
        SLOT(s2, s3, 1, 1, s1, oB, 0, LGKM0, RKA(1, 1), RKB(1, 1), RV0(0, 0)); \
        SLOT(s3, s0, 0, 0, s2, oA, 1, RV1(0, 0), lds_put(afree, gv)  , do { need += 1; SPIN(need); } while (0), \
             do { if (WRAP) { nx = nx == KV1 + 4 * KVG_B ? KV1 : nx; SPIN(nx); } } while (0)); } while (0)
#pragma unroll 1
    do { GROUP(false); GROUP(true); } while (need < 18);
    oB = pv_mma<0>(v0[1], s3, oB, idx);
    oB = pv_mma<1>(v1[1], s3, oB, idx);
    __builtin_amdgcn_s_setprio(0);
    STAMP(1, 2);
    attn_finish(oA, wr, head, qt0);
    attn_finish(oB, wr, head, qt0 + 1);
    STAMP(1, 3);
}
DI void gate16(const u16* __restrict__ x, const u16* __restrict__ gw, unsigned lds0, int n) {
    const int lane = threadIdx.x & 63, c = lane & 15, q = lane >> 4, b = blockIdx.x, xcd = b & 7, i = b >> 3;
    YG_MAP;
    (void)eg;
    __builtin_amdgcn_s_setprio(3);
    const u16* xr = x + (size_t)((tb * 2 + (sp & 1)) * 64 + n * 16 + c) * DM + q * 8;
    f32x4 z = f32x4{};
#pragma unroll
    for (int ks = 0; ks < 20; ++ks) z = MFMA16(*(const bf16x8*)(gw + ks * 32 + q * 8), *(const bf16x8*)(xr + ks * 32), z);
    if (lane < 16) *(LAS float*)(uintptr_t)(lds0 + LDS_G + (n * 16 + lane) * 4) = 1.0f / (1.0f + __expf(-z[0]));
    __builtin_amdgcn_s_setprio(HPRIO);
}
DI void attn_produce(const u16* __restrict__ x, const u16* __restrict__ gw, const char* __restrict__ ws, unsigned lds0, int pw) {
    const int lane = threadIdx.x & 63;
    const int b = blockIdx.x, head = (b & 7) * 4 + (b >> 6);
    unsigned so[6], sd[6];
#pragma unroll
    for (int j = 0; j < 6; ++j) {
        const int cc = min(j * 64 + lane, 359), tl = pw * 2 + (cc >= 180), c = cc >= 180 ? cc - 180 : cc;
        const int v = c - 96, g = v / 21, d = v - g * 21;
        so[j] = c < 64 ? WS_KA + (unsigned)(head * 64 + tl) * 1024 + c * 16
              : c < 96 ? WS_KB + (unsigned)(head * 64 + tl) * 512 + (c - 64) * 16
                       : WS_VV + (unsigned)((tl * 4 + g) * VP + (d < 20 ? head * 20 + d : 640)) * 16;
        sd[j] = c < 64 ? 4096u : c < 96 ? 2048u : 4 * VV_T;
    }
    auto issue = [&](unsigned g) {
        const unsigned dst = lds0 + kv_off(g) + pw * 2 * KVT_B;
#pragma unroll
        for (int j = 0; j < 5; ++j) { dma16c(ws, so[j], dst + j * 1024); so[j] += sd[j]; }
        dma16_lo<40>(ws, so[5], dst + 5 * 1024); so[5] += sd[5];
    };
#pragma unroll
    for (int j = 0; j < 6; ++j) { PIN(so[j]); PIN(sd[j]); }
    STAMP(1, 5);
    bar_raw();
    issue(0); issue(1); issue(2);
    if (pw == 0 && lane < 4) lds_put(lds0 + KV_FREE + lane * 4, 0u);
    lds_put(lds0 + KV_FULL + pw * 4, 1u);
    wait_vm<12>(); bar();
#pragma unroll 1
    for (unsigned g = 3; g < 13; ++g) { issue(g); wait_vm<12>(); lds_put(lds0 + KV_FULL + pw * 4, g - 1); }
    wait_vm<0>(); lds_put(lds0 + KV_FULL + pw * 4, 13u);
    gate16(x, gw, lds0, pw);
#pragma unroll 1
    for (unsigned g = 13; g < 16; ++g) {
        wait_vm<0>(); lds_put(lds0 + KV_FULL + pw * 4, g);
        kv_wait_free(lds0, g - 3);
        issue(g);
    }
    wait_vm<0>(); lds_put(lds0 + KV_FULL + pw * 4, 17u);
}
DI void attn_helper(const u16* __restrict__ cs, const u16* __restrict__ ss, const u16* __restrict__ wo,
                    const u16* __restrict__ x, const u16* __restrict__ gw, const char* __restrict__ ws, unsigned lds0, int hw) {
    const int lane = threadIdx.x & 63;
    const int b = blockIdx.x, xcd = b & 7, i = b >> 3;
    YG_MAP;
    STAMP(1, 5);
    bar_raw(); bar_raw();
    gate16(x, gw, lds0, 2 + hw);
    kv_wait_free(lds0, H_START);
    const char* pfp = (const char*)(sp >= 2 ? ss : cs) + (size_t)(tb * 128 + eg * 32 + hw * 16) * 4096 + (sp & 1) * 2048;
#pragma unroll 1
    for (int n = 0; n < 16; ++n) { touch(pfp + n * 128, (lane >> 2) * 4096, lds0 + LDS_SCR); __builtin_amdgcn_s_sleep(H_PACE); }
    touch((const char*)wo + ((tb & 7) * 80 + hw * 40) * 1280 + (eg * 2 + (sp >> 1)) * 160, min(lane >> 1, 39) * 1280 + (lane & 1) * 156, lds0 + LDS_SCR);
    touch((const char*)wo + ((tb & 7) * 80 + hw * 40 + 32) * 1280 + (eg * 2 + (sp >> 1)) * 160, ((lane >> 1) & 7) * 1280 + (lane & 1) * 156, lds0 + LDS_SCR);
    wait_count((gu32*)(ws + WS_FLAG + (272 + (sp & 1)) * 128), 0u, 128u);
    kv_wait_free(lds0, 9);
#pragma unroll 1
    for (int n = 0; n < 6; ++n) { yg_issue(cs, ss, ws, lds0, hw * 2 + (n & 1), n >> 1); __builtin_amdgcn_s_sleep(4); }
}

DI u32x4 ld128c(rsrc_t r, unsigned voff) { return __builtin_amdgcn_raw_buffer_load_b128(r, voff, 0, 16); }
DI u32x2 ld64c(rsrc_t r, unsigned voff) { return __builtin_amdgcn_raw_buffer_load_b64(r, voff, 0, 16); }

DI void phase_tail(const u16* __restrict__ cs, const u16* __restrict__ ss, const u16* __restrict__ wo, char* __restrict__ ws, u16* __restrict__ out, unsigned lds0, int wid) {
    const int lane = threadIdx.x & 63, c = lane & 15, q = lane >> 4;
    const int b = blockIdx.x, xcd = b & 7, i = b >> 3;
    YG_MAP;
    const int sb = tb * 2 + (sp & 1), ng = eg * 2 + (sp >> 1);
    STAMP(3, 0);
    __builtin_amdgcn_s_setprio(0);
    if (wid >= 4) {
        const int lw = wid - 4;
        unsigned wv[5];
#pragma unroll
        for (int j = 0; j < 5; ++j) { const unsigned ci = (unsigned)(lw + 4 * j) * 64u + lane, row = (ci * 6554u) >> 16; wv[j] = row * 1280u + (ci - row * 10u) * 16u; PIN(wv[j]); }
        wait_vm<0>(); bar();
#pragma unroll 1
        for (int st = 3; st < 16; ++st) {
            yg_issue(cs, ss, ws, lds0, lw, st);
            wait_vm<18>(); bar_raw();
            if (st == 6 && threadIdx.x == 256) __hip_atomic_fetch_add((gu32*)(ws + WS_FLAG + (256 + (i & 7)) * 128), 1u, RLX_AGENT);
        }
#pragma unroll
        for (int j = 0; j < 25; ++j) {
            dma16((const char*)wo + ng * 160 + (j / 5) * 128 * 1280, wv[j % 5], lds0 + lw * 1024 + j * 4096);
            if (j == 8 || j == 17) { wait_vm<18>(); bar_raw(); }
        }
        wait_vm<0>(); bar_raw();
        wait_count((gu32*)(ws + WS_FLAG + ((tb * 4 + eg) * 4 + sp) * 128), 0u, 3u);
        const int t0 = (lw >> 1) * 3;
        const unsigned yb = WS_YP + (unsigned)(((tb * 4 + eg) * 4 + sp) * 4) * 10240u + (unsigned)((lw & 1) * 5 + t0) * 1024u + lane * 16;
        u32x4 yv[3][3];
#pragma unroll
        for (int t = 0; t < 3; ++t)
#pragma unroll
            for (int v = 0; v < 3; ++v) yv[v][t] = ld128c(ws_rsrc(ws), yb + ((sp + 1 + v) & 3) * 10240 + (t0 + t < 5 ? t : 0) * 1024);
        SBAR();
        bar_raw();
        LAS f32x4* yw = (LAS f32x4*)(uintptr_t)(lds0 + lw * 6144 + lane * 16);
#pragma unroll
        for (int t = 0; t < 3; ++t)
#pragma unroll
            for (int u = 0; u < 2; ++u)
                yw[(u * 3 + t) * 64] = f32x4{(bflo(yv[0][t][2 * u]) + bflo(yv[1][t][2 * u])) + bflo(yv[2][t][2 * u]),
                                             (bfhi(yv[0][t][2 * u]) + bfhi(yv[1][t][2 * u])) + bfhi(yv[2][t][2 * u]),
                                             (bflo(yv[0][t][2 * u + 1]) + bflo(yv[1][t][2 * u + 1])) + bflo(yv[2][t][2 * u + 1]),
                                             (bfhi(yv[0][t][2 * u + 1]) + bfhi(yv[1][t][2 * u + 1])) + bfhi(yv[2][t][2 * u + 1])};
        bar();
        return;
    }
    const rsrc_t wr = ws_rsrc(ws);
    const int rh = wid & 1, kk = wid >> 1;
    bf16x8 fo[5][2][2];
    {
        const int ch = wid >> 1;
        f32x4 acc[4][5];
#pragma unroll
        for (int u = 0; u < 4; ++u)
#pragma unroll
            for (int t = 0; t < 5; ++t) acc[u][t] = f32x4{};
        bf16x8 frE[2][4], fnE[2][5], frO[2][4], fnO[2][5];
        const unsigned af = lds0 + 16384 + ch * 10240 + lane * 16;
        const unsigned a0 = lds0 + (unsigned)(rh * 64 + c) * 128u + (unsigned)((q ^ (c >> 1)) * 16);
        auto step = [&](bf16x8 (&fr)[2][4], bf16x8 (&fn)[2][5], const bf16x8 (&pr)[2][4], const bf16x8 (&pn)[2][5], int st, bool ld, bool mm) {
            lptr p, r0, r1;
            if (ld) {
                bar();
                const unsigned so = (st & 3) * F_STG;
                p = lbase(af + so); r0 = lbase(a0 + so); r1 = lbase((a0 ^ 64u) + so);
            }
#pragma unroll
            for (int k = 0; k < 2; ++k)
#pragma unroll
                for (int t = 0; t < 5; ++t) {
                    if (ld) {
                        fn[k][t] = lds128(p + t * 2048 + k * 1024);
                        if (t < 4) fr[k][t] = lds128((k ? r1 : r0) + t * 2048);
                    }
                    SBAR();
                    if (mm) {
#pragma unroll
                        for (int u = 0; u < 4; ++u) acc[u][t] = MFMA16(pn[k][t], pr[k][u], acc[u][t]);
                    }
                    SBAR();
                }
        };
        gu32* ocnt = (gu32*)(ws + WS_FLAG + (256 + (sb >> 2)) * 128);
        unsigned oseen = 0;
        const int poll_at = wid == 0 ? 8 : 99;
        step(frE, fnE, frO, fnO, 0, true, false);
        STAMP(3, 1);
#pragma unroll 1
        for (int st = 0; st < 14; st += 2) {
            step(frO, fnO, frE, fnE, st + 1, true, true);
            if (st == 2) wait_vm<0>();
            if (st == 2) STAMP(3, 7);
            if (st == poll_at) poll1(oseen, ocnt);
            step(frE, fnE, frO, fnO, st + 2, true, true);
        }
        STAMP(3, 4);
        if (wid == 0) {
            asm volatile("s_waitcnt vmcnt(0)" : "+v"(oseen)::"memory");
            wait_count(ocnt, oseen, 32u);
        }
        step(frO, fnO, frE, fnE, 15, true, true);
        STAMP(3, 5);
        step(frE, fnE, frO, fnO, 16, false, true);
        STAMP(3, 2);
        const unsigned yp = WS_YP + (unsigned)(((tb * 4 + eg) * 4 + wid) * 4 + sp) * 10240u + lane * 16;
        u32x4 pk[2][5];
#pragma unroll
        for (int u = 0; u < 2; ++u)
#pragma unroll
            for (int t = 0; t < 5; ++t) {
                const f32x4 v0 = acc[2 * u][t], v1 = acc[2 * u + 1][t];
                pk[u][t] = u32x4{pk2(v0[0], v0[1]), pk2(v0[2], v0[3]), pk2(v1[0], v1[1]), pk2(v1[2], v1[3])};
            }
        if (wid != sp) {
#pragma unroll
            for (int u = 0; u < 2; ++u)
#pragma unroll
                for (int t = 0; t < 5; ++t) st128(wr, yp, (u * 5 + t) * 1024, pk[u][t]);
        } else {
#pragma unroll
            for (int u = 0; u < 2; ++u)
#pragma unroll
                for (int t = 0; t < 5; ++t) *(LAS u32x4*)(uintptr_t)(lds0 + (u ? F_RING : 102400) + t * 1024 + lane * 16) = pk[u][t];
        }
        SBAR();
        STAMP(3, 3);
        bar();
        wait_vm<0>();
        STAMP(2, 5);
        if (lane == 0 && wid != sp) __hip_atomic_fetch_add((gu32*)(ws + WS_FLAG + ((tb * 4 + eg) * 4 + wid) * 128), 1u, RLX_AGENT);
        const unsigned oa = WS_OA + (unsigned)((sb * 2 + rh) * NH + kk * 4) * 1024u + q * 512 + c * 16;
        const unsigned ob = WS_OB + (unsigned)((sb * 2 + rh) * NH + kk * 16) * 256u + q * 512 + c * 8;
#pragma unroll
        for (int j = 0; j < 4; ++j)
#pragma unroll
            for (int k = 0; k < 2; ++k)
#pragma unroll
                for (int u = 0; u < 2; ++u) fo[j][k][u] = as_frag(ld128c(wr, oa + j * 8192 + k * 2048 + u * 256));
#pragma unroll
        for (int k = 0; k < 2; ++k)
#pragma unroll
            for (int u = 0; u < 2; ++u) {
                const u32x2 a = ld64c(wr, ob + k * 2048 + u * 128), bq = ld64c(wr, ob + k * 2048 + u * 128 + 256);
                fo[4][k][u] = as_frag(u32x4{a[0], a[1], bq[0], bq[1]});
            }
        SBAR();
        STAMP(3, 6);
    }
    const int s0 = sb * 64 + rh * 32 + c;
    f32x4 acc[2][5];
#pragma unroll
    for (int t = 0; t < 5; ++t) { acc[0][t] = f32x4{}; acc[1][t] = f32x4{}; }
    STAMP(2, 0);
    {
        const unsigned wl = lds0 + (unsigned)(c >> 2) * 160u + (c & 3) * 8;
        const lptr p0 = lbase(wl + (unsigned)(kk * 80 + (q >> 1) * 20 + (q & 1) * 4) * 160u), p1 = lbase(wl + (unsigned)(kk * 80 + (q >> 1) * 20 + (q & 1) * 4) * 160u + 51200);
        const lptr p2 = lbase(wl + (unsigned)(kk * 320 + q * 40 + 16) * 160u);
        bf16x8 fn[2][5];
        auto wof = [&](int n, int t) {
            const lptr pp = n < 4 ? p0 + (n >> 1) * 25600 + (n & 1) * 6400 : n < 8 ? p1 + ((n - 4) >> 1) * 25600 + (n & 1) * 6400 : p2 + (n & 1) * 25600;
            const s16x4 lo = ldstr(pp + t * 32), hi = ldstr(pp + t * 32 + (n < 8 ? 8 : 20) * 160);
            return __builtin_bit_cast(bf16x8, __builtin_shufflevector(lo, hi, 0, 1, 2, 3, 4, 5, 6, 7));
        };
#define WOF(n, t) wof(n, t)
#pragma unroll
        for (int t = 0; t < 5; ++t) fn[0][t] = WOF(0, t);
#pragma unroll
        for (int n = 0; n < 10; ++n)
#pragma unroll
            for (int t = 0; t < 5; ++t) {
                if (n < 9) fn[(n + 1) & 1][t] = WOF(n + 1, t);
                SBAR();
                acc[0][t] = MFMA16(fn[n & 1][t], fo[n >> 1][n & 1][0], acc[0][t]);
                acc[1][t] = MFMA16(fn[n & 1][t], fo[n >> 1][n & 1][1], acc[1][t]);
                SBAR();
            }
    }
    STAMP(2, 1);
    const float g0 = *(const LAS float*)(uintptr_t)(lds0 + LDS_G + (rh * 32 + c) * 4), g1 = *(const LAS float*)(uintptr_t)(lds0 + LDS_G + (rh * 32 + 16 + c) * 4);
    const int t0 = kk * 3;
    f32x4 fin[2][3];
    {
        LAS f32x4* xw = (LAS f32x4*)(uintptr_t)(lds0 + 3 * F_STG + wid * 6144 + lane * 16);
        const LAS f32x4* xr = (const LAS f32x4*)(uintptr_t)(lds0 + 3 * F_STG + (wid ^ 2) * 6144 + lane * 16);
        unsigned mk = 0u - (unsigned)kk;
        PIN(mk);
#pragma unroll
        for (int u = 0; u < 2; ++u)
#pragma unroll
            for (int t = 0; t < 3; ++t) {
                const f32x4 lo = acc[u][t], hi = acc[u][t < 2 ? 3 + t : 4];
                f32x4 snd;
#pragma unroll
                for (int e = 0; e < 4; ++e) { fin[u][t][e] = bsel(mk, hi[e], lo[e]); snd[e] = bsel(mk, lo[e], hi[e]); }
                xw[(u * 3 + t) * 64] = snd;
            }
        bar();
#pragma unroll
        for (int u = 0; u < 2; ++u)
#pragma unroll
            for (int t = 0; t < 3; ++t) fin[u][t] = (fin[u][t] + xr[(u * 3 + t) * 64]) * (u ? g1 : g0);
    }
#pragma unroll
    for (int t = 0; t < 3; ++t) {
        const u32x4 y = *(const LAS u32x4*)(uintptr_t)(lds0 + (rh ? F_RING : 102400) + (t0 + t < 5 ? t0 + t : 0) * 1024 + lane * 16);
#pragma unroll
        for (int u = 0; u < 2; ++u) fin[u][t] += f32x4{bflo(y[2 * u]), bfhi(y[2 * u]), bflo(y[2 * u + 1]), bfhi(y[2 * u + 1])} * (1.0f - (u ? g1 : g0));
    }
    bar_raw();
    STAMP(2, 2);
    const LAS f32x4* yr = (const LAS f32x4*)(uintptr_t)(lds0 + wid * 6144 + lane * 16);
    u16* op = out + (size_t)s0 * DM + ng * 80 + t0 * 16 + q * 4;
#pragma unroll
    for (int t = 0; t < 3; ++t) {
        if (t0 + t < 5) {
#pragma unroll
            for (int u = 0; u < 2; ++u) {
                const f32x4 v = fin[u][t] + yr[(u * 3 + t) * 64] * (1.0f - (u ? g1 : g0));
                *(u32x2*)(op + (size_t)u * 16 * DM + t * 16) = u32x2{pk2(v[0], v[1]), pk2(v[2], v[3])};
            }
        }
    }
    STAMP_END(2);
}

constexpr int LDS_B = LDS_SCR + 256;

#define CODE_LINES 220
__global__ __launch_bounds__(512) void k_all(const u16* __restrict__ x, const u16* __restrict__ wqkv, const u16* __restrict__ cd,
                                             const u16* __restrict__ sd, char* __restrict__ ws, const u16* __restrict__ wo, const u16* __restrict__ gw,
                                             const u16* __restrict__ cs, const u16* __restrict__ ss, u16* __restrict__ out) {
    extern __shared__ __attribute__((aligned(16))) char smem[];
    const unsigned lds0 = (unsigned)(uintptr_t)smem;
    const int wid = __builtin_amdgcn_readfirstlane(threadIdx.x >> 6);
    const int b = blockIdx.x, xcd = b & 7, i = b >> 3;
    const int rb = (xcd & 1) * 8 + (i & 7), cq = xcd >> 1, gl = i >> 3;
    asm volatile("" ::"s"(x), "s"(wqkv), "s"(cd), "s"(sd));
    if (wid < 4) {
        STAMP(0, 0);
        if (i < 2) {
            const char* pc;
            asm volatile("s_getpc_b64 %0" : "=s"(pc));
            touch((const char*)((uintptr_t)pc & ~(uintptr_t)127), min(threadIdx.x, (unsigned)CODE_LINES) * 128, lds0 + P_RING + 64);
        }
        if (threadIdx.x == 64) {
            const unsigned n = __hip_atomic_load((gu32*)(ws + WS_RSP + b * 4), RLX_AGENT) + 1u;
            __hip_atomic_store((gu32*)(ws + WS_REQ + b * 4), n, RLX_AGENT);
            *(volatile LAS unsigned*)(uintptr_t)(lds0 + P_RING) = n;
        }
        if (b == LEADER) {
            const unsigned tl = threadIdx.x;
            st128(ws_rsrc(ws), (tl * VP + 640) * 16, WS_VV, u32x4{ONES, ONES, ONES, ONES});
            st32(ws_rsrc(ws), tl * 128, WS_FLAG, 0u);
            if (tl < 18) st32(ws_rsrc(ws), (256 + tl) * 128, WS_FLAG, 0u);
            st32(ws_rsrc(ws), tl * 4, WS_ACK, 0u);
            wait_vm<0>();
        }
        asm volatile("s_waitcnt lgkmcnt(0)" ::: "memory");
        if (gl == 3) proj_mma<2>(ws, rb, cq, lds0, wid);
        else if (cq * 3 + gl < 8) proj_mma<0>(ws, rb, cq * 3 + gl, lds0, wid);
        else proj_mma<1>(ws, rb, cq * 3 + gl, lds0, wid);
        wait_vm<0>();
        STAMP(0, 4);
    } else proj_load(x, wqkv, cd, sd, ws, rb, cq, gl, lds0, wid - 4);
    bar();
    STAMP(1, 6);
    if (wid == 6) {
        const int lane = threadIdx.x & 63;
        if (lane < (gl != 3 ? 3 : 1)) __hip_atomic_fetch_add((gu32*)(ws + WS_FLAG + (lane ? 263 + ((cq * 3 + gl) & 3) * 2 + lane : 272 + (xcd & 1)) * 128), 1u, RLX_AGENT);
        STAMP(1, 4);
        wait_count((gu32*)(ws + WS_FLAG + (264 + xcd) * 128), 0u, 48u);
    }
    if (wid < 4) phase_attn(ws, lds0, wid);
    else if (wid < 6) attn_produce(x, gw, ws, lds0, wid - 4);
    else attn_helper(cs, ss, wo, x, gw, ws, lds0, wid - 6);
    phase_tail(cs, ss, wo, ws, out, lds0, wid);
    STAMP_FLUSH(0); STAMP_FLUSH(1);
}

extern "C" void kernel_launch(void* const* d_in, const int* in_sizes, int n_in, void* d_out, int out_size, void* d_ws, size_t ws_size,
                              hipStream_t stream) {
    const u16* x = (const u16*)d_in[0];
    const u16* wqkv = (const u16*)d_in[1];
    const u16* wo = (const u16*)d_in[2];
    const u16* cs = (const u16*)d_in[4];
    const u16* ss = (const u16*)d_in[5];
    const u16* cd = (const u16*)d_in[6];
    const u16* sd = (const u16*)d_in[7];
    const u16* gw = (const u16*)d_in[8];
    char* ws = (char*)d_ws;
    (void)hipFuncSetAttribute((const void*)k_all, hipFuncAttributeMaxDynamicSharedMemorySize, LDS_B);
    k_all<<<256, 512, LDS_B, stream>>>(x, wqkv, cd, sd, ws, wo, gw, cs, ss, (u16*)d_out);
}
```

```cpp
#include <hip/hip_runtime.h>
#include <hip/hip_bf16.h>
#include <stdint.h>

typedef unsigned short u16;
typedef __attribute__((ext_vector_type(8))) __bf16 bf16x8;
typedef __attribute__((ext_vector_type(4))) short s16x4;
typedef __attribute__((ext_vector_type(16))) float f32x16;
typedef __attribute__((ext_vector_type(4))) float f32x4;
typedef __attribute__((ext_vector_type(4))) unsigned u32x4;
typedef __attribute__((ext_vector_type(2))) unsigned u32x2;
#define LAS __attribute__((address_space(3)))
#define DI __device__ __forceinline__
typedef const LAS char* lptr;

constexpr int SEQ = 2048, DM = 640, NH = 32;
constexpr int VP = 648;
constexpr unsigned WS_QA = 0;
constexpr unsigned WS_KA = WS_QA + NH * 64 * 1024;
constexpr unsigned WS_QB = WS_KA + NH * 64 * 1024;
constexpr unsigned WS_KB = WS_QB + NH * 64 * 512;
constexpr unsigned WS_VV = WS_KB + NH * 64 * 512;
constexpr unsigned WS_AB = WS_VV + 64 * 4 * VP * 16;
constexpr unsigned WS_YP = WS_AB + 40 * 128 * 1024;
constexpr unsigned WS_OA = WS_YP + 4 * 64 * 40 * 1024;
constexpr unsigned WS_OB = WS_OA + 64 * NH * 1024;
constexpr unsigned WS_FLAG = WS_OB + 64 * NH * 256;
constexpr unsigned WS_REQ = WS_FLAG + 274 * 128;
constexpr unsigned WS_RSP = WS_REQ + 1024;
constexpr unsigned WS_ACK = WS_RSP + 1024;
constexpr unsigned WS_END = WS_ACK + 1024;
constexpr float QSCALE = 0.22360679774997896f * 1.4426950408889634f;
constexpr int SC1 = 16;

#define MFMA32(a, b, c) __builtin_amdgcn_mfma_f32_32x32x16_bf16((a), (b), (c), 0, 0, 0)
#define MFMA8(a, b, c) __builtin_amdgcn_mfma_f32_32x32x8bf16_1k(__builtin_bit_cast(s16x4, a), __builtin_bit_cast(s16x4, b), (c), 0, 0, 0)
#define MFMA16(a, b, c) __builtin_amdgcn_mfma_f32_16x16x32_bf16((a), (b), (c), 0, 0, 0)
#define SBAR() __builtin_amdgcn_sched_barrier(0)
#define PIN(x) asm volatile("" : "+v"(x))

typedef __attribute__((ext_vector_type(2))) __bf16 bf16x2;
typedef __attribute__((ext_vector_type(2))) float f32x2;
DI unsigned pk2(float lo, float hi) {
    f32x2 v = {lo, hi};
    return __builtin_bit_cast(unsigned, __builtin_convertvector(v, bf16x2));
}
DI float bsel(unsigned m, float a, float b) { return __uint_as_float((__float_as_uint(a) & m) | (__float_as_uint(b) & ~m)); }
DI float bflo(unsigned v) { return __uint_as_float(v << 16); }
DI float bfhi(unsigned v) { return __uint_as_float(v & 0xFFFF0000u); }
DI void dma16(const void* sbase, unsigned voff, unsigned lds) {
    asm volatile("s_mov_b32 m0, %2\n\ts_nop 0\n\tglobal_load_lds_dwordx4 %0, %1" ::"v"(voff), "s"(sbase), "s"(lds) : "memory");
}
DI void dma16c(const void* sbase, unsigned voff, unsigned lds) {
    asm volatile("s_mov_b32 m0, %2\n\ts_nop 0\n\tglobal_load_lds_dwordx4 %0, %1 sc1" ::"v"(voff), "s"(sbase), "s"(lds) : "memory");
}
template <int N> DI void dma16_lo(const void* sbase, unsigned voff, unsigned lds) {
    unsigned long long sv;
    asm volatile("s_mov_b32 m0, %3\n\ts_mov_b64 %0, exec\n\ts_mov_b64 exec, %4\n\tglobal_load_lds_dwordx4 %1, %2 sc1\n\ts_mov_b64 exec, %0"
                 : "=&s"(sv) : "v"(voff), "s"(sbase), "s"(lds), "s"((1ull << N) - 1) : "memory");
}
DI void touch(const void* sbase, unsigned voff, unsigned lds) {
    asm volatile("s_mov_b32 m0, %2\n\ts_nop 0\n\tglobal_load_lds_dword %0, %1" ::"v"(voff), "s"(sbase), "s"(lds) : "memory");
}
template <int N> DI void wait_vm() { asm volatile("s_waitcnt vmcnt(%0)" ::"i"(N) : "memory"); }
DI void bar() { asm volatile("s_waitcnt lgkmcnt(0)\n\ts_barrier" ::: "memory"); }
DI void bar_raw() { asm volatile("s_barrier" ::: "memory"); }
DI lptr lbase(unsigned a) { asm volatile("" : "+v"(a)); return (lptr)(uintptr_t)a; }
DI bf16x8 lds128(lptr p) { return *(const LAS bf16x8*)p; }
DI u32x2 lds64(lptr p) { return *(const LAS u32x2*)p; }
DI s16x4 ldstr(lptr p) {
    typedef short v4i16_t __attribute__((ext_vector_type(4)));
    return __builtin_bit_cast(s16x4, __builtin_amdgcn_ds_read_tr16_b64_v4i16((LAS v4i16_t*)p));
}
DI u32x4 pack8(const f32x16& x, int s, float sc) {
    u32x4 p;
    p[0] = pk2(x[8 * s + 0] * sc, x[8 * s + 1] * sc);
    p[1] = pk2(x[8 * s + 2] * sc, x[8 * s + 3] * sc);
    p[2] = pk2(x[8 * s + 4] * sc, x[8 * s + 5] * sc);
    p[3] = pk2(x[8 * s + 6] * sc, x[8 * s + 7] * sc);
    return p;
}
typedef __amdgpu_buffer_rsrc_t rsrc_t;
DI rsrc_t ws_rsrc(const void* ws) { return __builtin_amdgcn_make_buffer_rsrc((void*)ws, (short)0, (int)WS_END, 0x00020000); }
DI void st128(rsrc_t r, unsigned voff, unsigned soff, u32x4 v) { __builtin_amdgcn_raw_buffer_store_b128(v, r, voff + soff, 0, SC1); }
DI void st64(rsrc_t r, unsigned voff, unsigned soff, u32x2 v) { __builtin_amdgcn_raw_buffer_store_b64(v, r, voff, soff, SC1); }
DI void st32(rsrc_t r, unsigned voff, unsigned soff, unsigned v) { __builtin_amdgcn_raw_buffer_store_b32(v, r, voff, soff, SC1); }
DI u32x4 ld128(rsrc_t r, unsigned voff) { return __builtin_amdgcn_raw_buffer_load_b128(r, voff, 0, SC1); }
DI u32x2 ld64(rsrc_t r, unsigned voff) { return __builtin_amdgcn_raw_buffer_load_b64(r, voff, 0, SC1); }
DI bf16x8 as_frag(u32x4 v) { return __builtin_bit_cast(bf16x8, v); }
typedef __attribute__((ext_vector_type(16))) __bf16 bf16x16;
template <int HI> DI f32x16 pv_mma(bf16x8 v, const f32x16& p, const f32x16& o, int idx) {
    const auto ph = __builtin_shufflevector(p, p, 8 * HI, 8 * HI + 1, 8 * HI + 2, 8 * HI + 3, 8 * HI + 4, 8 * HI + 5, 8 * HI + 6, 8 * HI + 7);
    return __builtin_amdgcn_smfmac_f32_32x32x32_bf16(v, __builtin_bit_cast(bf16x16, ph), o, idx, 0, 0);
}
DI u32x4 pack8n(const f32x16& x, int s, float sc) {
    const u32x4 p = pack8(x, s, sc);
    const auto r0 = __builtin_amdgcn_permlane32_swap(p[0], p[2], false, false);
    const auto r1 = __builtin_amdgcn_permlane32_swap(p[1], p[3], false, false);
    return u32x4{r0[0], r1[0], r0[1], r1[1]};
}

typedef __attribute__((address_space(1))) unsigned gu32;
#define RLX_AGENT __ATOMIC_RELAXED, __HIP_MEMORY_SCOPE_AGENT
DI void poll1(unsigned& v, gu32* c) {
    unsigned long long sv;
    asm volatile("s_mov_b64 %1, exec\n\ts_mov_b64 exec, 1\n\tglobal_load_dword %0, %2, %3 sc1\n\ts_mov_b64 exec, %1" : "+v"(v), "=&s"(sv) : "v"(0u), "s"(c) : "memory");
}
DI void wait_count(gu32* c, unsigned seen, unsigned want) {
#pragma unroll 1
    for (unsigned spins = 0; (unsigned)__builtin_amdgcn_readfirstlane(seen) < want && spins < (1u << 17); ++spins) {
        __builtin_amdgcn_s_sleep(4);
        poll1(seen, c);
        asm volatile("s_waitcnt vmcnt(0)" : "+v"(seen)::"memory");
    }
    asm volatile("" ::: "memory");
}

constexpr int LEADER = 255;
DI void echo_round(u32x4& v, const char* ws, bool put) {
    const unsigned o = (threadIdx.x & 63) * 16;
    if (put) asm volatile("global_store_dwordx4 %0, %1, %2 sc1\n\ts_nop 1" ::"v"(o), "v"(v), "s"(ws + WS_RSP) : "memory");
    asm volatile("global_load_dwordx4 %0, %1, %2 sc1" : "+v"(v) : "v"(o), "s"(ws + WS_REQ) : "memory");
}

constexpr int P_STG = 45056;
constexpr int P_RING = 3 * P_STG;

DI void store_ab(const f32x16& acc, int dt, int stile, rsrc_t ws, int lane) {
    const float sc = dt >= 20 ? -1.f : 1.f;
    const int nt = dt >= 20 ? dt - 20 : dt;
    const int ks = (dt >= 20 ? 64 : 0) + stile;
    const unsigned dst = WS_AB + (unsigned)((nt * 2 + ((lane >> 4) & 1)) * 128 + ks) * 1024u + (unsigned)((lane >> 5) * 256 + (lane & 15) * 16);
#pragma unroll
    for (int s = 0; s < 2; ++s) st128(ws, dst, s * 512, pack8n(acc, s, sc));
}

DI void proj_load(const u16* __restrict__ x, const u16* __restrict__ wqkv, const u16* __restrict__ cd, const u16* __restrict__ sd,
                  const char* __restrict__ ws, int rb, int cq, int gl, unsigned lds0, int wid) {
    const int lane = threadIdx.x & 63;
    constexpr int NDMA = 10;
    const bool pa = gl != 3;
    const unsigned vrow = (unsigned)(lane >> 3) * 1280u + (unsigned)(((lane & 7) ^ ((4 * (wid & 1) + (lane >> 4)) & 7)) * 16);
    const char* xb = (const char*)x + ((size_t)rb * 128 + wid * 8) * 1280;
    int st = 3 * ((rb + gl) % 3);
    unsigned slot = lds0 + wid * 1024;
#pragma unroll
    for (int j = 0; j < 4; ++j) dma16(xb + j * 40960 + st * 128, vrow, slot + j * 4096);
    const char* wb[7];
    unsigned vo[5];
    const int dt0 = pa ? 28 + cq * 3 + gl : 7 * cq;
#pragma unroll
    for (int j = 0; j < 7; ++j) {
        const int dt = dt0 + j;
        const char* d = (const char*)(dt >= 20 ? sd : cd) + ((dt >= 20 ? dt - 20 : dt) * 32 + wid * 8) * 1280;
        wb[j] = pa && j ? (const char*)wqkv + (cq * 3 + gl) * 320 : d;
    }
#pragma unroll
    for (int j = 0; j < 5; ++j) { const unsigned i = (wid + 4 * j) * 64 + lane, q = (i * 3277u) >> 16; vo[j] = pa ? q * 3840u + (i - q * 20u) * 16u : vrow; }
    const int wstep = pa ? 64 * 3840 : 128;
    auto issue = [&](bool with_x) {
        if (with_x) {
#pragma unroll
            for (int j = 0; j < 4; ++j) dma16(xb + j * 40960 + st * 128, vrow, slot + j * 4096);
        }
        if (!pa) dma16(wb[6] + st * 128, vrow, slot + 40960);
        dma16(wb[0] + st * 128, vrow, slot + 16384);
#pragma unroll
        for (int j = 0; j < 5; ++j) dma16(wb[1 + j] + st * wstep, vo[j], slot + 20480 + j * 4096);
        slot = slot + P_STG == lds0 + wid * 1024 + P_RING ? lds0 + wid * 1024 : slot + P_STG;
        st = st == 9 ? 0 : st + 1;
    };
    const bool leader = blockIdx.x == LEADER && wid == 3;
    u32x4 rq = {0u, 0u, 0u, 0u};
    unsigned back = 0;
#pragma unroll 1
    for (int n = 0; n < 3; ++n) {
        issue(n > 0);
        if (n == 1) { wait_vm<NDMA>(); bar_raw(); }
    }
    wait_vm<NDMA>(); bar_raw(); bar_raw();
#pragma unroll 1
    for (int n = 3; n < 10; ++n) {
        if (leader) echo_round(rq, ws, n > 3);
        if (wid == 0 && n == 9) poll1(back, (gu32*)(ws + WS_RSP + blockIdx.x * 4));
        issue(true); wait_vm<NDMA>(); bar_raw(); bar_raw();
    }
    wait_vm<0>();
    bar_raw();
    bool acked = wid != 0;
    const unsigned want = __builtin_amdgcn_readfirstlane(*(const volatile LAS unsigned*)(uintptr_t)(lds0 + P_RING));
    PIN(back);
    if (!acked && (unsigned)__builtin_amdgcn_readfirstlane(back) == want) {
        if (lane == 0) __hip_atomic_store((gu32*)(ws + WS_ACK + blockIdx.x * 4), 1u, RLX_AGENT);
        acked = true;
    }
    bar_raw(); bar_raw(); bar_raw();
    if (!acked) {
#pragma unroll 1
        for (unsigned spins = 0; spins < (1u << 17) && (unsigned)__builtin_amdgcn_readfirstlane(back) != want; ++spins) {
            __builtin_amdgcn_s_sleep(4);
            poll1(back, (gu32*)(ws + WS_RSP + blockIdx.x * 4));
            asm volatile("s_waitcnt vmcnt(0)" : "+v"(back)::"memory");
        }
        if (lane == 0) __hip_atomic_store((gu32*)(ws + WS_ACK + blockIdx.x * 4), 1u, RLX_AGENT);
    }
    if (leader) {
#pragma unroll 1
        for (unsigned spins = 0; spins < (1u << 17); ++spins) {
            echo_round(rq, ws, true);
            u32x4 ak;
            asm volatile("global_load_dwordx4 %0, %2, %3 sc1\n\ts_waitcnt vmcnt(0)" : "=&v"(ak), "+v"(rq) : "v"(lane * 16), "s"(ws + WS_ACK) : "memory");
            if (__all((ak[0] & ak[1] & ak[2] & ak[3]) == 1u)) break;
            __builtin_amdgcn_s_sleep(4);
        }
    }
}

template <int PATH>
DI void proj_mma(char* __restrict__ ws, int rb, int grp, unsigned lds0, int wid) {
    const int lane = threadIdx.x & 63, r = lane & 31, h = lane >> 5;
    constexpr bool PATH_A = PATH < 2;
    constexpr int NW = PATH_A ? 5 : 0, ND = PATH_A ? 1 : 7, NT = NW + ND;
    const int dt0 = PATH_A ? 28 + grp : 7 * grp;
    const unsigned sw = lds0 + (unsigned)r * 128u + (unsigned)((h ^ ((r >> 1) & 7)) * 16);
    const unsigned awo = lds0 + 20480u + (unsigned)(8 * h + ((lane & 15) >> 2)) * 320u + (unsigned)(((lane >> 4) & 1) * 32 + (lane & 3) * 8);
    f32x16 acc[NT];
#pragma unroll
    for (int t = 0; t < NT; ++t) acc[t] = f32x16{};
    bf16x8 fx[2], fw[2][NT];
    lptr pr, px, pw;
    auto setp = [&](unsigned so, int ks) {
        pr = lbase((sw ^ (unsigned)(ks * 32)) + so);
        px = lbase((sw ^ (unsigned)(ks * 32)) + so + wid * 4096);
        if (PATH_A && ks == 0) pw = lbase(awo + so);
    };
    auto rd = [&](int ks, int b, int t) {
        if (t == 0) fx[b] = lds128(px);
        if (t < ND) fw[b][t] = lds128(pr + 16384 + t * 4096);
        else {
            s16x4 lo = ldstr(pw + ks * 16 * 320 + (t - ND) * 64), hi = ldstr(pw + ks * 16 * 320 + 4 * 320 + (t - ND) * 64);
            fw[b][t] = __builtin_bit_cast(bf16x8, __builtin_shufflevector(lo, hi, 0, 1, 2, 3, 4, 5, 6, 7));
        }
    };
    auto mm = [&](int b, int t) {
        if (PATH == 0 && t >= ND) acc[t] = MFMA32(fw[b][t], fx[b], acc[t]);
        else acc[t] = MFMA32(fx[b], fw[b][t], acc[t]);
    };
    unsigned so = 0;
    setp(0, 0);
#pragma unroll
    for (int t = 0; t < NT; ++t) PIN(acc[t]);
    bar_raw();
#pragma unroll
    for (int t = 0; t < NT; ++t) rd(0, 0, t);
    SBAR();
#pragma unroll 1
    for (int st = 0; st < 10; ++st) {
#pragma unroll
        for (int ks = 0; ks < 4; ++ks) {
            if (ks == 3) { bar_raw(); so = so == 2 * P_STG ? 0 : so + P_STG; }
            setp(so, (ks + 1) & 3);
#pragma unroll
            for (int t = 0; t < NT; ++t) { rd((ks + 1) & 3, (ks + 1) & 1, t); SBAR(); mm(ks & 1, t); SBAR(); }
        }
        bar_raw();
    }
    const rsrc_t wr = ws_rsrc(ws);
    const int stile = rb * 4 + wid;
    if (PATH == 0) {
        const float sc = grp < 4 ? QSCALE : 1.f;
        const unsigned t0 = (unsigned)(grp * 8 * 64 + stile);
        const unsigned va = WS_QA + t0 * 1024 + lane * 16, vb = WS_QB + (t0 + h * 64) * 512 + r * 16;
#pragma unroll
        for (int m = 0; m < 4; ++m) {
            u32x2 q[5];
#pragma unroll
            for (int i = 0; i < 5; ++i) {
                const int p = 5 * m + i, t = ND + (p >> 2), g = p & 3;
                q[i][0] = pk2(acc[t][4 * g] * sc, acc[t][4 * g + 1] * sc);
                q[i][1] = pk2(acc[t][4 * g + 2] * sc, acc[t][4 * g + 3] * sc);
            }
            st128(wr, va, (2 * m) * 64 * 1024, u32x4{q[0][0], q[0][1], q[1][0], q[1][1]});
            st128(wr, va, (2 * m + 1) * 64 * 1024, u32x4{q[3][0], q[3][1], q[4][0], q[4][1]});
            st128(wr, vb, (2 * m) * 64 * 512, u32x4{q[2][0], q[2][1], 0x00003F80u, 0u});
        }
    } else if (PATH == 1) {
        const unsigned vv = WS_VV + (unsigned)((stile * 4 + h) * VP + (grp - 8) * 160 + r) * 16;
#pragma unroll
        for (int t = 0; t < 5; ++t) {
            st128(wr, vv, t * 512, pack8n(acc[ND + t], 0, 1.f));
            st128(wr, vv, t * 512 + 2 * VP * 16, pack8n(acc[ND + t], 1, 1.f));
        }
    }
#pragma unroll
    for (int t = 0; t < ND; ++t) store_ab(acc[t], dt0 + t, stile, wr, lane);
}
#define YG_MAP const int tb = (xcd >> 2) * 8 + (i & 7), eg = i >> 3, sp = xcd & 3
constexpr int F_STG = 36864;
constexpr int F_RING = 4 * F_STG;
DI void yg_issue(const u16* __restrict__ cs, const u16* __restrict__ ss, const char* __restrict__ ws, unsigned lds0, int lw, int st) {
    const int lane = threadIdx.x & 63;
    const int b = blockIdx.x, xcd = b & 7, i = b >> 3;
    YG_MAP;
    const unsigned vrow = (unsigned)(lane >> 3) * 4096u + (unsigned)(((lane & 7) ^ ((4 * (lw & 1) + (lane >> 4)) & 7)) * 16);
    const char* rp = (const char*)(sp >= 2 ? ss : cs) + (size_t)(tb * 128 + lw * 8) * 4096 + (sp & 1) * 2048;
    const char* ab = ws + WS_AB + (size_t)((eg * 10 + (lw >> 1)) * 128 + sp * 32 + (lw & 1)) * 1024;
    const unsigned d = lds0 + lw * 1024 + (st & 3) * F_STG;
#pragma unroll
    for (int j = 0; j < 4; ++j) dma16(rp + j * 32 * 4096 + st * 128, vrow, d + j * 4096);
#pragma unroll
    for (int j = 0; j < 5; ++j) dma16c(ab + (j * 256 + st * 2) * 1024, lane * 16, d + 16384 + j * 4096);
}

DI float xhalf_max(float v) {
    auto rr = __builtin_amdgcn_permlane32_swap(__float_as_uint(v), __float_as_uint(v), false, false);
    return fmaxf(__uint_as_float(rr[0]), __uint_as_float(rr[1]));
}
DI float max16(const f32x16& s) {
    float a = fmaxf(fmaxf(s[0], s[1]), s[2]), b = fmaxf(fmaxf(s[3], s[4]), s[5]);
    a = fmaxf(fmaxf(a, s[6]), s[7]); b = fmaxf(fmaxf(b, s[8]), s[9]);
    a = fmaxf(fmaxf(a, s[10]), s[11]); b = fmaxf(fmaxf(b, s[12]), s[13]);
    a = fmaxf(fmaxf(a, s[14]), s[15]);
    return fmaxf(a, b);
}
#define EX2(v) __builtin_amdgcn_exp2f(v)
constexpr unsigned VV_T = 4 * VP * 16, VV_S = 2 * VP * 16;
constexpr unsigned ONES = 0x3F803F80u;

DI u32x4 fix_v(u32x4 v, int r) {
#pragma unroll
    for (int i = 0; i < 4; ++i) v[i] = r < 20 ? v[i] : (r == 20 ? ONES : 0u);
    return v;
}
DI f32x16 attn_safe(rsrc_t wr, int head, u32x4 qa, u32x4 qb, int lane) {
    const int r = lane & 31, h = lane >> 5;
    qb[2] = 0u;
    f32x16 o = f32x16{};
    float m = -INFINITY;
#pragma unroll 1
    for (int t = 0; t < 64; ++t) {
        const u32x4 ka = ld128(wr, WS_KA + (unsigned)(head * 64 + t) * 1024 + lane * 16);
        const u32x4 kb = ld128(wr, WS_KB + (unsigned)(head * 64 + t) * 512 + r * 16);
        const unsigned vp = WS_VV + (unsigned)((t * 4 + h) * VP + head * 20 + (r < 20 ? r : 0)) * 16;
        const u32x4 v0 = fix_v(ld128(wr, vp), r), v1 = fix_v(ld128(wr, vp + VV_S), r);
        f32x16 s = MFMA32(as_frag(ka), as_frag(qa), f32x16{});
        s = MFMA32(as_frag(kb), as_frag(qb), s);
        const float mn = fmaxf(m, xhalf_max(max16(s)));
        const float al = EX2(m - mn);
        m = mn;
#pragma unroll
        for (int i = 0; i < 16; ++i) { o[i] *= al; s[i] = EX2(s[i] - mn); }
        o = pv_mma<0>(as_frag(v0), s, o, (int)0xDDDDDDDD);
        o = pv_mma<1>(as_frag(v1), s, o, (int)0xDDDDDDDD);
    }
    return o;
}

DI void attn_finish(f32x16 o, rsrc_t wr, int head, int qt) {
    const int lane = threadIdx.x & 63, r = lane & 31, h = lane >> 5;
    auto rl = __builtin_amdgcn_permlane32_swap(__float_as_uint(o[8]), __float_as_uint(o[8]), false, false);
    float l = __uint_as_float(rl[1]);
    float z = 0.f;
#pragma unroll
    for (int k = 0; k < 12; ++k) z = __builtin_fmaf(o[k], 0.f, z);
    const bool ok = (z == 0.f) && (l >= 8.6736174e-19f) && (l <= 1.2676506e30f);
    if (__builtin_expect(!__all(ok), 0)) {
        const u32x4 q0 = ld128(wr, WS_QA + (unsigned)(head * 64 + qt) * 1024 + lane * 16);
        const u32x4 q1 = ld128(wr, h ? 0x80000000u : WS_QB + (unsigned)(head * 64 + qt) * 512 + r * 16);
        o = attn_safe(wr, head, q0, q1, lane);
        rl = __builtin_amdgcn_permlane32_swap(__float_as_uint(o[8]), __float_as_uint(o[8]), false, false);
        l = __uint_as_float(rl[1]);
    }
    const float inv = __builtin_amdgcn_rcpf(l);
    const unsigned ot = (unsigned)(qt * NH + head);
    st128(wr, lane * 16, WS_OA + ot * 1024, pack8(o, 0, inv));
    if (h == 0) st64(wr, r * 8, WS_OB + ot * 256, u32x2{pk2(o[8] * inv, o[9] * inv), pk2(o[10] * inv, o[11] * inv)});
}

constexpr int KVT_B = 2880, KVG_B = 4 * KVT_B, KV1 = 3 * F_STG;
constexpr int KV_FULL = KV1 + 4 * KVG_B;
constexpr int KV_FREE = KV_FULL + 16;
constexpr int LDS_G = KV_FREE + 16;
constexpr int LDS_SCR = LDS_G + 256;
DI unsigned kv_off(unsigned g) { return g < 9 ? g * KVG_B : KV1 + ((g - 9) & 3) * KVG_B; }
DI void kv_wait_free(unsigned lds0, unsigned n) {
    for (;;) {
        const u32x4 f = *(const volatile LAS u32x4*)(uintptr_t)(lds0 + KV_FREE);
        if (__builtin_amdgcn_readfirstlane(min(min(f[0], f[1]), min(f[2], f[3]))) >= n) break;
        __builtin_amdgcn_s_sleep(8);
    }
    asm volatile("" ::: "memory");
}
DI void lds_put(unsigned addr, unsigned v) { *(volatile LAS unsigned*)(uintptr_t)addr = v; }

DI void phase_attn(char* __restrict__ ws, unsigned lds0, int wid) {
    const int lane = threadIdx.x & 63, r = lane & 31, h = lane >> 5;
    const int b = blockIdx.x, head = (b & 7) * 4 + (b >> 6), qt0 = ((b >> 3) & 7) * 8 + wid * 2;
    const rsrc_t wr = ws_rsrc(ws);
    bar_raw();
    u32x4 qa[2];
    u32x2 qbv[2];
#pragma unroll
    for (int i = 0; i < 2; ++i) {
        qa[i] = ld128(wr, WS_QA + (unsigned)(head * 64 + qt0 + i) * 1024 + lane * 16);
        qbv[i] = ld64(wr, h ? 0x80000000u : WS_QB + (unsigned)(head * 64 + qt0 + i) * 512 + r * 16);
    }
    const int rv = r <= 20 ? r : 0x8000;
    const unsigned oka = lds0 + lane * 16, okb = lds0 + 1024 + r * 16 + h * 8, ov = lds0 + 1536u + (unsigned)(h * 21 + rv) * 16u;
    unsigned afull = lds0 + KV_FULL, afree = lds0 + KV_FREE + wid * 4;
    PIN(afull); PIN(afree);
    __builtin_amdgcn_s_setprio(2);
    bar_raw();
    bf16x8 ka[2], v0[2], v1[2];
    u32x2 kb[2];
    lptr pka = lbase(oka), pkb = lbase(okb), pv = lbase(ov), pkn;
#define LDK(n, T) do { ka[n] = lds128(pka + (T) * KVT_B); kb[n] = lds64(pkb + (T) * KVT_B); SBAR(); } while (0)
#define LDV(n, T) do { v0[n] = lds128(pv + (T) * KVT_B); v1[n] = lds128(pv + (T) * KVT_B + 42 * 16); SBAR(); } while (0)
    LDK(0, 0); LDK(1, 1); LDV(0, 0);
#pragma unroll
    for (int i = 0; i < 2; ++i) {
        f32x16 t = MFMA32(ka[0], as_frag(qa[i]), f32x16{});
        t = MFMA8(kb[0], qbv[i], t);
        const float m0 = xhalf_max(max16(t));
        qbv[i][0] = h ? (pk2(-m0, 0.f) & 0xFFFFu) : qbv[i][0];
    }
    f32x16 oA = f32x16{}, oB = f32x16{}, s0, s1, s2, s3 = f32x16{};
    int idx = (int)0xDDDDDDDD;
    PIN(idx);
    v0[1] = ka[0]; v1[1] = ka[0];
    s0 = MFMA32(ka[0], as_frag(qa[0]), f32x16{});
    s0 = MFMA8(kb[0], qbv[0], s0);
    SBAR();
#define GRP(MF, H, S, i) do { MF; SBAR(); H; SBAR(); S[i] = EX2(S[i]); S[i + 1] = EX2(S[i + 1]); S[i + 2] = EX2(S[i + 2]); S[i + 3] = EX2(S[i + 3]); PIN(S); SBAR(); } while (0)
#define SLOT(X, N, kn, Q, P, O, vn, H0, H1, H2, H3) do { \
        GRP(N = MFMA32(ka[kn], as_frag(qa[Q]), f32x16{}), H0, X, 0); \
        GRP(N = MFMA8(kb[kn], qbv[Q], N), H1, X, 4); \
        GRP(O = pv_mma<0>(v0[vn], P, O, idx), H2, X, 8); \
        GRP(O = pv_mma<1>(v1[vn], P, O, idx), H3, X, 12); } while (0)
#define RKA(n, T) ka[n] = lds128(pka + (T) * KVT_B)
#define RKB(n, T) kb[n] = lds64(pkb + (T) * KVT_B)
#define RV0(n, T) v0[n] = lds128(pv + (T) * KVT_B)
#define RV1(n, T) v1[n] = lds128(pv + (T) * KVT_B + 42 * 16)
#define LGKM0 __builtin_amdgcn_s_waitcnt(0xC07F)
#define NOP (void)0
    unsigned nx = KVG_B, gv = 0, need = 2;
    lptr pvn;
#define SPIN(x) asm volatile("" : "+s"(x))
#define GROUP(WRAP) do { \
        u32x2 full; unsigned fm, sfull; \
        SLOT(s0, s1, 0, 1, s3, oB, 1, LGKM0, RKA(0, 2), RKB(0, 2), RV0(1, 1)); \
        SLOT(s1, s2, 1, 0, s0, oA, 0, RV1(1, 1), full = *(const volatile LAS u32x2*)(uintptr_t)afull, do { gv += 1; PIN(gv); } while (0), pkn = lbase(okb + nx)); \
        SLOT(s2, s3, 1, 1, s1, oB, 0, LGKM0, RKA(1, 3), RKB(1, 3), RV0(0, 2)); \
        SLOT(s3, s0, 0, 0, s2, oA, 1, RV1(0, 2), do { fm = min(full[0], full[1]); PIN(fm); } while (0), do { sfull = __builtin_amdgcn_readfirstlane(fm); SPIN(sfull); } while (0), pka = lbase(oka + nx)); \
        pkb = pkn; \
          \
        SLOT(s0, s1, 0, 1, s3, oB, 1, \
             do { if (__builtin_expect(sfull < need, 0)) { \
                      do { __builtin_amdgcn_s_sleep(1); full = *(const volatile LAS u32x2*)(uintptr_t)afull; } while ((unsigned)__builtin_amdgcn_readfirstlane(min(full[0], full[1])) < need); } \
                  asm volatile("" ::: "memory"); LGKM0; } while (0), \
             RKA(0, 0), RKB(0, 0), RV0(1, 3)); \
        SLOT(s1, s2, 1, 0, s0, oA, 0, RV1(1, 3), pvn = lbase(ov + nx), do { nx += KVG_B; SPIN(nx); } while (0), \
             do { if (WRAP) { nx = nx == 9 * KVG_B ? KV1 : nx; SPIN(nx); } } while (0)); \
        pv = pvn; \
        SLOT(s2, s3, 1, 1, s1, oB, 0, LGKM0, RKA(1, 1), RKB(1, 1), RV0(0, 0)); \
        SLOT(s3, s0, 0, 0, s2, oA, 1, RV1(0, 0), lds_put(afree, gv)  , do { need += 1; SPIN(need); } while (0), \
             do { if (WRAP) { nx = nx == KV1 + 4 * KVG_B ? KV1 : nx; SPIN(nx); } } while (0)); } while (0)
#pragma unroll 1
    do { GROUP(false); GROUP(true); } while (need < 18);
    oB = pv_mma<0>(v0[1], s3, oB, idx);
    oB = pv_mma<1>(v1[1], s3, oB, idx);
    __builtin_amdgcn_s_setprio(0);
    attn_finish(oA, wr, head, qt0);
    attn_finish(oB, wr, head, qt0 + 1);
}
DI void gate16(const u16* __restrict__ x, const u16* __restrict__ gw, unsigned lds0, int n) {
    const int lane = threadIdx.x & 63, c = lane & 15, q = lane >> 4, b = blockIdx.x, xcd = b & 7, i = b >> 3;
    YG_MAP;
    (void)eg;
    __builtin_amdgcn_s_setprio(3);
    const u16* xr = x + (size_t)((tb * 2 + (sp & 1)) * 64 + n * 16 + c) * DM + q * 8;
    f32x4 z = f32x4{};
#pragma unroll
    for (int ks = 0; ks < 20; ++ks) z = MFMA16(*(const bf16x8*)(gw + ks * 32 + q * 8), *(const bf16x8*)(xr + ks * 32), z);
    if (lane < 16) *(LAS float*)(uintptr_t)(lds0 + LDS_G + (n * 16 + lane) * 4) = 1.0f / (1.0f + __expf(-z[0]));
}
DI void attn_produce(const u16* __restrict__ x, const u16* __restrict__ gw, const char* __restrict__ ws, unsigned lds0, int pw) {
    const int lane = threadIdx.x & 63;
    const int b = blockIdx.x, head = (b & 7) * 4 + (b >> 6);
    unsigned so[6], sd[6];
#pragma unroll
    for (int j = 0; j < 6; ++j) {
        const int cc = min(j * 64 + lane, 359), tl = pw * 2 + (cc >= 180), c = cc >= 180 ? cc - 180 : cc;
        const int v = c - 96, g = v / 21, d = v - g * 21;
        so[j] = c < 64 ? WS_KA + (unsigned)(head * 64 + tl) * 1024 + c * 16
              : c < 96 ? WS_KB + (unsigned)(head * 64 + tl) * 512 + (c - 64) * 16
                       : WS_VV + (unsigned)((tl * 4 + g) * VP + (d < 20 ? head * 20 + d : 640)) * 16;
        sd[j] = c < 64 ? 4096u : c < 96 ? 2048u : 4 * VV_T;
    }
    auto issue = [&](unsigned g) {
        const unsigned dst = lds0 + kv_off(g) + pw * 2 * KVT_B;
#pragma unroll
        for (int j = 0; j < 5; ++j) { dma16c(ws, so[j], dst + j * 1024); so[j] += sd[j]; }
        dma16_lo<40>(ws, so[5], dst + 5 * 1024); so[5] += sd[5];
    };
#pragma unroll
    for (int j = 0; j < 6; ++j) { PIN(so[j]); PIN(sd[j]); }
    bar_raw();
    issue(0); issue(1); issue(2);
    if (pw == 0 && lane < 4) lds_put(lds0 + KV_FREE + lane * 4, 0u);
    lds_put(lds0 + KV_FULL + pw * 4, 1u);
    wait_vm<12>(); bar();
#pragma unroll 1
    for (unsigned g = 3; g < 13; ++g) { issue(g); wait_vm<12>(); lds_put(lds0 + KV_FULL + pw * 4, g - 1); }
    wait_vm<0>(); lds_put(lds0 + KV_FULL + pw * 4, 13u);
    gate16(x, gw, lds0, pw);
#pragma unroll 1
    for (unsigned g = 13; g < 16; ++g) {
        wait_vm<0>(); lds_put(lds0 + KV_FULL + pw * 4, g);
        kv_wait_free(lds0, g - 3);
        issue(g);
    }
    wait_vm<0>(); lds_put(lds0 + KV_FULL + pw * 4, 17u);
}
DI void attn_helper(const u16* __restrict__ cs, const u16* __restrict__ ss, const u16* __restrict__ wo,
                    const u16* __restrict__ x, const u16* __restrict__ gw, const char* __restrict__ ws, unsigned lds0, int hw) {
    const int lane = threadIdx.x & 63;
    const int b = blockIdx.x, xcd = b & 7, i = b >> 3;
    YG_MAP;
    bar_raw(); bar_raw();
    gate16(x, gw, lds0, 2 + hw);
    kv_wait_free(lds0, 2);
    const char* pfp = (const char*)(sp >= 2 ? ss : cs) + (size_t)(tb * 128 + eg * 32 + hw * 16) * 4096 + (sp & 1) * 2048;
#pragma unroll 1
    for (int n = 0; n < 16; ++n) { touch(pfp + n * 128, (lane >> 2) * 4096, lds0 + LDS_SCR); __builtin_amdgcn_s_sleep(8); }
    touch((const char*)wo + ((tb & 7) * 80 + hw * 40) * 1280 + (eg * 2 + (sp >> 1)) * 160, min(lane >> 1, 39) * 1280 + (lane & 1) * 156, lds0 + LDS_SCR);
    touch((const char*)wo + ((tb & 7) * 80 + hw * 40 + 32) * 1280 + (eg * 2 + (sp >> 1)) * 160, ((lane >> 1) & 7) * 1280 + (lane & 1) * 156, lds0 + LDS_SCR);
    wait_count((gu32*)(ws + WS_FLAG + (272 + (sp & 1)) * 128), 0u, 128u);
    kv_wait_free(lds0, 9);
#pragma unroll 1
    for (int n = 0; n < 6; ++n) { yg_issue(cs, ss, ws, lds0, hw * 2 + (n & 1), n >> 1); __builtin_amdgcn_s_sleep(4); }
}

DI void phase_tail(const u16* __restrict__ cs, const u16* __restrict__ ss, const u16* __restrict__ wo, char* __restrict__ ws, u16* __restrict__ out, unsigned lds0, int wid) {
    const int lane = threadIdx.x & 63, c = lane & 15, q = lane >> 4;
    const int b = blockIdx.x, xcd = b & 7, i = b >> 3;
    YG_MAP;
    const int sb = tb * 2 + (sp & 1), ng = eg * 2 + (sp >> 1);
    __builtin_amdgcn_s_setprio(0);
    if (wid >= 4) {
        const int lw = wid - 4;
        unsigned wv[5];
#pragma unroll
        for (int j = 0; j < 5; ++j) { const unsigned ci = (unsigned)(lw + 4 * j) * 64u + lane, row = (ci * 6554u) >> 16; wv[j] = row * 1280u + (ci - row * 10u) * 16u; PIN(wv[j]); }
        wait_vm<0>(); bar();
#pragma unroll 1
        for (int st = 3; st < 16; ++st) {
            yg_issue(cs, ss, ws, lds0, lw, st);
            wait_vm<18>(); bar_raw();
            if (st == 6 && threadIdx.x == 256) __hip_atomic_fetch_add((gu32*)(ws + WS_FLAG + (256 + (i & 7)) * 128), 1u, RLX_AGENT);
        }
#pragma unroll
        for (int j = 0; j < 25; ++j) {
            dma16((const char*)wo + ng * 160 + (j / 5) * 128 * 1280, wv[j % 5], lds0 + lw * 1024 + j * 4096);
            if (j == 8 || j == 17) { wait_vm<18>(); bar_raw(); }
        }
        wait_vm<0>(); bar_raw();
        wait_count((gu32*)(ws + WS_FLAG + ((tb * 4 + eg) * 4 + sp) * 128), 0u, 3u);
        const int t0 = (lw >> 1) * 3;
        const unsigned yb = WS_YP + (unsigned)(((tb * 4 + eg) * 4 + sp) * 4) * 10240u + (unsigned)((lw & 1) * 5 + t0) * 1024u + lane * 16;
        u32x4 yv[3][3];
#pragma unroll
        for (int t = 0; t < 3; ++t)
#pragma unroll
            for (int v = 0; v < 3; ++v) yv[v][t] = ld128(ws_rsrc(ws), yb + ((sp + 1 + v) & 3) * 10240 + (t0 + t < 5 ? t : 0) * 1024);
        SBAR();
        bar_raw();
        LAS f32x4* yw = (LAS f32x4*)(uintptr_t)(lds0 + lw * 6144 + lane * 16);
#pragma unroll
        for (int t = 0; t < 3; ++t)
#pragma unroll
            for (int u = 0; u < 2; ++u)
                yw[(u * 3 + t) * 64] = f32x4{(bflo(yv[0][t][2 * u]) + bflo(yv[1][t][2 * u])) + bflo(yv[2][t][2 * u]),
                                             (bfhi(yv[0][t][2 * u]) + bfhi(yv[1][t][2 * u])) + bfhi(yv[2][t][2 * u]),
                                             (bflo(yv[0][t][2 * u + 1]) + bflo(yv[1][t][2 * u + 1])) + bflo(yv[2][t][2 * u + 1]),
                                             (bfhi(yv[0][t][2 * u + 1]) + bfhi(yv[1][t][2 * u + 1])) + bfhi(yv[2][t][2 * u + 1])};
        bar();
        return;
    }
    const rsrc_t wr = ws_rsrc(ws);
    const int rh = wid & 1, kk = wid >> 1;
    bf16x8 fo[5][2][2];
    {
        const int ch = wid >> 1;
        f32x4 acc[4][5];
#pragma unroll
        for (int u = 0; u < 4; ++u)
#pragma unroll
            for (int t = 0; t < 5; ++t) acc[u][t] = f32x4{};
        bf16x8 frE[2][4], fnE[2][5], frO[2][4], fnO[2][5];
        const unsigned af = lds0 + 16384 + ch * 10240 + lane * 16;
        const unsigned a0 = lds0 + (unsigned)(rh * 64 + c) * 128u + (unsigned)((q ^ (c >> 1)) * 16);
        auto step = [&](bf16x8 (&fr)[2][4], bf16x8 (&fn)[2][5], const bf16x8 (&pr)[2][4], const bf16x8 (&pn)[2][5], int st, bool ld, bool mm) {
            lptr p, r0, r1;
            if (ld) {
                bar();
                const unsigned so = (st & 3) * F_STG;
                p = lbase(af + so); r0 = lbase(a0 + so); r1 = lbase((a0 ^ 64u) + so);
            }
#pragma unroll
            for (int k = 0; k < 2; ++k)
#pragma unroll
                for (int t = 0; t < 5; ++t) {
                    if (ld) {
                        fn[k][t] = lds128(p + t * 2048 + k * 1024);
                        if (t < 4) fr[k][t] = lds128((k ? r1 : r0) + t * 2048);
                    }
                    SBAR();
                    if (mm) {
#pragma unroll
                        for (int u = 0; u < 4; ++u) acc[u][t] = MFMA16(pn[k][t], pr[k][u], acc[u][t]);
                    }
                    SBAR();
                }
        };
        gu32* ocnt = (gu32*)(ws + WS_FLAG + (256 + (sb >> 2)) * 128);
        unsigned oseen = 0;
        const int poll_at = wid == 0 ? 8 : 99;
        step(frE, fnE, frO, fnO, 0, true, false);
#pragma unroll 1
        for (int st = 0; st < 14; st += 2) {
            step(frO, fnO, frE, fnE, st + 1, true, true);
            if (st == 2) wait_vm<0>();
            if (st == poll_at) poll1(oseen, ocnt);
            step(frE, fnE, frO, fnO, st + 2, true, true);
        }
        if (wid == 0) {
            asm volatile("s_waitcnt vmcnt(0)" : "+v"(oseen)::"memory");
            wait_count(ocnt, oseen, 32u);
        }
        step(frO, fnO, frE, fnE, 15, true, true);
        step(frE, fnE, frO, fnO, 16, false, true);
        const unsigned yp = WS_YP + (unsigned)(((tb * 4 + eg) * 4 + wid) * 4 + sp) * 10240u + lane * 16;
        u32x4 pk[2][5];
#pragma unroll
        for (int u = 0; u < 2; ++u)
#pragma unroll
            for (int t = 0; t < 5; ++t) {
                const f32x4 v0 = acc[2 * u][t], v1 = acc[2 * u + 1][t];
                pk[u][t] = u32x4{pk2(v0[0], v0[1]), pk2(v0[2], v0[3]), pk2(v1[0], v1[1]), pk2(v1[2], v1[3])};
            }
        if (wid != sp) {
#pragma unroll
            for (int u = 0; u < 2; ++u)
#pragma unroll
                for (int t = 0; t < 5; ++t) st128(wr, yp, (u * 5 + t) * 1024, pk[u][t]);
        } else {
#pragma unroll
            for (int u = 0; u < 2; ++u)
#pragma unroll
                for (int t = 0; t < 5; ++t) *(LAS u32x4*)(uintptr_t)(lds0 + (u ? F_RING : 102400) + t * 1024 + lane * 16) = pk[u][t];
        }
        SBAR();
        bar();
        wait_vm<0>();
        if (lane == 0 && wid != sp) __hip_atomic_fetch_add((gu32*)(ws + WS_FLAG + ((tb * 4 + eg) * 4 + wid) * 128), 1u, RLX_AGENT);
        const unsigned oa = WS_OA + (unsigned)((sb * 2 + rh) * NH + kk * 4) * 1024u + q * 512 + c * 16;
        const unsigned ob = WS_OB + (unsigned)((sb * 2 + rh) * NH + kk * 16) * 256u + q * 512 + c * 8;
#pragma unroll
        for (int j = 0; j < 4; ++j)
#pragma unroll
            for (int k = 0; k < 2; ++k)
#pragma unroll
                for (int u = 0; u < 2; ++u) fo[j][k][u] = as_frag(ld128(wr, oa + j * 8192 + k * 2048 + u * 256));
#pragma unroll
        for (int k = 0; k < 2; ++k)
#pragma unroll
            for (int u = 0; u < 2; ++u) {
                const u32x2 a = ld64(wr, ob + k * 2048 + u * 128), bq = ld64(wr, ob + k * 2048 + u * 128 + 256);
                fo[4][k][u] = as_frag(u32x4{a[0], a[1], bq[0], bq[1]});
            }
        SBAR();
    }
    const int s0 = sb * 64 + rh * 32 + c;
    f32x4 acc[2][5];
#pragma unroll
    for (int t = 0; t < 5; ++t) { acc[0][t] = f32x4{}; acc[1][t] = f32x4{}; }
    {
        const unsigned wl = lds0 + (unsigned)(c >> 2) * 160u + (c & 3) * 8;
        const lptr p0 = lbase(wl + (unsigned)(kk * 80 + (q >> 1) * 20 + (q & 1) * 4) * 160u), p1 = lbase(wl + (unsigned)(kk * 80 + (q >> 1) * 20 + (q & 1) * 4) * 160u + 51200);
        const lptr p2 = lbase(wl + (unsigned)(kk * 320 + q * 40 + 16) * 160u);
        bf16x8 fn[2][5];
        auto wof = [&](int n, int t) {
            const lptr pp = n < 4 ? p0 + (n >> 1) * 25600 + (n & 1) * 6400 : n < 8 ? p1 + ((n - 4) >> 1) * 25600 + (n & 1) * 6400 : p2 + (n & 1) * 25600;
            const s16x4 lo = ldstr(pp + t * 32), hi = ldstr(pp + t * 32 + (n < 8 ? 8 : 20) * 160);
            return __builtin_bit_cast(bf16x8, __builtin_shufflevector(lo, hi, 0, 1, 2, 3, 4, 5, 6, 7));
        };
#define WOF(n, t) wof(n, t)
#pragma unroll
        for (int t = 0; t < 5; ++t) fn[0][t] = WOF(0, t);
#pragma unroll
        for (int n = 0; n < 10; ++n)
#pragma unroll
            for (int t = 0; t < 5; ++t) {
                if (n < 9) fn[(n + 1) & 1][t] = WOF(n + 1, t);
                SBAR();
                acc[0][t] = MFMA16(fn[n & 1][t], fo[n >> 1][n & 1][0], acc[0][t]);
                acc[1][t] = MFMA16(fn[n & 1][t], fo[n >> 1][n & 1][1], acc[1][t]);
                SBAR();
            }
    }
    const float g0 = *(const LAS float*)(uintptr_t)(lds0 + LDS_G + (rh * 32 + c) * 4), g1 = *(const LAS float*)(uintptr_t)(lds0 + LDS_G + (rh * 32 + 16 + c) * 4);
    const int t0 = kk * 3;
    f32x4 fin[2][3];
    {
        LAS f32x4* xw = (LAS f32x4*)(uintptr_t)(lds0 + 3 * F_STG + wid * 6144 + lane * 16);
        const LAS f32x4* xr = (const LAS f32x4*)(uintptr_t)(lds0 + 3 * F_STG + (wid ^ 2) * 6144 + lane * 16);
        unsigned mk = 0u - (unsigned)kk;
        PIN(mk);
#pragma unroll
        for (int u = 0; u < 2; ++u)
#pragma unroll
            for (int t = 0; t < 3; ++t) {
                const f32x4 lo = acc[u][t], hi = acc[u][t < 2 ? 3 + t : 4];
                f32x4 snd;
#pragma unroll
                for (int e = 0; e < 4; ++e) { fin[u][t][e] = bsel(mk, hi[e], lo[e]); snd[e] = bsel(mk, lo[e], hi[e]); }
                xw[(u * 3 + t) * 64] = snd;
            }
        bar();
#pragma unroll
        for (int u = 0; u < 2; ++u)
#pragma unroll
            for (int t = 0; t < 3; ++t) fin[u][t] = (fin[u][t] + xr[(u * 3 + t) * 64]) * (u ? g1 : g0);
    }
#pragma unroll
    for (int t = 0; t < 3; ++t) {
        const u32x4 y = *(const LAS u32x4*)(uintptr_t)(lds0 + (rh ? F_RING : 102400) + (t0 + t < 5 ? t0 + t : 0) * 1024 + lane * 16);
#pragma unroll
        for (int u = 0; u < 2; ++u) fin[u][t] += f32x4{bflo(y[2 * u]), bfhi(y[2 * u]), bflo(y[2 * u + 1]), bfhi(y[2 * u + 1])} * (1.0f - (u ? g1 : g0));
    }
    bar_raw();
    const LAS f32x4* yr = (const LAS f32x4*)(uintptr_t)(lds0 + wid * 6144 + lane * 16);
    u16* op = out + (size_t)s0 * DM + ng * 80 + t0 * 16 + q * 4;
#pragma unroll
    for (int t = 0; t < 3; ++t) {
        if (t0 + t < 5) {
#pragma unroll
            for (int u = 0; u < 2; ++u) {
                const f32x4 v = fin[u][t] + yr[(u * 3 + t) * 64] * (1.0f - (u ? g1 : g0));
                *(u32x2*)(op + (size_t)u * 16 * DM + t * 16) = u32x2{pk2(v[0], v[1]), pk2(v[2], v[3])};
            }
        }
    }
}

constexpr int LDS_B = LDS_SCR + 256;

#define CODE_LINES 220
__global__ __launch_bounds__(512, 2) void k_all(const u16* __restrict__ x, const u16* __restrict__ wqkv, const u16* __restrict__ cd,
                                             const u16* __restrict__ sd, char* __restrict__ ws, const u16* __restrict__ wo, const u16* __restrict__ gw,
                                             const u16* __restrict__ cs, const u16* __restrict__ ss, u16* __restrict__ out) {
    extern __shared__ __attribute__((aligned(16))) char smem[];
    const unsigned lds0 = (unsigned)(uintptr_t)smem;
    const int wid = __builtin_amdgcn_readfirstlane(threadIdx.x >> 6);
    const int b = blockIdx.x, xcd = b & 7, i = b >> 3;
    const int rb = (xcd & 1) * 8 + (i & 7), cq = xcd >> 1, gl = i >> 3;
    asm volatile("" ::"s"(x), "s"(wqkv), "s"(cd), "s"(sd));
    if (wid < 4) {
        if (i < 2) {
            const char* pc;
            asm volatile("s_getpc_b64 %0" : "=s"(pc));
            touch((const char*)((uintptr_t)pc & ~(uintptr_t)127), min(threadIdx.x, (unsigned)CODE_LINES) * 128, lds0 + P_RING + 64);
        }
        if (threadIdx.x == 64) {
            const unsigned n = __hip_atomic_load((gu32*)(ws + WS_RSP + b * 4), RLX_AGENT) + 1u;
            __hip_atomic_store((gu32*)(ws + WS_REQ + b * 4), n, RLX_AGENT);
            *(volatile LAS unsigned*)(uintptr_t)(lds0 + P_RING) = n;
        }
        if (b == LEADER) {
            const unsigned tl = threadIdx.x;
            st128(ws_rsrc(ws), (tl * VP + 640) * 16, WS_VV, u32x4{ONES, ONES, ONES, ONES});
            st32(ws_rsrc(ws), tl * 128, WS_FLAG, 0u);
            if (tl < 18) st32(ws_rsrc(ws), (256 + tl) * 128, WS_FLAG, 0u);
            st32(ws_rsrc(ws), tl * 4, WS_ACK, 0u);
            wait_vm<0>();
        }
        asm volatile("s_waitcnt lgkmcnt(0)" ::: "memory");
        if (gl == 3) proj_mma<2>(ws, rb, cq, lds0, wid);
        else if (cq * 3 + gl < 8) proj_mma<0>(ws, rb, cq * 3 + gl, lds0, wid);
        else proj_mma<1>(ws, rb, cq * 3 + gl, lds0, wid);
        wait_vm<0>();
    } else proj_load(x, wqkv, cd, sd, ws, rb, cq, gl, lds0, wid - 4);
    bar();
    if (wid == 6) {
        const int lane = threadIdx.x & 63;
        if (lane < (gl != 3 ? 3 : 1)) __hip_atomic_fetch_add((gu32*)(ws + WS_FLAG + (lane ? 263 + ((cq * 3 + gl) & 3) * 2 + lane : 272 + (xcd & 1)) * 128), 1u, RLX_AGENT);
        wait_count((gu32*)(ws + WS_FLAG + (264 + xcd) * 128), 0u, 48u);
    }
    if (wid < 4) phase_attn(ws, lds0, wid);
    else if (wid < 6) attn_produce(x, gw, ws, lds0, wid - 4);
    else attn_helper(cs, ss, wo, x, gw, ws, lds0, wid - 6);
    phase_tail(cs, ss, wo, ws, out, lds0, wid);
}

extern "C" void kernel_launch(void* const* d_in, const int* in_sizes, int n_in, void* d_out, int out_size, void* d_ws, size_t ws_size,
                              hipStream_t stream) {
    const u16* x = (const u16*)d_in[0];
    const u16* wqkv = (const u16*)d_in[1];
    const u16* wo = (const u16*)d_in[2];
    const u16* cs = (const u16*)d_in[4];
    const u16* ss = (const u16*)d_in[5];
    const u16* cd = (const u16*)d_in[6];
    const u16* sd = (const u16*)d_in[7];
    const u16* gw = (const u16*)d_in[8];
    char* ws = (char*)d_ws;
    (void)hipFuncSetAttribute((const void*)k_all, hipFuncAttributeMaxDynamicSharedMemorySize, LDS_B);
    k_all<<<256, 512, LDS_B, stream>>>(x, wqkv, cd, sd, ws, wo, gw, cs, ss, (u16*)d_out);
}
```

```cpp
#include <hip/hip_runtime.h>
#include <hip/hip_bf16.h>
#include <stdint.h>

typedef unsigned short u16;
typedef __attribute__((ext_vector_type(8))) __bf16 bf16x8;
typedef __attribute__((ext_vector_type(4))) short s16x4;
typedef __attribute__((ext_vector_type(16))) float f32x16;
typedef __attribute__((ext_vector_type(4))) float f32x4;
typedef __attribute__((ext_vector_type(4))) unsigned u32x4;
typedef __attribute__((ext_vector_type(2))) unsigned u32x2;
#define LAS __attribute__((address_space(3)))
#define DI __device__ __forceinline__
typedef const LAS char* lptr;

constexpr int SEQ = 2048, DM = 640, NH = 32;
constexpr int VP = 648;
constexpr unsigned WS_QA = 0;
constexpr unsigned WS_KA = WS_QA + NH * 64 * 1024;
constexpr unsigned WS_QB = WS_KA + NH * 64 * 1024;
constexpr unsigned WS_KB = WS_QB + NH * 64 * 512;
constexpr unsigned WS_VV = WS_KB + NH * 64 * 512;
constexpr unsigned WS_AB = WS_VV + 64 * 4 * VP * 16;
constexpr unsigned WS_YP = WS_AB + 40 * 128 * 1024;
constexpr unsigned WS_OA = WS_YP + 4 * 64 * 40 * 1024;
constexpr unsigned WS_OB = WS_OA + 64 * NH * 1024;
constexpr unsigned WS_FLAG = WS_OB + 64 * NH * 256;
constexpr unsigned WS_REQ = WS_FLAG + 274 * 128;
constexpr unsigned WS_RSP = WS_REQ + 1024;
constexpr unsigned WS_ACK = WS_RSP + 1024;
constexpr unsigned WS_END = WS_ACK + 1024;
constexpr float QSCALE = 0.22360679774997896f * 1.4426950408889634f;
constexpr int SC1 = 16;

#define MFMA32(a, b, c) __builtin_amdgcn_mfma_f32_32x32x16_bf16((a), (b), (c), 0, 0, 0)
#define MFMA8(a, b, c) __builtin_amdgcn_mfma_f32_32x32x8bf16_1k(__builtin_bit_cast(s16x4, a), __builtin_bit_cast(s16x4, b), (c), 0, 0, 0)
#define MFMA16(a, b, c) __builtin_amdgcn_mfma_f32_16x16x32_bf16((a), (b), (c), 0, 0, 0)
#define SBAR() __builtin_amdgcn_sched_barrier(0)
#define PIN(x) asm volatile("" : "+v"(x))

typedef __attribute__((ext_vector_type(2))) __bf16 bf16x2;
typedef __attribute__((ext_vector_type(2))) float f32x2;
DI unsigned pk2(float lo, float hi) {
    f32x2 v = {lo, hi};
    return __builtin_bit_cast(unsigned, __builtin_convertvector(v, bf16x2));
}
DI float bsel(unsigned m, float a, float b) { return __uint_as_float((__float_as_uint(a) & m) | (__float_as_uint(b) & ~m)); }
DI float bflo(unsigned v) { return __uint_as_float(v << 16); }
DI float bfhi(unsigned v) { return __uint_as_float(v & 0xFFFF0000u); }
DI void dma16(const void* sbase, unsigned voff, unsigned lds) {
    asm volatile("s_mov_b32 m0, %2\n\ts_nop 0\n\tglobal_load_lds_dwordx4 %0, %1" ::"v"(voff), "s"(sbase), "s"(lds) : "memory");
}
DI void dma16c(const void* sbase, unsigned voff, unsigned lds) {
    asm volatile("s_mov_b32 m0, %2\n\ts_nop 0\n\tglobal_load_lds_dwordx4 %0, %1 sc1" ::"v"(voff), "s"(sbase), "s"(lds) : "memory");
}
template <int N> DI void dma16_lo(const void* sbase, unsigned voff, unsigned lds) {
    unsigned long long sv;
    asm volatile("s_mov_b32 m0, %3\n\ts_mov_b64 %0, exec\n\ts_mov_b64 exec, %4\n\tglobal_load_lds_dwordx4 %1, %2 sc1\n\ts_mov_b64 exec, %0"
                 : "=&s"(sv) : "v"(voff), "s"(sbase), "s"(lds), "s"((1ull << N) - 1) : "memory");
}
DI void touch(const void* sbase, unsigned voff, unsigned lds) {
    asm volatile("s_mov_b32 m0, %2\n\ts_nop 0\n\tglobal_load_lds_dword %0, %1" ::"v"(voff), "s"(sbase), "s"(lds) : "memory");
}
template <int N> DI void wait_vm() { asm volatile("s_waitcnt vmcnt(%0)" ::"i"(N) : "memory"); }
DI void bar() { asm volatile("s_waitcnt lgkmcnt(0)\n\ts_barrier" ::: "memory"); }
DI void bar_raw() { asm volatile("s_barrier" ::: "memory"); }
DI lptr lbase(unsigned a) { asm volatile("" : "+v"(a)); return (lptr)(uintptr_t)a; }
DI bf16x8 lds128(lptr p) { return *(const LAS bf16x8*)p; }
DI u32x2 lds64(lptr p) { return *(const LAS u32x2*)p; }
DI s16x4 ldstr(lptr p) {
    typedef short v4i16_t __attribute__((ext_vector_type(4)));
    return __builtin_bit_cast(s16x4, __builtin_amdgcn_ds_read_tr16_b64_v4i16((LAS v4i16_t*)p));
}
DI u32x4 pack8(const f32x16& x, int s, float sc) {
    u32x4 p;
    p[0] = pk2(x[8 * s + 0] * sc, x[8 * s + 1] * sc);
    p[1] = pk2(x[8 * s + 2] * sc, x[8 * s + 3] * sc);
    p[2] = pk2(x[8 * s + 4] * sc, x[8 * s + 5] * sc);
    p[3] = pk2(x[8 * s + 6] * sc, x[8 * s + 7] * sc);
    return p;
}
typedef __amdgpu_buffer_rsrc_t rsrc_t;
DI rsrc_t ws_rsrc(const void* ws) { return __builtin_amdgcn_make_buffer_rsrc((void*)ws, (short)0, (int)WS_END, 0x00020000); }
DI void st128(rsrc_t r, unsigned voff, unsigned soff, u32x4 v) { __builtin_amdgcn_raw_buffer_store_b128(v, r, voff + soff, 0, SC1); }
DI void st64(rsrc_t r, unsigned voff, unsigned soff, u32x2 v) { __builtin_amdgcn_raw_buffer_store_b64(v, r, voff, soff, SC1); }
DI void st32(rsrc_t r, unsigned voff, unsigned soff, unsigned v) { __builtin_amdgcn_raw_buffer_store_b32(v, r, voff, soff, SC1); }
DI u32x4 ld128(rsrc_t r, unsigned voff) { return __builtin_amdgcn_raw_buffer_load_b128(r, voff, 0, SC1); }
DI u32x2 ld64(rsrc_t r, unsigned voff) { return __builtin_amdgcn_raw_buffer_load_b64(r, voff, 0, SC1); }
DI bf16x8 as_frag(u32x4 v) { return __builtin_bit_cast(bf16x8, v); }
typedef __attribute__((ext_vector_type(16))) __bf16 bf16x16;
template <int HI> DI f32x16 pv_mma(bf16x8 v, const f32x16& p, const f32x16& o, int idx) {
    const auto ph = __builtin_shufflevector(p, p, 8 * HI, 8 * HI + 1, 8 * HI + 2, 8 * HI + 3, 8 * HI + 4, 8 * HI + 5, 8 * HI + 6, 8 * HI + 7);
    return __builtin_amdgcn_smfmac_f32_32x32x32_bf16(v, __builtin_bit_cast(bf16x16, ph), o, idx, 0, 0);
}
DI u32x4 pack8n(const f32x16& x, int s, float sc) {
    const u32x4 p = pack8(x, s, sc);
    const auto r0 = __builtin_amdgcn_permlane32_swap(p[0], p[2], false, false);
    const auto r1 = __builtin_amdgcn_permlane32_swap(p[1], p[3], false, false);
    return u32x4{r0[0], r1[0], r0[1], r1[1]};
}

typedef __attribute__((address_space(1))) unsigned gu32;
#define RLX_AGENT __ATOMIC_RELAXED, __HIP_MEMORY_SCOPE_AGENT
DI void poll1(unsigned& v, gu32* c) {
    unsigned long long sv;
    asm volatile("s_mov_b64 %1, exec\n\ts_mov_b64 exec, 1\n\tglobal_load_dword %0, %2, %3 sc1\n\ts_mov_b64 exec, %1" : "+v"(v), "=&s"(sv) : "v"(0u), "s"(c) : "memory");
}
DI void wait_count(gu32* c, unsigned seen, unsigned want) {
#pragma unroll 1
    for (unsigned spins = 0; (unsigned)__builtin_amdgcn_readfirstlane(seen) < want && spins < (1u << 17); ++spins) {
        __builtin_amdgcn_s_sleep(4);
        poll1(seen, c);
        asm volatile("s_waitcnt vmcnt(0)" : "+v"(seen)::"memory");
    }
    asm volatile("" ::: "memory");
}

constexpr int LEADER = 255;
DI void echo_round(u32x4& v, const char* ws, bool put) {
    const unsigned o = (threadIdx.x & 63) * 16;
    if (put) asm volatile("global_store_dwordx4 %0, %1, %2 sc1\n\ts_nop 1" ::"v"(o), "v"(v), "s"(ws + WS_RSP) : "memory");
    asm volatile("global_load_dwordx4 %0, %1, %2 sc1" : "+v"(v) : "v"(o), "s"(ws + WS_REQ) : "memory");
}

constexpr int P_STG = 45056;
constexpr int P_RING = 3 * P_STG;

DI void proj_load(const u16* __restrict__ x, const u16* __restrict__ wqkv, const u16* __restrict__ cd, const u16* __restrict__ sd,
                  const char* __restrict__ ws, int rb, int cq, int gl, unsigned lds0, int wid) {
    const int lane = threadIdx.x & 63;
    constexpr int NDMA = 10;
    const bool pa = gl != 3;
    const unsigned vrow = (unsigned)(lane >> 3) * 1280u + (unsigned)(((lane & 7) ^ ((4 * (wid & 1) + (lane >> 4)) & 7)) * 16);
    const char* xb = (const char*)x + ((size_t)rb * 128 + wid * 8) * 1280;
    int st = 3 * ((rb + gl) % 3);
    unsigned slot = lds0 + wid * 1024;
#pragma unroll
    for (int j = 0; j < 4; ++j) dma16(xb + j * 40960 + st * 128, vrow, slot + j * 4096);
    const char* wb[7];
    unsigned vo[5];
    const int dt0 = pa ? 28 + cq * 3 + gl : 7 * cq;
#pragma unroll
    for (int j = 0; j < 7; ++j) {
        const int dt = dt0 + j;
        const char* d = (const char*)(dt >= 20 ? sd : cd) + ((dt >= 20 ? dt - 20 : dt) * 32 + wid * 8) * 1280;
        wb[j] = pa && j ? (const char*)wqkv + (cq * 3 + gl) * 320 : d;
    }
#pragma unroll
    for (int j = 0; j < 5; ++j) { const unsigned i = (wid + 4 * j) * 64 + lane, q = (i * 3277u) >> 16; vo[j] = pa ? q * 3840u + (i - q * 20u) * 16u : vrow; }
    const int wstep = pa ? 64 * 3840 : 128;
    auto issue = [&](bool with_x) {
        if (with_x) {
#pragma unroll
            for (int j = 0; j < 4; ++j) dma16(xb + j * 40960 + st * 128, vrow, slot + j * 4096);
        }
        if (!pa) dma16(wb[6] + st * 128, vrow, slot + 40960);
        dma16(wb[0] + st * 128, vrow, slot + 16384);
#pragma unroll
        for (int j = 0; j < 5; ++j) dma16(wb[1 + j] + st * wstep, vo[j], slot + 20480 + j * 4096);
        slot = slot + P_STG == lds0 + wid * 1024 + P_RING ? lds0 + wid * 1024 : slot + P_STG;
        st = st == 9 ? 0 : st + 1;
    };
    const bool leader = blockIdx.x == LEADER && wid == 3;
    u32x4 rq = {0u, 0u, 0u, 0u};
    unsigned back = 0;
#pragma unroll 1
    for (int n = 0; n < 3; ++n) {
        issue(n > 0);
        if (n == 1) { wait_vm<NDMA>(); bar_raw(); }
    }
    wait_vm<NDMA>(); bar_raw(); bar_raw();
#pragma unroll 1
    for (int n = 3; n < 10; ++n) {
        if (leader) echo_round(rq, ws, n > 3);
        if (wid == 0 && n == 9) poll1(back, (gu32*)(ws + WS_RSP + blockIdx.x * 4));
        issue(true); wait_vm<NDMA>(); bar_raw(); bar_raw();
    }
    wait_vm<0>();
    bar_raw();
    bool acked = wid != 0;
    const unsigned want = __builtin_amdgcn_readfirstlane(*(const volatile LAS unsigned*)(uintptr_t)(lds0 + P_RING));
    PIN(back);
    if (!acked && (unsigned)__builtin_amdgcn_readfirstlane(back) == want) {
        if (lane == 0) __hip_atomic_store((gu32*)(ws + WS_ACK + blockIdx.x * 4), 1u, RLX_AGENT);
        acked = true;
    }
    bar_raw(); bar_raw(); bar_raw();
    if (!acked) {
#pragma unroll 1
        for (unsigned spins = 0; spins < (1u << 17) && (unsigned)__builtin_amdgcn_readfirstlane(back) != want; ++spins) {
            __builtin_amdgcn_s_sleep(4);
            poll1(back, (gu32*)(ws + WS_RSP + blockIdx.x * 4));
            asm volatile("s_waitcnt vmcnt(0)" : "+v"(back)::"memory");
        }
        if (lane == 0) __hip_atomic_store((gu32*)(ws + WS_ACK + blockIdx.x * 4), 1u, RLX_AGENT);
    }
    if (leader) {
#pragma unroll 1
        for (unsigned spins = 0; spins < (1u << 17); ++spins) {
            echo_round(rq, ws, true);
            u32x4 ak;
            asm volatile("global_load_dwordx4 %0, %2, %3 sc1\n\ts_waitcnt vmcnt(0)" : "=&v"(ak), "+v"(rq) : "v"(lane * 16), "s"(ws + WS_ACK) : "memory");
            if (__all((ak[0] & ak[1] & ak[2] & ak[3]) == 1u)) break;
            __builtin_amdgcn_s_sleep(4);
        }
    }
}

template <int PATH>
DI void proj_mma(char* __restrict__ ws, int rb, int grp, unsigned lds0, int wid) {
    const int lane = threadIdx.x & 63, r = lane & 31, h = lane >> 5;
    constexpr bool PATH_A = PATH < 2;
    constexpr int NW = PATH_A ? 5 : 0, ND = PATH_A ? 1 : 7, NT = NW + ND;
    const int dt0 = PATH_A ? 28 + grp : 7 * grp;
    const unsigned sw = lds0 + (unsigned)r * 128u + (unsigned)((h ^ ((r >> 1) & 7)) * 16);
    const unsigned awo = lds0 + 20480u + (unsigned)(8 * h + ((lane & 15) >> 2)) * 320u + (unsigned)(((lane >> 4) & 1) * 32 + (lane & 3) * 8);
    f32x16 acc[NT];
#pragma unroll
    for (int t = 0; t < NT; ++t) acc[t] = f32x16{};
    bf16x8 fx[2], fw[2][NT];
    lptr pr, px, pw;
    auto setp = [&](unsigned so, int ks) {
        pr = lbase((sw ^ (unsigned)(ks * 32)) + so);
        px = lbase((sw ^ (unsigned)(ks * 32)) + so + wid * 4096);
        if (PATH_A && ks == 0) pw = lbase(awo + so);
    };
    auto rd = [&](int ks, int b, int t) {
        if (t == 0) fx[b] = lds128(px);
        if (t < ND) fw[b][t] = lds128(pr + 16384 + t * 4096);
        else {
            s16x4 lo = ldstr(pw + ks * 16 * 320 + (t - ND) * 64), hi = ldstr(pw + ks * 16 * 320 + 4 * 320 + (t - ND) * 64);
            fw[b][t] = __builtin_bit_cast(bf16x8, __builtin_shufflevector(lo, hi, 0, 1, 2, 3, 4, 5, 6, 7));
        }
    };
    auto mm = [&](int b, int t) {
        if (PATH == 0 && t >= ND) acc[t] = MFMA32(fw[b][t], fx[b], acc[t]);
        else acc[t] = MFMA32(fx[b], fw[b][t], acc[t]);
    };
    unsigned so = 0;
    setp(0, 0);
#pragma unroll
    for (int t = 0; t < NT; ++t) PIN(acc[t]);
    bar_raw();
#pragma unroll
    for (int t = 0; t < NT; ++t) rd(0, 0, t);
    SBAR();
#pragma unroll 1
    for (int st = 0; st < 10; ++st) {
#pragma unroll
        for (int ks = 0; ks < 4; ++ks) {
            if (ks == 3) { bar_raw(); so = so == 2 * P_STG ? 0 : so + P_STG; }
            setp(so, (ks + 1) & 3);
#pragma unroll
            for (int t = 0; t < NT; ++t) { rd((ks + 1) & 3, (ks + 1) & 1, t); SBAR(); mm(ks & 1, t); SBAR(); }
        }
        bar_raw();
    }
    const rsrc_t wr = ws_rsrc(ws);
    const int stile = rb * 4 + wid;
    if (PATH == 0) {
        const float sc = grp < 4 ? QSCALE : 1.f;
        const unsigned t0 = (unsigned)(grp * 8 * 64 + stile);
        const unsigned va = WS_QA + t0 * 1024 + lane * 16, vb = WS_QB + (t0 + h * 64) * 512 + r * 16;
#pragma unroll
        for (int m = 0; m < 4; ++m) {
            u32x2 q[5];
#pragma unroll
            for (int i = 0; i < 5; ++i) {
                const int p = 5 * m + i, t = ND + (p >> 2), g = p & 3;
                q[i][0] = pk2(acc[t][4 * g] * sc, acc[t][4 * g + 1] * sc);
                q[i][1] = pk2(acc[t][4 * g + 2] * sc, acc[t][4 * g + 3] * sc);
            }
            st128(wr, va, (2 * m) * 64 * 1024, u32x4{q[0][0], q[0][1], q[1][0], q[1][1]});
            st128(wr, va, (2 * m + 1) * 64 * 1024, u32x4{q[3][0], q[3][1], q[4][0], q[4][1]});
            st128(wr, vb, (2 * m) * 64 * 512, u32x4{q[2][0], q[2][1], 0x00003F80u, 0u});
        }
    } else if (PATH == 1) {
        const unsigned vv = WS_VV + (unsigned)((stile * 4 + h) * VP + (grp - 8) * 160 + r) * 16;
#pragma unroll
        for (int t = 0; t < 5; ++t) {
            st128(wr, vv, t * 512, pack8n(acc[ND + t], 0, 1.f));
            st128(wr, vv, t * 512 + 2 * VP * 16, pack8n(acc[ND + t], 1, 1.f));
        }
    }
#pragma unroll
    for (int t = 0; t < ND; ++t) {
        const float sc = dt0 + t >= 20 ? -1.f : 1.f;
        LAS u32x4* d = (LAS u32x4*)(uintptr_t)(lds0 + t * 8192 + wid * 2048 + lane * 16);
        d[0] = pack8n(acc[t], 0, sc); d[64] = pack8n(acc[t], 1, sc);
    }
}
#define YG_MAP const int tb = (xcd >> 2) * 8 + (i & 7), eg = i >> 3, sp = xcd & 3
constexpr int F_STG = 36864;
constexpr int F_RING = 4 * F_STG;
DI void yg_issue(const u16* __restrict__ cs, const u16* __restrict__ ss, const char* __restrict__ ws, unsigned lds0, int lw, int st) {
    const int lane = threadIdx.x & 63;
    const int b = blockIdx.x, xcd = b & 7, i = b >> 3;
    YG_MAP;
    const unsigned vrow = (unsigned)(lane >> 3) * 4096u + (unsigned)(((lane & 7) ^ ((4 * (lw & 1) + (lane >> 4)) & 7)) * 16);
    const char* rp = (const char*)(sp >= 2 ? ss : cs) + (size_t)(tb * 128 + lw * 8) * 4096 + (sp & 1) * 2048;
    const char* ab = ws + WS_AB + (size_t)((eg * 10 + (lw >> 1)) * 128 + sp * 32 + (lw & 1)) * 1024;
    const unsigned d = lds0 + lw * 1024 + (st & 3) * F_STG;
#pragma unroll
    for (int j = 0; j < 4; ++j) dma16(rp + j * 32 * 4096 + st * 128, vrow, d + j * 4096);
#pragma unroll
    for (int j = 0; j < 5; ++j) dma16c(ab + (j * 256 + st * 2) * 1024, lane * 16, d + 16384 + j * 4096);
}

DI float xhalf_max(float v) {
    auto rr = __builtin_amdgcn_permlane32_swap(__float_as_uint(v), __float_as_uint(v), false, false);
    return fmaxf(__uint_as_float(rr[0]), __uint_as_float(rr[1]));
}
DI float max16(const f32x16& s) {
    float a = fmaxf(fmaxf(s[0], s[1]), s[2]), b = fmaxf(fmaxf(s[3], s[4]), s[5]);
    a = fmaxf(fmaxf(a, s[6]), s[7]); b = fmaxf(fmaxf(b, s[8]), s[9]);
    a = fmaxf(fmaxf(a, s[10]), s[11]); b = fmaxf(fmaxf(b, s[12]), s[13]);
    a = fmaxf(fmaxf(a, s[14]), s[15]);
    return fmaxf(a, b);
}
#define EX2(v) __builtin_amdgcn_exp2f(v)
constexpr unsigned VV_T = 4 * VP * 16, VV_S = 2 * VP * 16;
constexpr unsigned ONES = 0x3F803F80u;

DI u32x4 fix_v(u32x4 v, int r) {
#pragma unroll
    for (int i = 0; i < 4; ++i) v[i] = r < 20 ? v[i] : (r == 20 ? ONES : 0u);
    return v;
}
DI f32x16 attn_safe(rsrc_t wr, int head, u32x4 qa, u32x4 qb, int lane) {
    const int r = lane & 31, h = lane >> 5;
    qb[2] = 0u;
    f32x16 o = f32x16{};
    float m = -INFINITY;
#pragma unroll 1
    for (int t = 0; t < 64; ++t) {
        const u32x4 ka = ld128(wr, WS_KA + (unsigned)(head * 64 + t) * 1024 + lane * 16);
        const u32x4 kb = ld128(wr, WS_KB + (unsigned)(head * 64 + t) * 512 + r * 16);
        const unsigned vp = WS_VV + (unsigned)((t * 4 + h) * VP + head * 20 + (r < 20 ? r : 0)) * 16;
        const u32x4 v0 = fix_v(ld128(wr, vp), r), v1 = fix_v(ld128(wr, vp + VV_S), r);
        f32x16 s = MFMA32(as_frag(ka), as_frag(qa), f32x16{});
        s = MFMA32(as_frag(kb), as_frag(qb), s);
        const float mn = fmaxf(m, xhalf_max(max16(s)));
        const float al = EX2(m - mn);
        m = mn;
#pragma unroll
        for (int i = 0; i < 16; ++i) { o[i] *= al; s[i] = EX2(s[i] - mn); }
        o = pv_mma<0>(as_frag(v0), s, o, (int)0xDDDDDDDD);
        o = pv_mma<1>(as_frag(v1), s, o, (int)0xDDDDDDDD);
    }
    return o;
}

DI void attn_finish(f32x16 o, rsrc_t wr, int head, int qt) {
    const int lane = threadIdx.x & 63, r = lane & 31, h = lane >> 5;
    auto rl = __builtin_amdgcn_permlane32_swap(__float_as_uint(o[8]), __float_as_uint(o[8]), false, false);
    float l = __uint_as_float(rl[1]);
    float z = 0.f;
#pragma unroll
    for (int k = 0; k < 12; ++k) z = __builtin_fmaf(o[k], 0.f, z);
    const bool ok = (z == 0.f) && (l >= 8.6736174e-19f) && (l <= 1.329228e36f);
    if (__builtin_expect(!__all(ok), 0)) {
        const u32x4 q0 = ld128(wr, WS_QA + (unsigned)(head * 64 + qt) * 1024 + lane * 16);
        const u32x4 q1 = ld128(wr, h ? 0x80000000u : WS_QB + (unsigned)(head * 64 + qt) * 512 + r * 16);
        o = attn_safe(wr, head, q0, q1, lane);
        rl = __builtin_amdgcn_permlane32_swap(__float_as_uint(o[8]), __float_as_uint(o[8]), false, false);
        l = __uint_as_float(rl[1]);
    }
    const float inv = __builtin_amdgcn_rcpf(l);
    const unsigned ot = (unsigned)(qt * NH + head);
    st128(wr, lane * 16, WS_OA + ot * 1024, pack8(o, 0, inv));
    if (h == 0) st64(wr, r * 8, WS_OB + ot * 256, u32x2{pk2(o[8] * inv, o[9] * inv), pk2(o[10] * inv, o[11] * inv)});
}

constexpr int KVT_B = 2880, KVG_B = 4 * KVT_B, KV1 = 3 * F_STG;
constexpr int KV_FULL = KV1 + 4 * KVG_B;
constexpr int KV_FREE = KV_FULL + 16;
constexpr int LDS_G = KV_FREE + 16;
constexpr int LDS_SCR = LDS_G + 256;
DI unsigned kv_off(unsigned g) { return g < 9 ? g * KVG_B : KV1 + ((g - 9) & 3) * KVG_B; }
DI void kv_wait_free(unsigned lds0, unsigned n) {
    for (;;) {
        const u32x4 f = *(const volatile LAS u32x4*)(uintptr_t)(lds0 + KV_FREE);
        if (__builtin_amdgcn_readfirstlane(min(min(f[0], f[1]), min(f[2], f[3]))) >= n) break;
        __builtin_amdgcn_s_sleep(8);
    }
    asm volatile("" ::: "memory");
}
DI void lds_put(unsigned addr, unsigned v) { *(volatile LAS unsigned*)(uintptr_t)addr = v; }

DI void phase_attn(char* __restrict__ ws, unsigned lds0, int wid) {
    const int lane = threadIdx.x & 63, r = lane & 31, h = lane >> 5;
    const int b = blockIdx.x, head = (b & 7) * 4 + (b >> 6), qt0 = ((b >> 3) & 7) * 8 + wid * 2;
    const rsrc_t wr = ws_rsrc(ws);
    bar_raw();
    u32x4 qa[2];
    u32x2 qbv[2];
#pragma unroll
    for (int i = 0; i < 2; ++i) {
        qa[i] = ld128(wr, WS_QA + (unsigned)(head * 64 + qt0 + i) * 1024 + lane * 16);
        qbv[i] = ld64(wr, h ? 0x80000000u : WS_QB + (unsigned)(head * 64 + qt0 + i) * 512 + r * 16);
    }
    const int rv = r <= 20 ? r : 0x8000;
    const unsigned oka = lds0 + lane * 16, okb = lds0 + 1024 + r * 16 + h * 8, ov = lds0 + 1536u + (unsigned)(h * 21 + rv) * 16u;
    unsigned afull = lds0 + KV_FULL, afree = lds0 + KV_FREE + wid * 4;
    PIN(afull); PIN(afree);
    __builtin_amdgcn_s_setprio(2);
    bar_raw();
    bf16x8 ka[2], v0[2], v1[2];
    u32x2 kb[2];
    lptr pka = lbase(oka), pkb = lbase(okb), pv = lbase(ov), pkn;
#define LDK(n, T) do { ka[n] = lds128(pka + (T) * KVT_B); kb[n] = lds64(pkb + (T) * KVT_B); SBAR(); } while (0)
#define LDV(n, T) do { v0[n] = lds128(pv + (T) * KVT_B); v1[n] = lds128(pv + (T) * KVT_B + 42 * 16); SBAR(); } while (0)
    LDK(0, 0); LDK(1, 1); LDV(0, 0);
#pragma unroll
    for (int i = 0; i < 2; ++i) {
        f32x16 t = MFMA32(ka[0], as_frag(qa[i]), f32x16{});
        t = MFMA8(kb[0], qbv[i], t);
        const float m0 = xhalf_max(max16(t));
        qbv[i][0] = h ? (pk2(-(m0 + 40.f), 0.f) & 0xFFFFu) : qbv[i][0];
    }
    f32x16 oA = f32x16{}, oB = f32x16{}, s0, s1, s2, s3 = f32x16{};
    int idx = (int)0xDDDDDDDD;
    PIN(idx);
    v0[1] = ka[0]; v1[1] = ka[0];
    s0 = MFMA32(ka[0], as_frag(qa[0]), f32x16{});
    s0 = MFMA8(kb[0], qbv[0], s0);
    SBAR();
#define GRP(MF, H, S, i) do { MF; SBAR(); H; SBAR(); S[i] = EX2(S[i]); S[i + 1] = EX2(S[i + 1]); S[i + 2] = EX2(S[i + 2]); S[i + 3] = EX2(S[i + 3]); PIN(S); SBAR(); } while (0)
#define SLOT(X, N, kn, Q, P, O, vn, H0, H1, H2, H3) do { \
        GRP(N = MFMA32(ka[kn], as_frag(qa[Q]), f32x16{}), H0, X, 0); \
        GRP(N = MFMA8(kb[kn], qbv[Q], N), H1, X, 4); \
        GRP(O = pv_mma<0>(v0[vn], P, O, idx), H2, X, 8); \
        GRP(O = pv_mma<1>(v1[vn], P, O, idx), H3, X, 12); } while (0)
#define RKA(n, T) ka[n] = lds128(pka + (T) * KVT_B)
#define RKB(n, T) kb[n] = lds64(pkb + (T) * KVT_B)
#define RV0(n, T) v0[n] = lds128(pv + (T) * KVT_B)
#define RV1(n, T) v1[n] = lds128(pv + (T) * KVT_B + 42 * 16)
#define LGKM0 __builtin_amdgcn_s_waitcnt(0xC07F)
#define NOP (void)0
    unsigned nx = KVG_B, gv = 0, need = 2;
    lptr pvn;
#define SPIN(x) asm volatile("" : "+s"(x))
#define GROUP(WRAP) do { \
        u32x2 full; unsigned fm, sfull; \
        SLOT(s0, s1, 0, 1, s3, oB, 1, LGKM0, RKA(0, 2), RKB(0, 2), RV0(1, 1)); \
        SLOT(s1, s2, 1, 0, s0, oA, 0, RV1(1, 1), full = *(const volatile LAS u32x2*)(uintptr_t)afull, do { gv += 1; PIN(gv); } while (0), pkn = lbase(okb + nx)); \
        SLOT(s2, s3, 1, 1, s1, oB, 0, LGKM0, RKA(1, 3), RKB(1, 3), RV0(0, 2)); \
        SLOT(s3, s0, 0, 0, s2, oA, 1, RV1(0, 2), do { fm = min(full[0], full[1]); PIN(fm); } while (0), do { sfull = __builtin_amdgcn_readfirstlane(fm); SPIN(sfull); } while (0), pka = lbase(oka + nx)); \
        pkb = pkn; \
          \
        SLOT(s0, s1, 0, 1, s3, oB, 1, \
             do { if (__builtin_expect(sfull < need, 0)) { \
                      do { __builtin_amdgcn_s_sleep(1); full = *(const volatile LAS u32x2*)(uintptr_t)afull; } while ((unsigned)__builtin_amdgcn_readfirstlane(min(full[0], full[1])) < need); } \
                  asm volatile("" ::: "memory"); LGKM0; } while (0), \
             RKA(0, 0), RKB(0, 0), RV0(1, 3)); \
        SLOT(s1, s2, 1, 0, s0, oA, 0, RV1(1, 3), pvn = lbase(ov + nx), do { nx += KVG_B; SPIN(nx); } while (0), \
             do { if (WRAP) { nx = nx == 9 * KVG_B ? KV1 : nx; SPIN(nx); } } while (0)); \
        pv = pvn; \
        SLOT(s2, s3, 1, 1, s1, oB, 0, LGKM0, RKA(1, 1), RKB(1, 1), RV0(0, 0)); \
        SLOT(s3, s0, 0, 0, s2, oA, 1, RV1(0, 0), lds_put(afree, gv)  , do { need += 1; SPIN(need); } while (0), \
             do { if (WRAP) { nx = nx == KV1 + 4 * KVG_B ? KV1 : nx; SPIN(nx); } } while (0)); } while (0)
#pragma unroll 1
    do { GROUP(false); GROUP(true); } while (need < 18);
    oB = pv_mma<0>(v0[1], s3, oB, idx);
    oB = pv_mma<1>(v1[1], s3, oB, idx);
    __builtin_amdgcn_s_setprio(0);
    attn_finish(oA, wr, head, qt0);
    attn_finish(oB, wr, head, qt0 + 1);
}
DI void gate16(const u16* __restrict__ x, const u16* __restrict__ gw, unsigned lds0, int n) {
    const int lane = threadIdx.x & 63, c = lane & 15, q = lane >> 4, b = blockIdx.x, xcd = b & 7, i = b >> 3;
    YG_MAP;
    (void)eg;
    __builtin_amdgcn_s_setprio(3);
    const u16* xr = x + (size_t)((tb * 2 + (sp & 1)) * 64 + n * 16 + c) * DM + q * 8;
    bf16x8 fa[20], fb[20];
#pragma unroll
    for (int ks = 0; ks < 20; ++ks) { fa[ks] = *(const bf16x8*)(gw + ks * 32 + q * 8); fb[ks] = *(const bf16x8*)(xr + ks * 32); }
    SBAR();
    f32x4 z = f32x4{};
#pragma unroll
    for (int ks = 0; ks < 20; ++ks) z = MFMA16(fa[ks], fb[ks], z);
    if (lane < 16) *(LAS float*)(uintptr_t)(lds0 + LDS_G + (n * 16 + lane) * 4) = 1.0f / (1.0f + __expf(-z[0]));
}
DI void attn_produce(const u16* __restrict__ x, const u16* __restrict__ gw, const char* __restrict__ ws, unsigned lds0, int pw) {
    const int lane = threadIdx.x & 63;
    const int b = blockIdx.x, head = (b & 7) * 4 + (b >> 6);
    unsigned so[6], sd[6];
#pragma unroll
    for (int j = 0; j < 6; ++j) {
        const int cc = min(j * 64 + lane, 359), tl = pw * 2 + (cc >= 180), c = cc >= 180 ? cc - 180 : cc;
        const int v = c - 96, g = v / 21, d = v - g * 21;
        so[j] = c < 64 ? WS_KA + (unsigned)(head * 64 + tl) * 1024 + c * 16
              : c < 96 ? WS_KB + (unsigned)(head * 64 + tl) * 512 + (c - 64) * 16
                       : WS_VV + (unsigned)((tl * 4 + g) * VP + (d < 20 ? head * 20 + d : 640)) * 16;
        sd[j] = c < 64 ? 4096u : c < 96 ? 2048u : 4 * VV_T;
    }
    auto issue = [&](unsigned g) {
        const unsigned dst = lds0 + kv_off(g) + pw * 2 * KVT_B;
#pragma unroll
        for (int j = 0; j < 5; ++j) { dma16c(ws, so[j], dst + j * 1024); so[j] += sd[j]; }
        dma16_lo<40>(ws, so[5], dst + 5 * 1024); so[5] += sd[5];
    };
#pragma unroll
    for (int j = 0; j < 6; ++j) { PIN(so[j]); PIN(sd[j]); }
    if (pw == 1) wait_count((gu32*)(ws + WS_FLAG + (264 + (b & 7)) * 128), 0u, 48u);
    bar_raw();
    issue(0); issue(1); issue(2);
    if (pw == 0 && lane < 4) lds_put(lds0 + KV_FREE + lane * 4, 0u);
    lds_put(lds0 + KV_FULL + pw * 4, 1u);
    wait_vm<12>(); bar();
#pragma unroll 1
    for (unsigned g = 3; g < 13; ++g) { issue(g); wait_vm<12>(); lds_put(lds0 + KV_FULL + pw * 4, g - 1); }
    wait_vm<0>(); lds_put(lds0 + KV_FULL + pw * 4, 13u);
#pragma unroll 1
    for (int n = 0; n < 2; ++n) gate16(x, gw, lds0, 2 * n + pw);
#pragma unroll 1
    for (unsigned g = 13; g < 16; ++g) {
        wait_vm<0>(); lds_put(lds0 + KV_FULL + pw * 4, g);
        kv_wait_free(lds0, g - 3);
        issue(g);
    }
    wait_vm<0>(); lds_put(lds0 + KV_FULL + pw * 4, 17u);
}
DI void attn_helper(const u16* __restrict__ cs, const u16* __restrict__ ss, const u16* __restrict__ wo,
                    const char* __restrict__ ws, unsigned lds0, int hw) {
    const int lane = threadIdx.x & 63;
    const int b = blockIdx.x, xcd = b & 7, i = b >> 3;
    YG_MAP;
    const int gl = i >> 3, nt = gl == 3 ? 7 : 1, dt0 = gl == 3 ? 7 * (xcd >> 1) : 28 + (xcd >> 1) * 3 + gl;
    u32x4 abv[7][4];
#pragma unroll
    for (int t = 0; t < 7; ++t)
#pragma unroll
        for (int n = 0; n < 4; ++n) abv[t][n] = *(const LAS u32x4*)(uintptr_t)(lds0 + t * 8192 + hw * 4096 + n * 1024 + lane * 16);
    bar(); bar_raw();
    __builtin_amdgcn_s_setprio(3);
    kv_wait_free(lds0, 1);
#pragma unroll
    for (int t = 0; t < 7; ++t) {
        if (t < nt) {
            const int dt = dt0 + t, et = dt >= 20 ? dt - 20 : dt, ks = (dt >= 20 ? 64 : 0) + ((xcd & 1) * 8 + (i & 7)) * 4 + hw * 2;
            const unsigned dst = WS_AB + (unsigned)((et * 2 + ((lane >> 4) & 1)) * 128 + ks) * 1024u + (unsigned)((lane >> 5) * 256 + (lane & 15) * 16);
#pragma unroll
            for (int n = 0; n < 4; ++n) st128(ws_rsrc(ws), dst, n * 512, abv[t][n]);
            __builtin_amdgcn_s_sleep(4);
        }
    }
    wait_vm<0>();
    if (lane == 0) __hip_atomic_fetch_add((gu32*)(ws + WS_FLAG + (272 + (xcd & 1)) * 128), 1u, RLX_AGENT);

    const char* pfp = (const char*)(sp >= 2 ? ss : cs) + (size_t)(tb * 128 + eg * 32 + hw * 16) * 4096 + (sp & 1) * 2048;
#pragma unroll 1
    for (int n = 0; n < 16; ++n) { touch(pfp + n * 128, (lane >> 2) * 4096, lds0 + LDS_SCR); __builtin_amdgcn_s_sleep(8); }
    touch((const char*)wo + ((tb & 7) * 80 + hw * 40) * 1280 + (eg * 2 + (sp >> 1)) * 160, min(lane >> 1, 39) * 1280 + (lane & 1) * 156, lds0 + LDS_SCR);
    touch((const char*)wo + ((tb & 7) * 80 + hw * 40 + 32) * 1280 + (eg * 2 + (sp >> 1)) * 160, ((lane >> 1) & 7) * 1280 + (lane & 1) * 156, lds0 + LDS_SCR);
    wait_count((gu32*)(ws + WS_FLAG + (272 + (sp & 1)) * 128), 0u, 256u);
    kv_wait_free(lds0, 9);
#pragma unroll 1
    for (int n = 0; n < 6; ++n) { yg_issue(cs, ss, ws, lds0, hw * 2 + (n & 1), n >> 1); __builtin_amdgcn_s_sleep(4); }
}

DI void phase_tail(const u16* __restrict__ cs, const u16* __restrict__ ss, const u16* __restrict__ wo, char* __restrict__ ws, u16* __restrict__ out, unsigned lds0, int wid) {
    const int lane = threadIdx.x & 63, c = lane & 15, q = lane >> 4;
    const int b = blockIdx.x, xcd = b & 7, i = b >> 3;
    YG_MAP;
    const int sb = tb * 2 + (sp & 1), ng = eg * 2 + (sp >> 1);
    __builtin_amdgcn_s_setprio(0);
    if (wid >= 4) {
        const int lw = wid - 4;
        unsigned wv[5];
#pragma unroll
        for (int j = 0; j < 5; ++j) { const unsigned ci = (unsigned)(lw + 4 * j) * 64u + lane, row = (ci * 6554u) >> 16; wv[j] = row * 1280u + (ci - row * 10u) * 16u; PIN(wv[j]); }
        wait_vm<0>(); bar();
#pragma unroll 1
        for (int st = 3; st < 16; ++st) {
            yg_issue(cs, ss, ws, lds0, lw, st);
            wait_vm<18>(); bar_raw();
            if (st == 6 && threadIdx.x == 256) __hip_atomic_fetch_add((gu32*)(ws + WS_FLAG + (256 + (i & 7)) * 128), 1u, RLX_AGENT);
        }
#pragma unroll
        for (int j = 0; j < 25; ++j) {
            dma16((const char*)wo + ng * 160 + (j / 5) * 128 * 1280, wv[j % 5], lds0 + lw * 1024 + j * 4096);
            if (j == 8 || j == 17) { wait_vm<18>(); bar_raw(); }
        }
        wait_vm<0>(); bar_raw();
        wait_count((gu32*)(ws + WS_FLAG + ((tb * 4 + eg) * 4 + sp) * 128), 0u, 3u);
        const int t0 = (lw >> 1) * 3;
        const unsigned yb = WS_YP + (unsigned)(((tb * 4 + eg) * 4 + sp) * 4) * 10240u + (unsigned)((lw & 1) * 5 + t0) * 1024u + lane * 16;
        u32x4 yv[3][3];
#pragma unroll
        for (int t = 0; t < 3; ++t)
#pragma unroll
            for (int v = 0; v < 3; ++v) yv[v][t] = ld128(ws_rsrc(ws), yb + ((sp + 1 + v) & 3) * 10240 + (t0 + t < 5 ? t : 0) * 1024);
        SBAR();
        bar_raw();
        LAS f32x4* yw = (LAS f32x4*)(uintptr_t)(lds0 + lw * 6144 + lane * 16);
#pragma unroll
        for (int t = 0; t < 3; ++t)
#pragma unroll
            for (int u = 0; u < 2; ++u)
                yw[(u * 3 + t) * 64] = f32x4{(bflo(yv[0][t][2 * u]) + bflo(yv[1][t][2 * u])) + bflo(yv[2][t][2 * u]),
                                             (bfhi(yv[0][t][2 * u]) + bfhi(yv[1][t][2 * u])) + bfhi(yv[2][t][2 * u]),
                                             (bflo(yv[0][t][2 * u + 1]) + bflo(yv[1][t][2 * u + 1])) + bflo(yv[2][t][2 * u + 1]),
                                             (bfhi(yv[0][t][2 * u + 1]) + bfhi(yv[1][t][2 * u + 1])) + bfhi(yv[2][t][2 * u + 1])};
        bar();
        return;
    }
    const rsrc_t wr = ws_rsrc(ws);
    const int rh = wid & 1, kk = wid >> 1;
    bf16x8 fo[5][2][2];
    {
        const int ch = wid >> 1;
        f32x4 acc[4][5];
#pragma unroll
        for (int u = 0; u < 4; ++u)
#pragma unroll
            for (int t = 0; t < 5; ++t) acc[u][t] = f32x4{};
        bf16x8 frE[2][4], fnE[2][5], frO[2][4], fnO[2][5];
        const unsigned af = lds0 + 16384 + ch * 10240 + lane * 16;
        const unsigned a0 = lds0 + (unsigned)(rh * 64 + c) * 128u + (unsigned)((q ^ (c >> 1)) * 16);
        auto step = [&](bf16x8 (&fr)[2][4], bf16x8 (&fn)[2][5], const bf16x8 (&pr)[2][4], const bf16x8 (&pn)[2][5], int st, bool ld, bool mm) {
            lptr p, r0, r1;
            if (ld) {
                bar();
                const unsigned so = (st & 3) * F_STG;
                p = lbase(af + so); r0 = lbase(a0 + so); r1 = lbase((a0 ^ 64u) + so);
            }
#pragma unroll
            for (int k = 0; k < 2; ++k)
#pragma unroll
                for (int t = 0; t < 5; ++t) {
                    if (ld) {
                        fn[k][t] = lds128(p + t * 2048 + k * 1024);
                        if (t < 4) fr[k][t] = lds128((k ? r1 : r0) + t * 2048);
                    }
                    SBAR();
                    if (mm) {
#pragma unroll
                        for (int u = 0; u < 4; ++u) acc[u][t] = MFMA16(pn[k][t], pr[k][u], acc[u][t]);
                    }
                    SBAR();
                }
        };
        gu32* ocnt = (gu32*)(ws + WS_FLAG + (256 + (sb >> 2)) * 128);
        unsigned oseen = 0;
        const int poll_at = wid == 0 ? 8 : 99;
        step(frE, fnE, frO, fnO, 0, true, false);
#pragma unroll 1
        for (int st = 0; st < 14; st += 2) {
            step(frO, fnO, frE, fnE, st + 1, true, true);
            if (st == 2) wait_vm<0>();
            if (st == poll_at) poll1(oseen, ocnt);
            step(frE, fnE, frO, fnO, st + 2, true, true);
        }
        if (wid == 0) {
            asm volatile("s_waitcnt vmcnt(0)" : "+v"(oseen)::"memory");
            wait_count(ocnt, oseen, 32u);
        }
        step(frO, fnO, frE, fnE, 15, true, true);
        step(frE, fnE, frO, fnO, 16, false, true);
        const unsigned yp = WS_YP + (unsigned)(((tb * 4 + eg) * 4 + wid) * 4 + sp) * 10240u + lane * 16;
        u32x4 pk[2][5];
#pragma unroll
        for (int u = 0; u < 2; ++u)
#pragma unroll
            for (int t = 0; t < 5; ++t) {
                const f32x4 v0 = acc[2 * u][t], v1 = acc[2 * u + 1][t];
                pk[u][t] = u32x4{pk2(v0[0], v0[1]), pk2(v0[2], v0[3]), pk2(v1[0], v1[1]), pk2(v1[2], v1[3])};
            }
        if (wid != sp) {
#pragma unroll
            for (int u = 0; u < 2; ++u)
#pragma unroll
                for (int t = 0; t < 5; ++t) st128(wr, yp, (u * 5 + t) * 1024, pk[u][t]);
        } else {
#pragma unroll
            for (int u = 0; u < 2; ++u)
#pragma unroll
                for (int t = 0; t < 5; ++t) *(LAS u32x4*)(uintptr_t)(lds0 + (u ? F_RING : 102400) + t * 1024 + lane * 16) = pk[u][t];
        }
        SBAR();
        bar();
        wait_vm<0>();
        if (lane == 0 && wid != sp) __hip_atomic_fetch_add((gu32*)(ws + WS_FLAG + ((tb * 4 + eg) * 4 + wid) * 128), 1u, RLX_AGENT);
        const unsigned oa = WS_OA + (unsigned)((sb * 2 + rh) * NH + kk * 4) * 1024u + q * 512 + c * 16;
        const unsigned ob = WS_OB + (unsigned)((sb * 2 + rh) * NH + kk * 16) * 256u + q * 512 + c * 8;
#pragma unroll
        for (int j = 0; j < 4; ++j)
#pragma unroll
            for (int k = 0; k < 2; ++k)
#pragma unroll
                for (int u = 0; u < 2; ++u) fo[j][k][u] = as_frag(ld128(wr, oa + j * 8192 + k * 2048 + u * 256));
#pragma unroll
        for (int k = 0; k < 2; ++k)
#pragma unroll
            for (int u = 0; u < 2; ++u) {
                const u32x2 a = ld64(wr, ob + k * 2048 + u * 128), bq = ld64(wr, ob + k * 2048 + u * 128 + 256);
                fo[4][k][u] = as_frag(u32x4{a[0], a[1], bq[0], bq[1]});
            }
        SBAR();
    }
    const int s0 = sb * 64 + rh * 32 + c;
    f32x4 acc[2][5];
#pragma unroll
    for (int t = 0; t < 5; ++t) { acc[0][t] = f32x4{}; acc[1][t] = f32x4{}; }
    {
        const unsigned wl = lds0 + (unsigned)(c >> 2) * 160u + (c & 3) * 8;
        const lptr p0 = lbase(wl + (unsigned)(kk * 80 + (q >> 1) * 20 + (q & 1) * 4) * 160u), p1 = lbase(wl + (unsigned)(kk * 80 + (q >> 1) * 20 + (q & 1) * 4) * 160u + 51200);
        const lptr p2 = lbase(wl + (unsigned)(kk * 320 + q * 40 + 16) * 160u);
        bf16x8 fn[2][5];
        auto wof = [&](int n, int t) {
            const lptr pp = n < 4 ? p0 + (n >> 1) * 25600 + (n & 1) * 6400 : n < 8 ? p1 + ((n - 4) >> 1) * 25600 + (n & 1) * 6400 : p2 + (n & 1) * 25600;
            const s16x4 lo = ldstr(pp + t * 32), hi = ldstr(pp + t * 32 + (n < 8 ? 8 : 20) * 160);
            return __builtin_bit_cast(bf16x8, __builtin_shufflevector(lo, hi, 0, 1, 2, 3, 4, 5, 6, 7));
        };
#define WOF(n, t) wof(n, t)
#pragma unroll
        for (int t = 0; t < 5; ++t) fn[0][t] = WOF(0, t);
#pragma unroll
        for (int n = 0; n < 10; ++n)
#pragma unroll
            for (int t = 0; t < 5; ++t) {
                if (n < 9) fn[(n + 1) & 1][t] = WOF(n + 1, t);
                SBAR();
                acc[0][t] = MFMA16(fn[n & 1][t], fo[n >> 1][n & 1][0], acc[0][t]);
                acc[1][t] = MFMA16(fn[n & 1][t], fo[n >> 1][n & 1][1], acc[1][t]);
                SBAR();
            }
    }
    const float g0 = *(const LAS float*)(uintptr_t)(lds0 + LDS_G + (rh * 32 + c) * 4), g1 = *(const LAS float*)(uintptr_t)(lds0 + LDS_G + (rh * 32 + 16 + c) * 4);
    const int t0 = kk * 3;
    f32x4 fin[2][3];
    {
        LAS f32x4* xw = (LAS f32x4*)(uintptr_t)(lds0 + 3 * F_STG + wid * 6144 + lane * 16);
        const LAS f32x4* xr = (const LAS f32x4*)(uintptr_t)(lds0 + 3 * F_STG + (wid ^ 2) * 6144 + lane * 16);
        unsigned mk = 0u - (unsigned)kk;
        PIN(mk);
#pragma unroll
        for (int u = 0; u < 2; ++u)
#pragma unroll
            for (int t = 0; t < 3; ++t) {
                const f32x4 lo = acc[u][t], hi = acc[u][t < 2 ? 3 + t : 4];
                f32x4 snd;
#pragma unroll
                for (int e = 0; e < 4; ++e) { fin[u][t][e] = bsel(mk, hi[e], lo[e]); snd[e] = bsel(mk, lo[e], hi[e]); }
                xw[(u * 3 + t) * 64] = snd;
            }
        bar();
#pragma unroll
        for (int u = 0; u < 2; ++u)
#pragma unroll
            for (int t = 0; t < 3; ++t) fin[u][t] = (fin[u][t] + xr[(u * 3 + t) * 64]) * (u ? g1 : g0);
    }
#pragma unroll
    for (int t = 0; t < 3; ++t) {
        const u32x4 y = *(const LAS u32x4*)(uintptr_t)(lds0 + (rh ? F_RING : 102400) + (t0 + t < 5 ? t0 + t : 0) * 1024 + lane * 16);
#pragma unroll
        for (int u = 0; u < 2; ++u) fin[u][t] += f32x4{bflo(y[2 * u]), bfhi(y[2 * u]), bflo(y[2 * u + 1]), bfhi(y[2 * u + 1])} * (1.0f - (u ? g1 : g0));
    }
    bar_raw();
    const LAS f32x4* yr = (const LAS f32x4*)(uintptr_t)(lds0 + wid * 6144 + lane * 16);
    u16* op = out + (size_t)s0 * DM + ng * 80 + t0 * 16 + q * 4;
#pragma unroll
    for (int t = 0; t < 3; ++t) {
        if (t0 + t < 5) {
#pragma unroll
            for (int u = 0; u < 2; ++u) {
                const f32x4 v = fin[u][t] + yr[(u * 3 + t) * 64] * (1.0f - (u ? g1 : g0));
                *(u32x2*)(op + (size_t)u * 16 * DM + t * 16) = u32x2{pk2(v[0], v[1]), pk2(v[2], v[3])};
            }
        }
    }
}

constexpr int LDS_B = LDS_SCR + 256;

#define CODE_LINES 220
__global__ __launch_bounds__(512, 2) void k_all(const u16* __restrict__ x, const u16* __restrict__ wqkv, const u16* __restrict__ cd,
                                             const u16* __restrict__ sd, char* __restrict__ ws, const u16* __restrict__ wo, const u16* __restrict__ gw,
                                             const u16* __restrict__ cs, const u16* __restrict__ ss, u16* __restrict__ out) {
    extern __shared__ __attribute__((aligned(16))) char smem[];
    const unsigned lds0 = (unsigned)(uintptr_t)smem;
    const int wid = __builtin_amdgcn_readfirstlane(threadIdx.x >> 6);
    const int b = blockIdx.x, xcd = b & 7, i = b >> 3;
    const int rb = (xcd & 1) * 8 + (i & 7), cq = xcd >> 1, gl = i >> 3;
    asm volatile("" ::"s"(x), "s"(wqkv), "s"(cd), "s"(sd));
    if (wid < 4) {
        if (i < 2) {
            const char* pc;
            asm volatile("s_getpc_b64 %0" : "=s"(pc));
            touch((const char*)((uintptr_t)pc & ~(uintptr_t)127), min(threadIdx.x, (unsigned)CODE_LINES) * 128, lds0 + P_RING + 64);
        }
        if (threadIdx.x == 64) {
            const unsigned n = __hip_atomic_load((gu32*)(ws + WS_RSP + b * 4), RLX_AGENT) + 1u;
            __hip_atomic_store((gu32*)(ws + WS_REQ + b * 4), n, RLX_AGENT);
            *(volatile LAS unsigned*)(uintptr_t)(lds0 + P_RING) = n;
        }
        if (b == LEADER) {
            const unsigned tl = threadIdx.x;
            st128(ws_rsrc(ws), (tl * VP + 640) * 16, WS_VV, u32x4{ONES, ONES, ONES, ONES});
            st32(ws_rsrc(ws), tl * 128, WS_FLAG, 0u);
            if (tl < 18) st32(ws_rsrc(ws), (256 + tl) * 128, WS_FLAG, 0u);
            st32(ws_rsrc(ws), tl * 4, WS_ACK, 0u);
            wait_vm<0>();
        }
        asm volatile("s_waitcnt lgkmcnt(0)" ::: "memory");
        if (gl == 3) proj_mma<2>(ws, rb, cq, lds0, wid);
        else if (cq * 3 + gl < 8) proj_mma<0>(ws, rb, cq * 3 + gl, lds0, wid);
        else proj_mma<1>(ws, rb, cq * 3 + gl, lds0, wid);
        wait_vm<0>();
    } else proj_load(x, wqkv, cd, sd, ws, rb, cq, gl, lds0, wid - 4);
    bar();
    if (wid == 5) {
        const int lane = threadIdx.x & 63;
        if (gl != 3 && lane < 2) __hip_atomic_fetch_add((gu32*)(ws + WS_FLAG + (264 + ((cq * 3 + gl) & 3) * 2 + lane) * 128), 1u, RLX_AGENT);
    }
    if (wid < 4) phase_attn(ws, lds0, wid);
    else if (wid < 6) attn_produce(x, gw, ws, lds0, wid - 4);
    else attn_helper(cs, ss, wo, ws, lds0, wid - 6);
    phase_tail(cs, ss, wo, ws, out, lds0, wid);
}

extern "C" void kernel_launch(void* const* d_in, const int* in_sizes, int n_in, void* d_out, int out_size, void* d_ws, size_t ws_size,
                              hipStream_t stream) {
    const u16* x = (const u16*)d_in[0];
    const u16* wqkv = (const u16*)d_in[1];
    const u16* wo = (const u16*)d_in[2];
    const u16* cs = (const u16*)d_in[4];
    const u16* ss = (const u16*)d_in[5];
    const u16* cd = (const u16*)d_in[6];
    const u16* sd = (const u16*)d_in[7];
    const u16* gw = (const u16*)d_in[8];
    char* ws = (char*)d_ws;
    (void)hipFuncSetAttribute((const void*)k_all, hipFuncAttributeMaxDynamicSharedMemorySize, LDS_B);
    k_all<<<256, 512, LDS_B, stream>>>(x, wqkv, cd, sd, ws, wo, gw, cs, ss, (u16*)d_out);
}
```

```cpp
#include <hip/hip_runtime.h>
#include <hip/hip_bf16.h>
#include <stdint.h>

typedef unsigned short u16;
typedef __attribute__((ext_vector_type(8))) __bf16 bf16x8;
typedef __attribute__((ext_vector_type(4))) short s16x4;
typedef __attribute__((ext_vector_type(16))) float f32x16;
typedef __attribute__((ext_vector_type(4))) float f32x4;
typedef __attribute__((ext_vector_type(4))) unsigned u32x4;
typedef __attribute__((ext_vector_type(2))) unsigned u32x2;
#define LAS __attribute__((address_space(3)))
#define DI __device__ __forceinline__
typedef const LAS char* lptr;

constexpr int SEQ = 2048, DM = 640, NH = 32;
constexpr int VP = 648;
constexpr unsigned WS_QA = 0;
constexpr unsigned WS_KA = WS_QA + NH * 64 * 1024;
constexpr unsigned WS_QB = WS_KA + NH * 64 * 1024;
constexpr unsigned WS_KB = WS_QB + NH * 64 * 512;
constexpr unsigned WS_VV = WS_KB + NH * 64 * 512;
constexpr unsigned WS_AB = WS_VV + 64 * 4 * VP * 16;
constexpr unsigned WS_YP = WS_AB + 40 * 128 * 1024;
constexpr unsigned WS_OA = WS_YP + 4 * 64 * 40 * 1024;
constexpr unsigned WS_OB = WS_OA + 64 * NH * 1024;
constexpr unsigned WS_G = WS_OB + 64 * NH * 256;
constexpr unsigned WS_FLAG = WS_G + SEQ * 4;
constexpr unsigned WS_REQ = WS_FLAG + 274 * 128;
constexpr unsigned WS_RSP = WS_REQ + 1024;
constexpr unsigned WS_ACK = WS_RSP + 1024;
constexpr unsigned WS_END = WS_ACK + 1024;
constexpr float QSCALE = 0.22360679774997896f * 1.4426950408889634f;
constexpr int SC1 = 16;

#define MFMA32(a, b, c) __builtin_amdgcn_mfma_f32_32x32x16_bf16((a), (b), (c), 0, 0, 0)
#define MFMA8(a, b, c) __builtin_amdgcn_mfma_f32_32x32x8bf16_1k(__builtin_bit_cast(s16x4, a), __builtin_bit_cast(s16x4, b), (c), 0, 0, 0)
#define MFMA16(a, b, c) __builtin_amdgcn_mfma_f32_16x16x32_bf16((a), (b), (c), 0, 0, 0)
#define SBAR() __builtin_amdgcn_sched_barrier(0)
#define PIN(x) asm volatile("" : "+v"(x))

typedef __attribute__((ext_vector_type(2))) __bf16 bf16x2;
typedef __attribute__((ext_vector_type(2))) float f32x2;
DI unsigned pk2(float lo, float hi) {
    f32x2 v = {lo, hi};
    return __builtin_bit_cast(unsigned, __builtin_convertvector(v, bf16x2));
}
DI float bsel(unsigned m, float a, float b) { return __uint_as_float((__float_as_uint(a) & m) | (__float_as_uint(b) & ~m)); }
DI float bflo(unsigned v) { return __uint_as_float(v << 16); }
DI float bfhi(unsigned v) { return __uint_as_float(v & 0xFFFF0000u); }
DI void dma16(const void* sbase, unsigned voff, unsigned lds) {
    asm volatile("s_mov_b32 m0, %2\n\ts_nop 0\n\tglobal_load_lds_dwordx4 %0, %1" ::"v"(voff), "s"(sbase), "s"(lds) : "memory");
}
DI void dma16c(const void* sbase, unsigned voff, unsigned lds) {
    asm volatile("s_mov_b32 m0, %2\n\ts_nop 0\n\tglobal_load_lds_dwordx4 %0, %1 sc1" ::"v"(voff), "s"(sbase), "s"(lds) : "memory");
}
template <int N> DI void dma16_lo(const void* sbase, unsigned voff, unsigned lds) {
    unsigned long long sv;
    asm volatile("s_mov_b32 m0, %3\n\ts_mov_b64 %0, exec\n\ts_mov_b64 exec, %4\n\tglobal_load_lds_dwordx4 %1, %2 sc1\n\ts_mov_b64 exec, %0"
                 : "=&s"(sv) : "v"(voff), "s"(sbase), "s"(lds), "s"((1ull << N) - 1) : "memory");
}
DI void touch(const void* sbase, unsigned voff, unsigned lds) {
    asm volatile("s_mov_b32 m0, %2\n\ts_nop 0\n\tglobal_load_lds_dword %0, %1" ::"v"(voff), "s"(sbase), "s"(lds) : "memory");
}
template <int N> DI void wait_vm() { asm volatile("s_waitcnt vmcnt(%0)" ::"i"(N) : "memory"); }
DI void bar() { asm volatile("s_waitcnt lgkmcnt(0)\n\ts_barrier" ::: "memory"); }
DI void bar_raw() { asm volatile("s_barrier" ::: "memory"); }
DI lptr lbase(unsigned a) { asm volatile("" : "+v"(a)); return (lptr)(uintptr_t)a; }
DI bf16x8 lds128(lptr p) { return *(const LAS bf16x8*)p; }
DI u32x2 lds64(lptr p) { return *(const LAS u32x2*)p; }
DI s16x4 ldstr(lptr p) {
    typedef short v4i16_t __attribute__((ext_vector_type(4)));
    return __builtin_bit_cast(s16x4, __builtin_amdgcn_ds_read_tr16_b64_v4i16((LAS v4i16_t*)p));
}
DI u32x4 pack8(const f32x16& x, int s, float sc) {
    u32x4 p;
    p[0] = pk2(x[8 * s + 0] * sc, x[8 * s + 1] * sc);
    p[1] = pk2(x[8 * s + 2] * sc, x[8 * s + 3] * sc);
    p[2] = pk2(x[8 * s + 4] * sc, x[8 * s + 5] * sc);
    p[3] = pk2(x[8 * s + 6] * sc, x[8 * s + 7] * sc);
    return p;
}
typedef __amdgpu_buffer_rsrc_t rsrc_t;
DI rsrc_t ws_rsrc(const void* ws) { return __builtin_amdgcn_make_buffer_rsrc((void*)ws, (short)0, (int)WS_END, 0x00020000); }
DI void st128(rsrc_t r, unsigned voff, unsigned soff, u32x4 v) { __builtin_amdgcn_raw_buffer_store_b128(v, r, voff + soff, 0, SC1); }
DI void st64(rsrc_t r, unsigned voff, unsigned soff, u32x2 v) { __builtin_amdgcn_raw_buffer_store_b64(v, r, voff, soff, SC1); }
DI void st32(rsrc_t r, unsigned voff, unsigned soff, unsigned v) { __builtin_amdgcn_raw_buffer_store_b32(v, r, voff, soff, SC1); }
DI u32x4 ld128(rsrc_t r, unsigned voff) { return __builtin_amdgcn_raw_buffer_load_b128(r, voff, 0, SC1); }
DI u32x2 ld64(rsrc_t r, unsigned voff) { return __builtin_amdgcn_raw_buffer_load_b64(r, voff, 0, SC1); }
DI bf16x8 as_frag(u32x4 v) { return __builtin_bit_cast(bf16x8, v); }
typedef __attribute__((ext_vector_type(16))) __bf16 bf16x16;
template <int HI> DI f32x16 pv_mma(bf16x8 v, const f32x16& p, const f32x16& o, int idx) {
    const auto ph = __builtin_shufflevector(p, p, 8 * HI, 8 * HI + 1, 8 * HI + 2, 8 * HI + 3, 8 * HI + 4, 8 * HI + 5, 8 * HI + 6, 8 * HI + 7);
    return __builtin_amdgcn_smfmac_f32_32x32x32_bf16(v, __builtin_bit_cast(bf16x16, ph), o, idx, 0, 0);
}
DI u32x4 pack8n(const f32x16& x, int s, float sc) {
    const u32x4 p = pack8(x, s, sc);
    const auto r0 = __builtin_amdgcn_permlane32_swap(p[0], p[2], false, false);
    const auto r1 = __builtin_amdgcn_permlane32_swap(p[1], p[3], false, false);
    return u32x4{r0[0], r1[0], r0[1], r1[1]};
}

typedef __attribute__((address_space(1))) unsigned gu32;
#define RLX_AGENT __ATOMIC_RELAXED, __HIP_MEMORY_SCOPE_AGENT
DI void poll1(unsigned& v, gu32* c) {
    unsigned long long sv;
    asm volatile("s_mov_b64 %1, exec\n\ts_mov_b64 exec, 1\n\tglobal_load_dword %0, %2, %3 sc1\n\ts_mov_b64 exec, %1" : "+v"(v), "=&s"(sv) : "v"(0u), "s"(c) : "memory");
}
DI void wait_count(gu32* c, unsigned seen, unsigned want) {
#pragma unroll 1
    for (unsigned spins = 0; (unsigned)__builtin_amdgcn_readfirstlane(seen) < want && spins < (1u << 17); ++spins) {
        __builtin_amdgcn_s_sleep(4);
        poll1(seen, c);
        asm volatile("s_waitcnt vmcnt(0)" : "+v"(seen)::"memory");
    }
    asm volatile("" ::: "memory");
}

constexpr int LEADER = 255;
DI void echo_round(u32x4& v, const char* ws, bool put) {
    const unsigned o = (threadIdx.x & 63) * 16;
    if (put) asm volatile("global_store_dwordx4 %0, %1, %2 sc1\n\ts_nop 1" ::"v"(o), "v"(v), "s"(ws + WS_RSP) : "memory");
    asm volatile("global_load_dwordx4 %0, %1, %2 sc1" : "+v"(v) : "v"(o), "s"(ws + WS_REQ) : "memory");
}

constexpr int P_STG = 45056;
constexpr int P_RING = 3 * P_STG;
constexpr int P_GW = P_RING + 512;

DI void proj_load(const u16* __restrict__ x, const u16* __restrict__ wqkv, const u16* __restrict__ cd, const u16* __restrict__ sd,
                  const char* __restrict__ ws, int rb, int cq, int gl, unsigned lds0, int wid) {
    const int lane = threadIdx.x & 63;
    constexpr int NDMA = 10;
    const bool pa = gl != 3;
    const unsigned vrow = (unsigned)(lane >> 3) * 1280u + (unsigned)(((lane & 7) ^ ((4 * (wid & 1) + (lane >> 4)) & 7)) * 16);
    const char* xb = (const char*)x + ((size_t)rb * 128 + wid * 8) * 1280;
    int st = 3 * ((rb + gl) % 3);
    unsigned slot = lds0 + wid * 1024;
#pragma unroll
    for (int j = 0; j < 4; ++j) dma16(xb + j * 40960 + st * 128, vrow, slot + j * 4096);
    const char* wb[7];
    unsigned vo[5];
    const int dt0 = pa ? 28 + cq * 3 + gl : 7 * cq;
#pragma unroll
    for (int j = 0; j < 7; ++j) {
        const int dt = dt0 + j;
        const char* d = (const char*)(dt >= 20 ? sd : cd) + ((dt >= 20 ? dt - 20 : dt) * 32 + wid * 8) * 1280;
        wb[j] = pa && j ? (const char*)wqkv + (cq * 3 + gl) * 320 : d;
    }
#pragma unroll
    for (int j = 0; j < 5; ++j) { const unsigned i = (wid + 4 * j) * 64 + lane, q = (i * 3277u) >> 16; vo[j] = pa ? q * 3840u + (i - q * 20u) * 16u : vrow; }
    const int wstep = pa ? 64 * 3840 : 128;
    auto issue = [&](bool with_x) {
        if (with_x) {
#pragma unroll
            for (int j = 0; j < 4; ++j) dma16(xb + j * 40960 + st * 128, vrow, slot + j * 4096);
        }
        if (!pa) dma16(wb[6] + st * 128, vrow, slot + 40960);
        dma16(wb[0] + st * 128, vrow, slot + 16384);
#pragma unroll
        for (int j = 0; j < 5; ++j) dma16(wb[1 + j] + st * wstep, vo[j], slot + 20480 + j * 4096);
        slot = slot + P_STG == lds0 + wid * 1024 + P_RING ? lds0 + wid * 1024 : slot + P_STG;
        st = st == 9 ? 0 : st + 1;
    };
    const bool leader = blockIdx.x == LEADER && wid == 3;
    u32x4 rq = {0u, 0u, 0u, 0u};
    unsigned back = 0;
#pragma unroll 1
    for (int n = 0; n < 3; ++n) {
        issue(n > 0);
        if (n == 1) { wait_vm<NDMA>(); bar_raw(); }
    }
    wait_vm<NDMA>(); bar_raw(); bar_raw();
#pragma unroll 1
    for (int n = 3; n < 10; ++n) {
        if (leader) echo_round(rq, ws, n > 3);
        if (wid == 0 && n == 9) poll1(back, (gu32*)(ws + WS_RSP + blockIdx.x * 4));
        issue(true); wait_vm<NDMA>(); bar_raw(); bar_raw();
    }
    wait_vm<0>();
    bar_raw();
    bool acked = wid != 0;
    const unsigned want = __builtin_amdgcn_readfirstlane(*(const volatile LAS unsigned*)(uintptr_t)(lds0 + P_RING));
    PIN(back);
    if (!acked && (unsigned)__builtin_amdgcn_readfirstlane(back) == want) {
        if (lane == 0) __hip_atomic_store((gu32*)(ws + WS_ACK + blockIdx.x * 4), 1u, RLX_AGENT);
        acked = true;
    }
    bar_raw(); bar_raw(); bar_raw();
    if (!acked) {
#pragma unroll 1
        for (unsigned spins = 0; spins < (1u << 17) && (unsigned)__builtin_amdgcn_readfirstlane(back) != want; ++spins) {
            __builtin_amdgcn_s_sleep(4);
            poll1(back, (gu32*)(ws + WS_RSP + blockIdx.x * 4));
            asm volatile("s_waitcnt vmcnt(0)" : "+v"(back)::"memory");
        }
        if (lane == 0) __hip_atomic_store((gu32*)(ws + WS_ACK + blockIdx.x * 4), 1u, RLX_AGENT);
    }
    if (leader) {
#pragma unroll 1
        for (unsigned spins = 0; spins < (1u << 17); ++spins) {
            echo_round(rq, ws, true);
            u32x4 ak;
            asm volatile("global_load_dwordx4 %0, %2, %3 sc1\n\ts_waitcnt vmcnt(0)" : "=&v"(ak), "+v"(rq) : "v"(lane * 16), "s"(ws + WS_ACK) : "memory");
            if (__all((ak[0] & ak[1] & ak[2] & ak[3]) == 1u)) break;
            __builtin_amdgcn_s_sleep(4);
        }
    }
}

template <int PATH>
DI void proj_mma(char* __restrict__ ws, const u16* __restrict__ gw, int rb, int grp, unsigned lds0, int wid) {
    const int lane = threadIdx.x & 63, r = lane & 31, h = lane >> 5;
    constexpr bool PATH_A = PATH < 2;
    constexpr int NW = PATH_A ? 5 : 0, ND = PATH_A ? 1 : 7, NT = NW + ND;
    const int dt0 = PATH_A ? 28 + grp : 7 * grp;
    const unsigned sw = lds0 + (unsigned)r * 128u + (unsigned)((h ^ ((r >> 1) & 7)) * 16);
    const unsigned awo = lds0 + 20480u + (unsigned)(8 * h + ((lane & 15) >> 2)) * 320u + (unsigned)(((lane >> 4) & 1) * 32 + (lane & 3) * 8);
    f32x16 acc[NT];
#pragma unroll
    for (int t = 0; t < NT; ++t) acc[t] = f32x16{};
    bf16x8 fx[2], fw[2][NT];
    lptr pr, px, pw;
    auto setp = [&](unsigned so, int ks) {
        pr = lbase((sw ^ (unsigned)(ks * 32)) + so);
        px = lbase((sw ^ (unsigned)(ks * 32)) + so + wid * 4096);
        if (PATH_A && ks == 0) pw = lbase(awo + so);
    };
    auto rd = [&](int ks, int b, int t) {
        if (t == 0) fx[b] = lds128(px);
        if (t < ND) fw[b][t] = lds128(pr + 16384 + t * 4096);
        else {
            s16x4 lo = ldstr(pw + ks * 16 * 320 + (t - ND) * 64), hi = ldstr(pw + ks * 16 * 320 + 4 * 320 + (t - ND) * 64);
            fw[b][t] = __builtin_bit_cast(bf16x8, __builtin_shufflevector(lo, hi, 0, 1, 2, 3, 4, 5, 6, 7));
        }
    };
    auto mm = [&](int b, int t) {
        if (PATH == 0 && t >= ND) acc[t] = MFMA32(fw[b][t], fx[b], acc[t]);
        else acc[t] = MFMA32(fx[b], fw[b][t], acc[t]);
    };
    constexpr bool GATE = PATH == 3;
    f32x16 z = f32x16{};
    u32x4 fg[2];
    unsigned ag = lds0 + P_GW + (3 * (rb % 3)) * 128 + h * 16;
    if (GATE) {
        if (wid < 2) dma16((const char*)gw + wid * 256, lane * 16, lds0 + P_GW + wid * 256);
        wait_vm<0>();
    }
    unsigned so = 0;
    setp(0, 0);
#pragma unroll
    for (int t = 0; t < NT; ++t) PIN(acc[t]);
    bar_raw();
    if (GATE) fg[0] = *(const LAS u32x4*)(uintptr_t)ag;
#pragma unroll
    for (int t = 0; t < NT; ++t) rd(0, 0, t);
    SBAR();
#pragma unroll 1
    for (int st = 0; st < 10; ++st) {
#pragma unroll
        for (int ks = 0; ks < 4; ++ks) {
            if (ks == 3) {
                bar_raw();
                so = so == 2 * P_STG ? 0 : so + P_STG;
                ag = ag + 128 >= lds0 + P_GW + 1280 ? ag - 1152 : ag + 128;
            }
            setp(so, (ks + 1) & 3);
#pragma unroll
            for (int t = 0; t < NT; ++t) {
                rd((ks + 1) & 3, (ks + 1) & 1, t);
                if (GATE && t == 1) fg[(ks + 1) & 1] = *(const LAS u32x4*)(uintptr_t)(ag + ((ks + 1) & 3) * 32);
                SBAR(); mm(ks & 1, t);
                if (GATE && t == NT - 1) z = MFMA32(as_frag(fg[ks & 1]), fx[ks & 1], z);
                SBAR();
            }
        }
        bar_raw();
    }
    const rsrc_t wr = ws_rsrc(ws);
    const int stile = rb * 4 + wid;
    if (PATH == 0) {
        const float sc = grp < 4 ? QSCALE : 1.f;
        const unsigned t0 = (unsigned)(grp * 8 * 64 + stile);
        const unsigned va = WS_QA + t0 * 1024 + lane * 16, vb = WS_QB + (t0 + h * 64) * 512 + r * 16;
#pragma unroll
        for (int m = 0; m < 4; ++m) {
            u32x2 q[5];
#pragma unroll
            for (int i = 0; i < 5; ++i) {
                const int p = 5 * m + i, t = ND + (p >> 2), g = p & 3;
                q[i][0] = pk2(acc[t][4 * g] * sc, acc[t][4 * g + 1] * sc);
                q[i][1] = pk2(acc[t][4 * g + 2] * sc, acc[t][4 * g + 3] * sc);
            }
            st128(wr, va, (2 * m) * 64 * 1024, u32x4{q[0][0], q[0][1], q[1][0], q[1][1]});
            st128(wr, va, (2 * m + 1) * 64 * 1024, u32x4{q[3][0], q[3][1], q[4][0], q[4][1]});
            st128(wr, vb, (2 * m) * 64 * 512, u32x4{q[2][0], q[2][1], 0x00003F80u, 0u});
        }
    } else if (PATH == 1) {
        const unsigned vv = WS_VV + (unsigned)((stile * 4 + h) * VP + (grp - 8) * 160 + r) * 16;
#pragma unroll
        for (int t = 0; t < 5; ++t) {
            st128(wr, vv, t * 512, pack8n(acc[ND + t], 0, 1.f));
            st128(wr, vv, t * 512 + 2 * VP * 16, pack8n(acc[ND + t], 1, 1.f));
        }
    }
#pragma unroll
    for (int t = 0; t < ND; ++t) {
        const float sc = dt0 + t >= 20 ? -1.f : 1.f;
        LAS u32x4* d = (LAS u32x4*)(uintptr_t)(lds0 + t * 8192 + wid * 2048 + lane * 16);
        d[0] = pack8n(acc[t], 0, sc); d[64] = pack8n(acc[t], 1, sc);
    }
    if (GATE && h == 0) st32(wr, (stile * 32 + r) * 4, WS_G, __float_as_uint(1.0f / (1.0f + __expf(-z[0]))));
}
#define YG_MAP const int tb = (xcd >> 2) * 8 + (i & 7), eg = i >> 3, sp = xcd & 3
constexpr int F_STG = 36864;
constexpr int F_RING = 4 * F_STG;
DI void yg_issue(const u16* __restrict__ cs, const u16* __restrict__ ss, const char* __restrict__ ws, unsigned lds0, int lw, int st) {
    const int lane = threadIdx.x & 63;
    const int b = blockIdx.x, xcd = b & 7, i = b >> 3;
    YG_MAP;
    const unsigned vrow = (unsigned)(lane >> 3) * 4096u + (unsigned)(((lane & 7) ^ ((4 * (lw & 1) + (lane >> 4)) & 7)) * 16);
    const char* rp = (const char*)(sp >= 2 ? ss : cs) + (size_t)(tb * 128 + lw * 8) * 4096 + (sp & 1) * 2048;
    const char* ab = ws + WS_AB + (size_t)((eg * 10 + (lw >> 1)) * 128 + sp * 32 + (lw & 1)) * 1024;
    const unsigned d = lds0 + lw * 1024 + (st & 3) * F_STG;
#pragma unroll
    for (int j = 0; j < 4; ++j) dma16(rp + j * 32 * 4096 + st * 128, vrow, d + j * 4096);
#pragma unroll
    for (int j = 0; j < 5; ++j) dma16c(ab + (j * 256 + st * 2) * 1024, lane * 16, d + 16384 + j * 4096);
}

DI float xhalf_max(float v) {
    auto rr = __builtin_amdgcn_permlane32_swap(__float_as_uint(v), __float_as_uint(v), false, false);
    return fmaxf(__uint_as_float(rr[0]), __uint_as_float(rr[1]));
}
DI float max16(const f32x16& s) {
    float a = fmaxf(fmaxf(s[0], s[1]), s[2]), b = fmaxf(fmaxf(s[3], s[4]), s[5]);
    a = fmaxf(fmaxf(a, s[6]), s[7]); b = fmaxf(fmaxf(b, s[8]), s[9]);
    a = fmaxf(fmaxf(a, s[10]), s[11]); b = fmaxf(fmaxf(b, s[12]), s[13]);
    a = fmaxf(fmaxf(a, s[14]), s[15]);
    return fmaxf(a, b);
}
#define EX2(v) __builtin_amdgcn_exp2f(v)
constexpr unsigned VV_T = 4 * VP * 16, VV_S = 2 * VP * 16;
constexpr unsigned ONES = 0x3F803F80u;

DI u32x4 fix_v(u32x4 v, int r) {
#pragma unroll
    for (int i = 0; i < 4; ++i) v[i] = r < 20 ? v[i] : (r == 20 ? ONES : 0u);
    return v;
}
DI f32x16 attn_safe(rsrc_t wr, int head, u32x4 qa, u32x4 qb, int lane) {
    const int r = lane & 31, h = lane >> 5;
    qb[2] = 0u;
    f32x16 o = f32x16{};
    float m = -INFINITY;
#pragma unroll 1
    for (int t = 0; t < 64; ++t) {
        const u32x4 ka = ld128(wr, WS_KA + (unsigned)(head * 64 + t) * 1024 + lane * 16);
        const u32x4 kb = ld128(wr, WS_KB + (unsigned)(head * 64 + t) * 512 + r * 16);
        const unsigned vp = WS_VV + (unsigned)((t * 4 + h) * VP + head * 20 + (r < 20 ? r : 0)) * 16;
        const u32x4 v0 = fix_v(ld128(wr, vp), r), v1 = fix_v(ld128(wr, vp + VV_S), r);
        f32x16 s = MFMA32(as_frag(ka), as_frag(qa), f32x16{});
        s = MFMA32(as_frag(kb), as_frag(qb), s);
        const float mn = fmaxf(m, xhalf_max(max16(s)));
        const float al = EX2(m - mn);
        m = mn;
#pragma unroll
        for (int i = 0; i < 16; ++i) { o[i] *= al; s[i] = EX2(s[i] - mn); }
        o = pv_mma<0>(as_frag(v0), s, o, (int)0xDDDDDDDD);
        o = pv_mma<1>(as_frag(v1), s, o, (int)0xDDDDDDDD);
    }
    return o;
}

DI void attn_finish(f32x16 o, rsrc_t wr, int head, int qt) {
    const int lane = threadIdx.x & 63, r = lane & 31, h = lane >> 5;
    auto rl = __builtin_amdgcn_permlane32_swap(__float_as_uint(o[8]), __float_as_uint(o[8]), false, false);
    float l = __uint_as_float(rl[1]);
    float z = 0.f;
#pragma unroll
    for (int k = 0; k < 12; ++k) z = __builtin_fmaf(o[k], 0.f, z);
    const bool ok = (z == 0.f) && (l >= 8.6736174e-19f) && (l <= 1.329228e36f);
    if (__builtin_expect(!__all(ok), 0)) {
        const u32x4 q0 = ld128(wr, WS_QA + (unsigned)(head * 64 + qt) * 1024 + lane * 16);
        const u32x4 q1 = ld128(wr, h ? 0x80000000u : WS_QB + (unsigned)(head * 64 + qt) * 512 + r * 16);
        o = attn_safe(wr, head, q0, q1, lane);
        rl = __builtin_amdgcn_permlane32_swap(__float_as_uint(o[8]), __float_as_uint(o[8]), false, false);
        l = __uint_as_float(rl[1]);
    }
    const float inv = __builtin_amdgcn_rcpf(l);
    const unsigned ot = (unsigned)(qt * NH + head);
    st128(wr, lane * 16, WS_OA + ot * 1024, pack8(o, 0, inv));
    if (h == 0) st64(wr, r * 8, WS_OB + ot * 256, u32x2{pk2(o[8] * inv, o[9] * inv), pk2(o[10] * inv, o[11] * inv)});
}

constexpr int KVT_B = 2880, KVG_B = 4 * KVT_B, KV1 = 3 * F_STG;
constexpr int KV_FULL = KV1 + 4 * KVG_B;
constexpr int KV_FREE = KV_FULL + 16;
constexpr int W_CNT = KV_FREE + 16;
constexpr int LDS_SCR = W_CNT + 16;
DI unsigned kv_off(unsigned g) { return g < 9 ? g * KVG_B : KV1 + ((g - 9) & 3) * KVG_B; }
DI void kv_wait_free(unsigned lds0, unsigned n) {
    for (;;) {
        const u32x4 f = *(const volatile LAS u32x4*)(uintptr_t)(lds0 + KV_FREE);
        if (__builtin_amdgcn_readfirstlane(min(min(f[0], f[1]), min(f[2], f[3]))) >= n) break;
        __builtin_amdgcn_s_sleep(8);
    }
    asm volatile("" ::: "memory");
}
DI void lds_put(unsigned addr, unsigned v) { *(volatile LAS unsigned*)(uintptr_t)addr = v; }

DI void phase_attn(char* __restrict__ ws, unsigned lds0, int wid) {
    const int lane = threadIdx.x & 63, r = lane & 31, h = lane >> 5;
    const int b = blockIdx.x, head = (b & 7) * 4 + (b >> 6), qt0 = ((b >> 3) & 7) * 8 + wid * 2;
    const rsrc_t wr = ws_rsrc(ws);
    bar_raw();
    u32x4 qa[2];
    u32x2 qbv[2];
#pragma unroll
    for (int i = 0; i < 2; ++i) {
        qa[i] = ld128(wr, WS_QA + (unsigned)(head * 64 + qt0 + i) * 1024 + lane * 16);
        qbv[i] = ld64(wr, h ? 0x80000000u : WS_QB + (unsigned)(head * 64 + qt0 + i) * 512 + r * 16);
    }
    const int rv = r <= 20 ? r : 0x8000;
    const unsigned oka = lds0 + lane * 16, okb = lds0 + 1024 + r * 16 + h * 8, ov = lds0 + 1536u + (unsigned)(h * 21 + rv) * 16u;
    unsigned afull = lds0 + KV_FULL, afree = lds0 + KV_FREE + wid * 4;
    PIN(afull); PIN(afree);
    __builtin_amdgcn_s_setprio(2);
    bar_raw();
    bf16x8 ka[2], v0[2], v1[2];
    u32x2 kb[2];
    lptr pka = lbase(oka), pkb = lbase(okb), pv = lbase(ov), pkn;
#define LDK(n, T) do { ka[n] = lds128(pka + (T) * KVT_B); kb[n] = lds64(pkb + (T) * KVT_B); SBAR(); } while (0)
#define LDV(n, T) do { v0[n] = lds128(pv + (T) * KVT_B); v1[n] = lds128(pv + (T) * KVT_B + 42 * 16); SBAR(); } while (0)
    LDK(0, 0); LDK(1, 1); LDV(0, 0);
#pragma unroll
    for (int i = 0; i < 2; ++i) {
        f32x16 t = MFMA32(ka[0], as_frag(qa[i]), f32x16{});
        t = MFMA8(kb[0], qbv[i], t);
        const float m0 = xhalf_max(max16(t));
        qbv[i][0] = h ? (pk2(-(m0 + 40.f), 0.f) & 0xFFFFu) : qbv[i][0];
    }
    f32x16 oA = f32x16{}, oB = f32x16{}, s0, s1, s2, s3 = f32x16{};
    int idx = (int)0xDDDDDDDD;
    PIN(idx);
    v0[1] = ka[0]; v1[1] = ka[0];
    s0 = MFMA32(ka[0], as_frag(qa[0]), f32x16{});
    s0 = MFMA8(kb[0], qbv[0], s0);
    SBAR();
#define GRP(MF, H, S, i) do { MF; SBAR(); H; SBAR(); S[i] = EX2(S[i]); S[i + 1] = EX2(S[i + 1]); S[i + 2] = EX2(S[i + 2]); S[i + 3] = EX2(S[i + 3]); PIN(S); SBAR(); } while (0)
#define SLOT(X, N, kn, Q, P, O, vn, H0, H1, H2, H3) do { \
        GRP(N = MFMA32(ka[kn], as_frag(qa[Q]), f32x16{}), H0, X, 0); \
        GRP(N = MFMA8(kb[kn], qbv[Q], N), H1, X, 4); \
        GRP(O = pv_mma<0>(v0[vn], P, O, idx), H2, X, 8); \
        GRP(O = pv_mma<1>(v1[vn], P, O, idx), H3, X, 12); } while (0)
#define RKA(n, T) ka[n] = lds128(pka + (T) * KVT_B)
#define RKB(n, T) kb[n] = lds64(pkb + (T) * KVT_B)
#define RV0(n, T) v0[n] = lds128(pv + (T) * KVT_B)
#define RV1(n, T) v1[n] = lds128(pv + (T) * KVT_B + 42 * 16)
#define LGKM0 __builtin_amdgcn_s_waitcnt(0xC07F)
#define NOP (void)0
    unsigned nx = KVG_B, gv = 0, need = 2;
    lptr pvn;
#define SPIN(x) asm volatile("" : "+s"(x))
#define GROUP(WRAP) do { \
        u32x2 full; unsigned fm, sfull; \
        SLOT(s0, s1, 0, 1, s3, oB, 1, LGKM0, RKA(0, 2), RKB(0, 2), RV0(1, 1)); \
        SLOT(s1, s2, 1, 0, s0, oA, 0, RV1(1, 1), full = *(const volatile LAS u32x2*)(uintptr_t)afull, do { gv += 1; PIN(gv); } while (0), pkn = lbase(okb + nx)); \
        SLOT(s2, s3, 1, 1, s1, oB, 0, LGKM0, RKA(1, 3), RKB(1, 3), RV0(0, 2)); \
        SLOT(s3, s0, 0, 0, s2, oA, 1, RV1(0, 2), do { fm = min(full[0], full[1]); PIN(fm); } while (0), do { sfull = __builtin_amdgcn_readfirstlane(fm); SPIN(sfull); } while (0), pka = lbase(oka + nx)); \
        pkb = pkn; \
          \
        SLOT(s0, s1, 0, 1, s3, oB, 1, \
             do { if (__builtin_expect(sfull < need, 0)) { \
                      do { __builtin_amdgcn_s_sleep(1); full = *(const volatile LAS u32x2*)(uintptr_t)afull; } while ((unsigned)__builtin_amdgcn_readfirstlane(min(full[0], full[1])) < need); } \
                  asm volatile("" ::: "memory"); LGKM0; } while (0), \
             RKA(0, 0), RKB(0, 0), RV0(1, 3)); \
        SLOT(s1, s2, 1, 0, s0, oA, 0, RV1(1, 3), pvn = lbase(ov + nx), do { nx += KVG_B; SPIN(nx); } while (0), \
             do { if (WRAP) { nx = nx == 9 * KVG_B ? KV1 : nx; SPIN(nx); } } while (0)); \
        pv = pvn; \
        SLOT(s2, s3, 1, 1, s1, oB, 0, LGKM0, RKA(1, 1), RKB(1, 1), RV0(0, 0)); \
        SLOT(s3, s0, 0, 0, s2, oA, 1, RV1(0, 0), lds_put(afree, gv)  , do { need += 1; SPIN(need); } while (0), \
             do { if (WRAP) { nx = nx == KV1 + 4 * KVG_B ? KV1 : nx; SPIN(nx); } } while (0)); } while (0)
#pragma unroll 1
    do { GROUP(false); GROUP(true); } while (need < 18);
    oB = pv_mma<0>(v0[1], s3, oB, idx);
    oB = pv_mma<1>(v1[1], s3, oB, idx);
    __builtin_amdgcn_s_setprio(0);
    attn_finish(oA, wr, head, qt0);
    attn_finish(oB, wr, head, qt0 + 1);
}
DI void w_add(unsigned lds0) {
    if ((threadIdx.x & 63) == 0) __hip_atomic_fetch_add((LAS unsigned*)(uintptr_t)(lds0 + W_CNT), 1u, __ATOMIC_RELAXED, __HIP_MEMORY_SCOPE_WORKGROUP);
}
DI void w_wait(unsigned lds0, unsigned n) {
    while ((unsigned)__builtin_amdgcn_readfirstlane(*(const volatile LAS unsigned*)(uintptr_t)(lds0 + W_CNT)) < n) __builtin_amdgcn_s_sleep(4);
    asm volatile("" ::: "memory");
}
struct WoFrag { bf16x8 f[5][5]; };
DI void wo_stage(const u16* __restrict__ wo, unsigned lds0, int lw) {
    const int lane = threadIdx.x & 63, b = blockIdx.x, xcd = b & 7, i = b >> 3;
    YG_MAP;
    (void)tb;
    unsigned wv[5];
#pragma unroll
    for (int j = 0; j < 5; ++j) { const unsigned ci = j * 64u + lane, row = (ci * 6554u) >> 16; wv[j] = row * 1280u + (ci - row * 10u) * 16u; }
    kv_wait_free(lds0, 3 + 2 * lw);
#pragma unroll 1
    for (int n = lw * 5; n < lw * 5 + 5; ++n)
#pragma unroll
        for (int j = 0; j < 5; ++j) dma16((const char*)wo + (eg * 2 + (sp >> 1)) * 160 + n * 32 * 1280, wv[j], lds0 + (n * 5 + j) * 1024);
}
DI void wo_pickup(WoFrag& W, unsigned lds0, int lw) {
    const int lane = threadIdx.x & 63, c = lane & 15, q = lane >> 4;
    wait_vm<0>();
    const unsigned wl = lds0 + (unsigned)(lw * 160 + (c >> 2)) * 160u + (c & 3) * 8;
    const lptr p0 = lbase(wl + (unsigned)((q >> 1) * 20 + (q & 1) * 4) * 160u), p1 = lbase(wl + (unsigned)(q * 40 + 16) * 160u);
#pragma unroll
    for (int n = 0; n < 5; ++n)
#pragma unroll
        for (int t = 0; t < 5; ++t) {
            const lptr pp = (n < 4 ? p0 + n * 6400 : p1) + t * 32;
            const s16x4 lo = ldstr(pp), hi = ldstr(pp + (n < 4 ? 8 : 20) * 160);
            W.f[n][t] = __builtin_bit_cast(bf16x8, __builtin_shufflevector(lo, hi, 0, 1, 2, 3, 4, 5, 6, 7));
        }
#pragma unroll
    for (int n = 0; n < 5; ++n)
#pragma unroll
        for (int t = 0; t < 5; ++t) PIN(W.f[n][t]);
    w_add(lds0);
}
DI void attn_produce(const u16* __restrict__ wo, const char* __restrict__ ws, unsigned lds0, int pw, WoFrag& W) {
    const int lane = threadIdx.x & 63;
    const int b = blockIdx.x, head = (b & 7) * 4 + (b >> 6);
    unsigned so[6], sd[6];
#pragma unroll
    for (int j = 0; j < 6; ++j) {
        const int cc = min(j * 64 + lane, 359), tl = pw * 2 + (cc >= 180), c = cc >= 180 ? cc - 180 : cc;
        const int v = c - 96, g = v / 21, d = v - g * 21;
        so[j] = c < 64 ? WS_KA + (unsigned)(head * 64 + tl) * 1024 + c * 16
              : c < 96 ? WS_KB + (unsigned)(head * 64 + tl) * 512 + (c - 64) * 16
                       : WS_VV + (unsigned)((tl * 4 + g) * VP + (d < 20 ? head * 20 + d : 640)) * 16;
        sd[j] = c < 64 ? 4096u : c < 96 ? 2048u : 4 * VV_T;
    }
    auto issue = [&](unsigned g) {
        const unsigned dst = lds0 + kv_off(g) + pw * 2 * KVT_B;
#pragma unroll
        for (int j = 0; j < 5; ++j) { dma16c(ws, so[j], dst + j * 1024); so[j] += sd[j]; }
        dma16_lo<40>(ws, so[5], dst + 5 * 1024); so[5] += sd[5];
    };
#pragma unroll
    for (int j = 0; j < 6; ++j) { PIN(so[j]); PIN(sd[j]); }
    if (pw == 1) wait_count((gu32*)(ws + WS_FLAG + (264 + (b & 7)) * 128), 0u, 48u);
    bar_raw();
    issue(0); issue(1); issue(2);
    if (pw == 0 && lane < 5) lds_put(lds0 + KV_FREE + lane * 4, 0u);
    lds_put(lds0 + KV_FULL + pw * 4, 1u);
    wait_vm<12>(); bar();
#pragma unroll 1
    for (unsigned g = 3; g < 13; ++g) { issue(g); wait_vm<12>(); lds_put(lds0 + KV_FULL + pw * 4, g - 1); }
    wait_vm<0>(); lds_put(lds0 + KV_FULL + pw * 4, 13u);
    __builtin_amdgcn_s_setprio(3);
    wo_stage(wo, lds0, pw);
    wo_pickup(W, lds0, pw);
#pragma unroll 1
    for (unsigned g = 13; g < 16; ++g) {
        wait_vm<0>(); lds_put(lds0 + KV_FULL + pw * 4, g);
        kv_wait_free(lds0, g - 3);
        issue(g);
    }
    wait_vm<0>(); lds_put(lds0 + KV_FULL + pw * 4, 17u);
}
DI void attn_helper(const u16* __restrict__ cs, const u16* __restrict__ ss, const u16* __restrict__ wo,
                    const char* __restrict__ ws, unsigned lds0, int hw, WoFrag& W) {
    const int lane = threadIdx.x & 63;
    const int b = blockIdx.x, xcd = b & 7, i = b >> 3;
    YG_MAP;
    const int gl = i >> 3, nt = gl == 3 ? 7 : 1, dt0 = gl == 3 ? 7 * (xcd >> 1) : 28 + (xcd >> 1) * 3 + gl;
    u32x4 abv[7][4];
#pragma unroll
    for (int t = 0; t < 7; ++t)
#pragma unroll
        for (int n = 0; n < 4; ++n) abv[t][n] = *(const LAS u32x4*)(uintptr_t)(lds0 + t * 8192 + hw * 4096 + n * 1024 + lane * 16);
    bar(); bar_raw();
    __builtin_amdgcn_s_setprio(3);
    kv_wait_free(lds0, 1);
#pragma unroll
    for (int t = 0; t < 7; ++t) {
        if (t < nt) {
            const int dt = dt0 + t, et = dt >= 20 ? dt - 20 : dt, ks = (dt >= 20 ? 64 : 0) + ((xcd & 1) * 8 + (i & 7)) * 4 + hw * 2;
            const unsigned dst = WS_AB + (unsigned)((et * 2 + ((lane >> 4) & 1)) * 128 + ks) * 1024u + (unsigned)((lane >> 5) * 256 + (lane & 15) * 16);
#pragma unroll
            for (int n = 0; n < 4; ++n) st128(ws_rsrc(ws), dst, n * 512, abv[t][n]);
            __builtin_amdgcn_s_sleep(4);
        }
    }
    wait_vm<0>();
    if (lane == 0) __hip_atomic_fetch_add((gu32*)(ws + WS_FLAG + (272 + (xcd & 1)) * 128), 1u, RLX_AGENT);

    const char* pfp = (const char*)(sp >= 2 ? ss : cs) + (size_t)(tb * 128 + eg * 32 + hw * 16) * 4096 + (sp & 1) * 2048;
#pragma unroll 1
    for (int n = 0; n < 16; ++n) { touch(pfp + n * 128, (lane >> 2) * 4096, lds0 + LDS_SCR); __builtin_amdgcn_s_sleep(8); }
    wo_stage(wo, lds0, 2 + hw);
    wait_count((gu32*)(ws + WS_FLAG + (272 + hw) * 128), 0u, 256u);
    wo_pickup(W, lds0, 2 + hw);
    w_wait(lds0, 4);
#pragma unroll 1
    for (int n = 0; n < 6; ++n) { yg_issue(cs, ss, ws, lds0, hw * 2 + (n & 1), n >> 1); __builtin_amdgcn_s_sleep(4); }
}

constexpr int Y_OWN0 = 81920, Y_OWN1 = Y_OWN0 + 5120;
DI void tail_load(const u16* __restrict__ cs, const u16* __restrict__ ss, char* __restrict__ ws, unsigned lds0, int wid, const WoFrag& W) {
    const int lane = threadIdx.x & 63, c = lane & 15, q = lane >> 4;
    const int b = blockIdx.x, xcd = b & 7, i = b >> 3;
    YG_MAP;
    (void)eg;
    const int sb = tb * 2 + (sp & 1);
    __builtin_amdgcn_s_setprio(0);
    const int lw = wid - 4;
    wait_vm<0>(); bar();
#pragma unroll 1
    for (int st = 3; st < 16; ++st) {
        yg_issue(cs, ss, ws, lds0, lw, st);
        wait_vm<18>(); bar_raw();
        if (st == 6 && threadIdx.x == 256) __hip_atomic_fetch_add((gu32*)(ws + WS_FLAG + (256 + (i & 7)) * 128), 1u, RLX_AGENT);
    }
    const rsrc_t wr = ws_rsrc(ws);
    bf16x8 fo[4][5];
    const unsigned oa = WS_OA + (unsigned)(sb * 2 * NH + lw * 8) * 1024u + q * 512 + c * 16;
    const unsigned ob = WS_OB + (unsigned)(sb * 2 * NH + lw * 8) * 256u + q * 512 + c * 8;
    auto o12 = [&](int v) {
        SBAR();
#pragma unroll
        for (int u = 2 * v; u < 2 * v + 2; ++u) {
#pragma unroll
            for (int n = 0; n < 4; ++n) fo[u][n] = as_frag(ld128(wr, oa + v * NH * 1024 + n * 2048 + (u & 1) * 256));
            const u32x2 x0 = ld64(wr, ob + v * NH * 256 + (u & 1) * 128), x1 = ld64(wr, ob + v * NH * 256 + (u & 1) * 128 + 256);
            fo[u][4] = as_frag(u32x4{x0[0], x0[1], x1[0], x1[1]});
        }
        SBAR();
    };
    wait_vm<9>(); bar_raw();
    o12(0); wait_vm<12>(); bar_raw();
    o12(1);
    LAS f32x4* xw = (LAS f32x4*)(uintptr_t)(lds0 + lw * 20480 + lane * 16);
#pragma unroll
    for (int u = 0; u < 4; ++u) {
        f32x4 acc[5];
#pragma unroll
        for (int t = 0; t < 5; ++t) acc[t] = MFMA16(W.f[0][t], fo[u][0], f32x4{});
#pragma unroll
        for (int n = 1; n < 5; ++n)
#pragma unroll
            for (int t = 0; t < 5; ++t) acc[t] = MFMA16(W.f[n][t], fo[u][n], acc[t]);
#pragma unroll
        for (int t = 0; t < 5; ++t) xw[(u * 5 + t) * 64] = acc[t];
        SBAR();
    }
    bar();
}
DI void tail_mma(char* __restrict__ ws, u16* __restrict__ out, unsigned lds0, int wid) {
    const int lane = threadIdx.x & 63, c = lane & 15, q = lane >> 4;
    const int b = blockIdx.x, xcd = b & 7, i = b >> 3;
    YG_MAP;
    const int sb = tb * 2 + (sp & 1), ng = eg * 2 + (sp >> 1);
    __builtin_amdgcn_s_setprio(0);
    const rsrc_t wr = ws_rsrc(ws);
    const int rh = wid & 1, kk = wid >> 1;
    {
        const int ch = wid >> 1;
        f32x4 acc[4][5];
#pragma unroll
        for (int u = 0; u < 4; ++u)
#pragma unroll
            for (int t = 0; t < 5; ++t) acc[u][t] = f32x4{};
        bf16x8 frE[2][4], fnE[2][5], frO[2][4], fnO[2][5];
        const unsigned af = lds0 + 16384 + ch * 10240 + lane * 16;
        const unsigned a0 = lds0 + (unsigned)(rh * 64 + c) * 128u + (unsigned)((q ^ (c >> 1)) * 16);
        auto step = [&](bf16x8 (&fr)[2][4], bf16x8 (&fn)[2][5], const bf16x8 (&pr)[2][4], const bf16x8 (&pn)[2][5], int st, bool ld, bool mm) {
            lptr p, r0, r1;
            if (ld) {
                bar();
                const unsigned so = (st & 3) * F_STG;
                p = lbase(af + so); r0 = lbase(a0 + so); r1 = lbase((a0 ^ 64u) + so);
            }
#pragma unroll
            for (int k = 0; k < 2; ++k)
#pragma unroll
                for (int t = 0; t < 5; ++t) {
                    if (ld) {
                        fn[k][t] = lds128(p + t * 2048 + k * 1024);
                        if (t < 4) fr[k][t] = lds128((k ? r1 : r0) + t * 2048);
                    }
                    SBAR();
                    if (mm) {
#pragma unroll
                        for (int u = 0; u < 4; ++u) acc[u][t] = MFMA16(pn[k][t], pr[k][u], acc[u][t]);
                    }
                    SBAR();
                }
        };
        gu32* ocnt = (gu32*)(ws + WS_FLAG + (256 + (sb >> 2)) * 128);
        unsigned oseen = 0;
        const int poll_at = wid == 0 ? 8 : 99;
        step(frE, fnE, frO, fnO, 0, true, false);
#pragma unroll 1
        for (int st = 0; st < 14; st += 2) {
            if (st == poll_at + 4) {
                asm volatile("s_waitcnt vmcnt(0)" : "+v"(oseen)::"memory");
                wait_count(ocnt, oseen, 32u);
            }
            step(frO, fnO, frE, fnE, st + 1, true, true);
            if (st == 2) wait_vm<0>();
            if (st == poll_at) poll1(oseen, ocnt);
            step(frE, fnE, frO, fnO, st + 2, true, true);
        }
        step(frO, fnO, frE, fnE, 15, true, true);
        step(frE, fnE, frO, fnO, 16, false, true);
        const unsigned yp = WS_YP + (unsigned)(((tb * 4 + eg) * 4 + wid) * 4 + sp) * 10240u + lane * 16;
        u32x4 pk[2][5];
#pragma unroll
        for (int u = 0; u < 2; ++u)
#pragma unroll
            for (int t = 0; t < 5; ++t) {
                const f32x4 v0 = acc[2 * u][t], v1 = acc[2 * u + 1][t];
                pk[u][t] = u32x4{pk2(v0[0], v0[1]), pk2(v0[2], v0[3]), pk2(v1[0], v1[1]), pk2(v1[2], v1[3])};
            }
        if (wid != sp) {
#pragma unroll
            for (int u = 0; u < 2; ++u)
#pragma unroll
                for (int t = 0; t < 5; ++t) st128(wr, yp, (u * 5 + t) * 1024, pk[u][t]);
        } else {
#pragma unroll
            for (int u = 0; u < 2; ++u)
#pragma unroll
                for (int t = 0; t < 5; ++t) *(LAS u32x4*)(uintptr_t)(lds0 + (u ? Y_OWN1 : Y_OWN0) + t * 1024 + lane * 16) = pk[u][t];
        }
        SBAR();
        wait_vm<0>();
        if (lane == 0 && wid != sp) __hip_atomic_fetch_add((gu32*)(ws + WS_FLAG + ((tb * 4 + eg) * 4 + wid) * 128), 1u, RLX_AGENT);
    }
    const int t0 = kk * 3;
    wait_count((gu32*)(ws + WS_FLAG + ((tb * 4 + eg) * 4 + sp) * 128), 0u, 3u);
    u32x4 yv[4][3];
    const unsigned yb = WS_YP + (unsigned)(((tb * 4 + eg) * 4 + sp) * 4) * 10240u + (unsigned)(rh * 5 + t0) * 1024u + lane * 16;
#pragma unroll
    for (int t = 0; t < 3; ++t) {
#pragma unroll
        for (int v = 0; v < 3; ++v) yv[v][t] = ld128(wr, yb + ((sp + 1 + v) & 3) * 10240 + (t0 + t < 5 ? t : 0) * 1024);
    }
    const float g0 = __uint_as_float(__builtin_amdgcn_raw_buffer_load_b32(wr, WS_G + (sb * 64 + rh * 32 + c) * 4, 0, SC1));
    const float g1 = __uint_as_float(__builtin_amdgcn_raw_buffer_load_b32(wr, WS_G + (sb * 64 + rh * 32 + 16 + c) * 4, 0, SC1));
    SBAR();
    bar();
#pragma unroll
    for (int t = 0; t < 3; ++t) yv[3][t] = *(const LAS u32x4*)(uintptr_t)(lds0 + (rh ? Y_OWN1 : Y_OWN0) + (t0 + t < 5 ? t0 + t : 0) * 1024 + lane * 16);
    const LAS f32x4* xr = (const LAS f32x4*)(uintptr_t)(lds0 + rh * 10240 + t0 * 1024 + lane * 16);
    u16* op = out + (size_t)(sb * 64 + rh * 32 + c) * DM + ng * 80 + t0 * 16 + q * 4;
#pragma unroll
    for (int t = 0; t < 3; ++t) {
        if (t0 + t < 5) {
#pragma unroll
            for (int u = 0; u < 2; ++u) {
                const float g = u ? g1 : g0;
                const f32x4 y = {(bflo(yv[0][t][2 * u]) + bflo(yv[1][t][2 * u])) + (bflo(yv[2][t][2 * u]) + bflo(yv[3][t][2 * u])),
                                 (bfhi(yv[0][t][2 * u]) + bfhi(yv[1][t][2 * u])) + (bfhi(yv[2][t][2 * u]) + bfhi(yv[3][t][2 * u])),
                                 (bflo(yv[0][t][2 * u + 1]) + bflo(yv[1][t][2 * u + 1])) + (bflo(yv[2][t][2 * u + 1]) + bflo(yv[3][t][2 * u + 1])),
                                 (bfhi(yv[0][t][2 * u + 1]) + bfhi(yv[1][t][2 * u + 1])) + (bfhi(yv[2][t][2 * u + 1]) + bfhi(yv[3][t][2 * u + 1]))};
                const f32x4 v = ((xr[(u * 5 + t) * 64] + xr[(20 + u * 5 + t) * 64]) + (xr[(40 + u * 5 + t) * 64] + xr[(60 + u * 5 + t) * 64])) * g + y * (1.0f - g);
                *(u32x2*)(op + (size_t)u * 16 * DM + t * 16) = u32x2{pk2(v[0], v[1]), pk2(v[2], v[3])};
            }
        }
    }
}

constexpr int LDS_B = LDS_SCR + 256;

#define CODE_LINES 234
__global__ __launch_bounds__(512, 2) void k_all(const u16* __restrict__ x, const u16* __restrict__ wqkv, const u16* __restrict__ cd,
                                             const u16* __restrict__ sd, char* __restrict__ ws, const u16* __restrict__ wo, const u16* __restrict__ gw,
                                             const u16* __restrict__ cs, const u16* __restrict__ ss, u16* __restrict__ out) {
    extern __shared__ __attribute__((aligned(16))) char smem[];
    const unsigned lds0 = (unsigned)(uintptr_t)smem;
    const int wid = __builtin_amdgcn_readfirstlane(threadIdx.x >> 6);
    const int b = blockIdx.x, xcd = b & 7, i = b >> 3;
    const int rb = (xcd & 1) * 8 + (i & 7), cq = xcd >> 1, gl = i >> 3;
    asm volatile("" ::"s"(x), "s"(wqkv), "s"(cd), "s"(sd));
    if (wid < 4) {
        if (i < 2) {
            const char* pc;
            asm volatile("s_getpc_b64 %0" : "=s"(pc));
            touch((const char*)((uintptr_t)pc & ~(uintptr_t)127), min(threadIdx.x, (unsigned)CODE_LINES) * 128, lds0 + P_RING + 64);
        }
        if (threadIdx.x == 64) {
            const unsigned n = __hip_atomic_load((gu32*)(ws + WS_RSP + b * 4), RLX_AGENT) + 1u;
            __hip_atomic_store((gu32*)(ws + WS_REQ + b * 4), n, RLX_AGENT);
            *(volatile LAS unsigned*)(uintptr_t)(lds0 + P_RING) = n;
        }
        if (b == LEADER) {
            const unsigned tl = threadIdx.x;
            st128(ws_rsrc(ws), (tl * VP + 640) * 16, WS_VV, u32x4{ONES, ONES, ONES, ONES});
            st32(ws_rsrc(ws), tl * 128, WS_FLAG, 0u);
            if (tl < 18) st32(ws_rsrc(ws), (256 + tl) * 128, WS_FLAG, 0u);
            st32(ws_rsrc(ws), tl * 4, WS_ACK, 0u);
            wait_vm<0>();
        }
        asm volatile("s_waitcnt lgkmcnt(0)" ::: "memory");
        if (gl == 3) { if (cq == 0) proj_mma<3>(ws, gw, rb, cq, lds0, wid); else proj_mma<2>(ws, gw, rb, cq, lds0, wid); }
        else if (cq * 3 + gl < 8) proj_mma<0>(ws, gw, rb, cq * 3 + gl, lds0, wid);
        else proj_mma<1>(ws, gw, rb, cq * 3 + gl, lds0, wid);
        wait_vm<0>();
    } else proj_load(x, wqkv, cd, sd, ws, rb, cq, gl, lds0, wid - 4);
    bar();
    if (wid == 5) {
        const int lane = threadIdx.x & 63;
        if (gl != 3 && lane < 2) __hip_atomic_fetch_add((gu32*)(ws + WS_FLAG + (264 + ((cq * 3 + gl) & 3) * 2 + lane) * 128), 1u, RLX_AGENT);
    }
    if (wid < 4) {
        phase_attn(ws, lds0, wid);
        tail_mma(ws, out, lds0, wid);
    } else {
        WoFrag W;
        if (wid < 6) attn_produce(wo, ws, lds0, wid - 4, W); else attn_helper(cs, ss, wo, ws, lds0, wid - 6, W);
        tail_load(cs, ss, ws, lds0, wid, W);
    }
}

extern "C" void kernel_launch(void* const* d_in, const int* in_sizes, int n_in, void* d_out, int out_size, void* d_ws, size_t ws_size,
                              hipStream_t stream) {
    const u16* x = (const u16*)d_in[0];
    const u16* wqkv = (const u16*)d_in[1];
    const u16* wo = (const u16*)d_in[2];
    const u16* cs = (const u16*)d_in[4];
    const u16* ss = (const u16*)d_in[5];
    const u16* cd = (const u16*)d_in[6];
    const u16* sd = (const u16*)d_in[7];
    const u16* gw = (const u16*)d_in[8];
    char* ws = (char*)d_ws;
    (void)hipFuncSetAttribute((const void*)k_all, hipFuncAttributeMaxDynamicSharedMemorySize, LDS_B);
    k_all<<<256, 512, LDS_B, stream>>>(x, wqkv, cd, sd, ws, wo, gw, cs, ss, (u16*)d_out);
}
```

```cpp
#include <hip/hip_runtime.h>
#include <hip/hip_bf16.h>
#include <stdint.h>

typedef unsigned short u16;
typedef __attribute__((ext_vector_type(8))) __bf16 bf16x8;
typedef __attribute__((ext_vector_type(4))) short s16x4;
typedef __attribute__((ext_vector_type(16))) float f32x16;
typedef __attribute__((ext_vector_type(4))) float f32x4;
typedef __attribute__((ext_vector_type(4))) unsigned u32x4;
typedef __attribute__((ext_vector_type(2))) unsigned u32x2;
#define LAS __attribute__((address_space(3)))
#define DI __device__ __forceinline__
typedef const LAS char* lptr;

constexpr int SEQ = 2048, DM = 640, NH = 32;
constexpr int VP = 648;
constexpr unsigned WS_QA = 0;
constexpr unsigned WS_KA = WS_QA + NH * 64 * 1024;
constexpr unsigned WS_QB = WS_KA + NH * 64 * 1024;
constexpr unsigned WS_KB = WS_QB + NH * 64 * 512;
constexpr unsigned WS_VV = WS_KB + NH * 64 * 512;
constexpr unsigned WS_AB = WS_VV + 64 * 4 * VP * 16;
constexpr unsigned WS_YP = WS_AB + 40 * 128 * 1024;
constexpr unsigned WS_OA = WS_YP + 4 * 64 * 40 * 1024;
constexpr unsigned WS_OB = WS_OA + 64 * NH * 1024;
constexpr unsigned WS_G = WS_OB + 64 * NH * 256;
constexpr unsigned WS_FLAG = WS_G + SEQ * 16;
constexpr unsigned WS_REQ = WS_FLAG + 274 * 128;
constexpr unsigned WS_RSP = WS_REQ + 1024;
constexpr unsigned WS_ACK = WS_RSP + 1024;
constexpr unsigned WS_END = WS_ACK + 1024;
constexpr float QSCALE = 0.22360679774997896f * 1.4426950408889634f;
constexpr int SC1 = 16;

#define MFMA32(a, b, c) __builtin_amdgcn_mfma_f32_32x32x16_bf16((a), (b), (c), 0, 0, 0)
#define MFMA8(a, b, c) __builtin_amdgcn_mfma_f32_32x32x8bf16_1k(__builtin_bit_cast(s16x4, a), __builtin_bit_cast(s16x4, b), (c), 0, 0, 0)
#define MFMA16(a, b, c) __builtin_amdgcn_mfma_f32_16x16x32_bf16((a), (b), (c), 0, 0, 0)
#define SBAR() __builtin_amdgcn_sched_barrier(0)
#define PIN(x) asm volatile("" : "+v"(x))

typedef __attribute__((ext_vector_type(2))) __bf16 bf16x2;
typedef __attribute__((ext_vector_type(2))) float f32x2;
DI unsigned pk2(float lo, float hi) {
    f32x2 v = {lo, hi};
    return __builtin_bit_cast(unsigned, __builtin_convertvector(v, bf16x2));
}
DI float bsel(unsigned m, float a, float b) { return __uint_as_float((__float_as_uint(a) & m) | (__float_as_uint(b) & ~m)); }
DI float bflo(unsigned v) { return __uint_as_float(v << 16); }
DI float bfhi(unsigned v) { return __uint_as_float(v & 0xFFFF0000u); }
DI void dma16(const void* sbase, unsigned voff, unsigned lds) {
    asm volatile("s_mov_b32 m0, %2\n\ts_nop 0\n\tglobal_load_lds_dwordx4 %0, %1" ::"v"(voff), "s"(sbase), "s"(lds) : "memory");
}
DI void dma16c(const void* sbase, unsigned voff, unsigned lds) {
    asm volatile("s_mov_b32 m0, %2\n\ts_nop 0\n\tglobal_load_lds_dwordx4 %0, %1 sc1" ::"v"(voff), "s"(sbase), "s"(lds) : "memory");
}
template <int N> DI void dma16_lo(const void* sbase, unsigned voff, unsigned lds) {
    unsigned long long sv;
    asm volatile("s_mov_b32 m0, %3\n\ts_mov_b64 %0, exec\n\ts_mov_b64 exec, %4\n\tglobal_load_lds_dwordx4 %1, %2 sc1\n\ts_mov_b64 exec, %0"
                 : "=&s"(sv) : "v"(voff), "s"(sbase), "s"(lds), "s"((1ull << N) - 1) : "memory");
}
DI void touch(const void* sbase, unsigned voff, unsigned lds) {
    asm volatile("s_mov_b32 m0, %2\n\ts_nop 0\n\tglobal_load_lds_dword %0, %1" ::"v"(voff), "s"(sbase), "s"(lds) : "memory");
}
template <int N> DI void wait_vm() { asm volatile("s_waitcnt vmcnt(%0)" ::"i"(N) : "memory"); }
DI void bar() { asm volatile("s_waitcnt lgkmcnt(0)\n\ts_barrier" ::: "memory"); }
DI void bar_raw() { asm volatile("s_barrier" ::: "memory"); }
DI lptr lbase(unsigned a) { asm volatile("" : "+v"(a)); return (lptr)(uintptr_t)a; }
DI bf16x8 lds128(lptr p) { return *(const LAS bf16x8*)p; }
DI u32x2 lds64(lptr p) { return *(const LAS u32x2*)p; }
DI s16x4 ldstr(lptr p) {
    typedef short v4i16_t __attribute__((ext_vector_type(4)));
    return __builtin_bit_cast(s16x4, __builtin_amdgcn_ds_read_tr16_b64_v4i16((LAS v4i16_t*)p));
}
DI u32x4 pack8(const f32x16& x, int s, float sc) {
    u32x4 p;
    p[0] = pk2(x[8 * s + 0] * sc, x[8 * s + 1] * sc);
    p[1] = pk2(x[8 * s + 2] * sc, x[8 * s + 3] * sc);
    p[2] = pk2(x[8 * s + 4] * sc, x[8 * s + 5] * sc);
    p[3] = pk2(x[8 * s + 6] * sc, x[8 * s + 7] * sc);
    return p;
}
typedef __amdgpu_buffer_rsrc_t rsrc_t;
DI rsrc_t ws_rsrc(const void* ws) { return __builtin_amdgcn_make_buffer_rsrc((void*)ws, (short)0, (int)WS_END, 0x00020000); }
DI void st128(rsrc_t r, unsigned voff, unsigned soff, u32x4 v) { __builtin_amdgcn_raw_buffer_store_b128(v, r, voff + soff, 0, SC1); }
DI void st64(rsrc_t r, unsigned voff, unsigned soff, u32x2 v) { __builtin_amdgcn_raw_buffer_store_b64(v, r, voff, soff, SC1); }
DI void st32(rsrc_t r, unsigned voff, unsigned soff, unsigned v) { __builtin_amdgcn_raw_buffer_store_b32(v, r, voff, soff, SC1); }
DI u32x4 ld128(rsrc_t r, unsigned voff) { return __builtin_amdgcn_raw_buffer_load_b128(r, voff, 0, SC1); }
DI u32x2 ld64(rsrc_t r, unsigned voff) { return __builtin_amdgcn_raw_buffer_load_b64(r, voff, 0, SC1); }
DI bf16x8 as_frag(u32x4 v) { return __builtin_bit_cast(bf16x8, v); }
typedef __attribute__((ext_vector_type(16))) __bf16 bf16x16;
template <int HI> DI f32x16 pv_mma(bf16x8 v, const f32x16& p, const f32x16& o, int idx) {
    const auto ph = __builtin_shufflevector(p, p, 8 * HI, 8 * HI + 1, 8 * HI + 2, 8 * HI + 3, 8 * HI + 4, 8 * HI + 5, 8 * HI + 6, 8 * HI + 7);
    return __builtin_amdgcn_smfmac_f32_32x32x32_bf16(v, __builtin_bit_cast(bf16x16, ph), o, idx, 0, 0);
}
DI u32x4 pack8n(const f32x16& x, int s, float sc) {
    const u32x4 p = pack8(x, s, sc);
    const auto r0 = __builtin_amdgcn_permlane32_swap(p[0], p[2], false, false);
    const auto r1 = __builtin_amdgcn_permlane32_swap(p[1], p[3], false, false);
    return u32x4{r0[0], r1[0], r0[1], r1[1]};
}

typedef __attribute__((address_space(1))) unsigned gu32;
#define RLX_AGENT __ATOMIC_RELAXED, __HIP_MEMORY_SCOPE_AGENT
DI void poll1(unsigned& v, gu32* c) {
    unsigned long long sv;
    asm volatile("s_mov_b64 %1, exec\n\ts_mov_b64 exec, 1\n\tglobal_load_dword %0, %2, %3 sc1\n\ts_mov_b64 exec, %1" : "+v"(v), "=&s"(sv) : "v"(0u), "s"(c) : "memory");
}
DI void wait_count(gu32* c, unsigned seen, unsigned want) {
#pragma unroll 1
    for (unsigned spins = 0; (unsigned)__builtin_amdgcn_readfirstlane(seen) < want && spins < (1u << 17); ++spins) {
        __builtin_amdgcn_s_sleep(4);
        poll1(seen, c);
        asm volatile("s_waitcnt vmcnt(0)" : "+v"(seen)::"memory");
    }
    asm volatile("" ::: "memory");
}

constexpr int LEADER = 255;

DI void echo_round(u32x4& v, const char* ws, bool put) {
    const unsigned o = (threadIdx.x & 63) * 16;
    if (put) asm volatile("global_store_dwordx4 %0, %1, %2 sc1\n\ts_nop 1" ::"v"(o), "v"(v), "s"(ws + WS_RSP) : "memory");
    asm volatile("global_load_dwordx4 %0, %1, %2 sc1" : "+v"(v) : "v"(o), "s"(ws + WS_REQ) : "memory");
}

constexpr int P_STG = 45056;
constexpr int P_RING = 3 * P_STG;
constexpr int P_GW = P_RING + 512;

DI void proj_load(const u16* __restrict__ x, const u16* __restrict__ wqkv, const u16* __restrict__ cd, const u16* __restrict__ sd,
                  const char* __restrict__ ws, int rb, int cq, int gl, unsigned lds0, int wid) {
    const int lane = threadIdx.x & 63;
    constexpr int NDMA = 10;
    const bool pa = gl != 3;
    const unsigned vrow = (unsigned)(lane >> 3) * 1280u + (unsigned)(((lane & 7) ^ ((4 * (wid & 1) + (lane >> 4)) & 7)) * 16);
    const char* xb = (const char*)x + ((size_t)rb * 128 + wid * 8) * 1280;
    int st = 3 * ((rb + gl) % 3);
    unsigned slot = lds0 + wid * 1024;
#pragma unroll
    for (int j = 0; j < 4; ++j) dma16(xb + j * 40960 + st * 128, vrow, slot + j * 4096);
    const char* wb[7];
    unsigned vo[5];
    const int dt0 = pa ? 28 + cq * 3 + gl : 7 * cq;
#pragma unroll
    for (int j = 0; j < 7; ++j) {
        const int dt = dt0 + j;
        const char* d = (const char*)(dt >= 20 ? sd : cd) + ((dt >= 20 ? dt - 20 : dt) * 32 + wid * 8) * 1280;
        wb[j] = pa && j ? (const char*)wqkv + (cq * 3 + gl) * 320 : d;
    }
#pragma unroll
    for (int j = 0; j < 5; ++j) { const unsigned i = (wid + 4 * j) * 64 + lane, q = (i * 3277u) >> 16; vo[j] = pa ? q * 3840u + (i - q * 20u) * 16u : vrow; }
    const int wstep = pa ? 64 * 3840 : 128;
    auto issue = [&](bool with_x) {
        if (with_x) {
#pragma unroll
            for (int j = 0; j < 4; ++j) dma16(xb + j * 40960 + st * 128, vrow, slot + j * 4096);
        }
        if (!pa) dma16(wb[6] + st * 128, vrow, slot + 40960);
        dma16(wb[0] + st * 128, vrow, slot + 16384);
#pragma unroll
        for (int j = 0; j < 5; ++j) dma16(wb[1 + j] + st * wstep, vo[j], slot + 20480 + j * 4096);
        slot = slot + P_STG == lds0 + wid * 1024 + P_RING ? lds0 + wid * 1024 : slot + P_STG;
        st = st == 9 ? 0 : st + 1;
    };
    const bool leader = blockIdx.x == LEADER && wid == 3;
    u32x4 rq = {0u, 0u, 0u, 0u};
    unsigned back = 0;
#pragma unroll 1
    for (int n = 0; n < 3; ++n) {
        issue(n > 0);
        if (n == 1) { wait_vm<NDMA>(); bar_raw(); }
    }
    wait_vm<NDMA>(); bar_raw(); bar_raw();
#pragma unroll 1
    for (int n = 3; n < 10; ++n) {
        if (leader && n < 6) echo_round(rq, ws, n > 3);
        if (wid == 0 && n == 9) poll1(back, (gu32*)(ws + WS_RSP + blockIdx.x * 4));
        issue(true); wait_vm<NDMA>(); bar_raw(); bar_raw();
    }
    wait_vm<0>();
    bar_raw();
    bool acked = wid != 0;
    const unsigned want = __builtin_amdgcn_readfirstlane(*(const volatile LAS unsigned*)(uintptr_t)(lds0 + P_RING));
    PIN(back);
    if (!acked && (unsigned)__builtin_amdgcn_readfirstlane(back) == want) {
        if (lane == 0) __hip_atomic_store((gu32*)(ws + WS_ACK + blockIdx.x * 4), 1u, RLX_AGENT);
        acked = true;
    }
    bar_raw(); bar_raw(); bar_raw();
    if (!acked) {
#pragma unroll 1
        for (unsigned spins = 0; spins < (1u << 17) && (unsigned)__builtin_amdgcn_readfirstlane(back) != want; ++spins) {
            __builtin_amdgcn_s_sleep(4);
            poll1(back, (gu32*)(ws + WS_RSP + blockIdx.x * 4));
            asm volatile("s_waitcnt vmcnt(0)" : "+v"(back)::"memory");
        }
        if (lane == 0) __hip_atomic_store((gu32*)(ws + WS_ACK + blockIdx.x * 4), 1u, RLX_AGENT);
    }
}
DI void echo_rest(const char* __restrict__ ws) {
    u32x4 rq = {0u, 0u, 0u, 0u};
#pragma unroll 1
    for (unsigned spins = 0; spins < (1u << 17); ++spins) {
        echo_round(rq, ws, spins != 0);
        u32x4 ak;
        asm volatile("global_load_dwordx4 %0, %2, %3 sc1\n\ts_waitcnt vmcnt(0)" : "=&v"(ak), "+v"(rq) : "v"((threadIdx.x & 63) * 16), "s"(ws + WS_ACK) : "memory");
        if (__all((ak[0] & ak[1] & ak[2] & ak[3]) == 1u)) break;
        __builtin_amdgcn_s_sleep(4);
    }
}

template <int PATH>
DI void proj_mma(char* __restrict__ ws, const u16* __restrict__ gw, int rb, int grp, unsigned lds0, int wid) {
    const int lane = threadIdx.x & 63, r = lane & 31, h = lane >> 5;
    constexpr bool PATH_A = PATH < 2;
    constexpr int NW = PATH_A ? 5 : 0, ND = PATH_A ? 1 : 7, NT = NW + ND;
    const int dt0 = PATH_A ? 28 + grp : 7 * grp;
    const unsigned sw = lds0 + (unsigned)r * 128u + (unsigned)(((h ^ ((r >> 1) & 7)) * 16) ^ (PATH_A ? 0 : grp * 32));
    const unsigned awo = lds0 + 20480u + (unsigned)(8 * h + ((lane & 15) >> 2)) * 320u + (unsigned)(((lane >> 4) & 1) * 32 + (lane & 3) * 8);
    f32x16 acc[NT];
#pragma unroll
    for (int t = 0; t < NT; ++t) acc[t] = f32x16{};
    bf16x8 fx[2], fw[2][NT];
    lptr pr, px, pw;
    auto setp = [&](unsigned so, int ks) {
        pr = lbase((sw ^ (unsigned)(ks * 32)) + so);
        px = lbase((sw ^ (unsigned)(ks * 32)) + so + wid * 4096);
        if (PATH_A && ks == 0) pw = lbase(awo + so);
    };
    auto rd = [&](int ks, int b, int t) {
        if (t == 0) fx[b] = lds128(px);
        if (t < ND) fw[b][t] = lds128(pr + 16384 + t * 4096);
        else {
            s16x4 lo = ldstr(pw + ks * 16 * 320 + (t - ND) * 64), hi = ldstr(pw + ks * 16 * 320 + 4 * 320 + (t - ND) * 64);
            fw[b][t] = __builtin_bit_cast(bf16x8, __builtin_shufflevector(lo, hi, 0, 1, 2, 3, 4, 5, 6, 7));
        }
    };
    auto mm = [&](int b, int t) {
        if (PATH == 0 && t >= ND) acc[t] = MFMA32(fw[b][t], fx[b], acc[t]);
        else acc[t] = MFMA32(fx[b], fw[b][t], acc[t]);
    };
    constexpr bool GATE = !PATH_A;
    f32x16 z = f32x16{};
    u32x4 fg;
    unsigned ag = lds0 + P_GW + (3 * (rb % 3)) * 128 + grp * 32 + h * 16;
    if (GATE) {
        if (wid < 2) dma16((const char*)gw + wid * 256, lane * 16, lds0 + P_GW + wid * 256);
        wait_vm<0>();
    }
    unsigned so = 0;
    setp(0, 0);
#pragma unroll
    for (int t = 0; t < NT; ++t) PIN(acc[t]);
    bar_raw();
    if (GATE) fg = *(const LAS u32x4*)(uintptr_t)ag;
#pragma unroll
    for (int t = 0; t < NT; ++t) rd(0, 0, t);
    SBAR();
#pragma unroll 1
    for (int st = 0; st < 10; ++st) {
#pragma unroll
        for (int ks = 0; ks < 4; ++ks) {
            if (ks == 3) {
                bar_raw();
                so = so == 2 * P_STG ? 0 : so + P_STG;
                ag = ag + 128 >= lds0 + P_GW + 1280 ? ag - 1152 : ag + 128;
            }
            setp(so, (ks + 1) & 3);
#pragma unroll
            for (int t = 0; t < NT; ++t) {
                rd((ks + 1) & 3, (ks + 1) & 1, t);
                if (GATE && ks == 3 && t == 1) fg = *(const LAS u32x4*)(uintptr_t)ag;
                SBAR(); mm(ks & 1, t);
                if (GATE && ks == 0 && t == NT - 1) z = MFMA32(as_frag(fg), fx[0], z);
                SBAR();
            }
        }
        bar_raw();
    }
    const rsrc_t wr = ws_rsrc(ws);
    const int stile = rb * 4 + wid;
    if (PATH == 0) {
        const float sc = grp < 4 ? QSCALE : 1.f;
        const unsigned t0 = (unsigned)(grp * 8 * 64 + stile);
        const unsigned va = WS_QA + t0 * 1024 + lane * 16, vb = WS_QB + (t0 + h * 64) * 512 + r * 16;
#pragma unroll
        for (int m = 0; m < 4; ++m) {
            u32x2 q[5];
#pragma unroll
            for (int i = 0; i < 5; ++i) {
                const int p = 5 * m + i, t = ND + (p >> 2), g = p & 3;
                q[i][0] = pk2(acc[t][4 * g] * sc, acc[t][4 * g + 1] * sc);
                q[i][1] = pk2(acc[t][4 * g + 2] * sc, acc[t][4 * g + 3] * sc);
            }
            st128(wr, va, (2 * m) * 64 * 1024, u32x4{q[0][0], q[0][1], q[1][0], q[1][1]});
            st128(wr, va, (2 * m + 1) * 64 * 1024, u32x4{q[3][0], q[3][1], q[4][0], q[4][1]});
            st128(wr, vb, (2 * m) * 64 * 512, u32x4{q[2][0], q[2][1], 0x00003F80u, 0u});
        }
    } else if (PATH == 1) {
        const unsigned vv = WS_VV + (unsigned)((stile * 4 + h) * VP + (grp - 8) * 160 + r) * 16;
#pragma unroll
        for (int t = 0; t < 5; ++t) {
            st128(wr, vv, t * 512, pack8n(acc[ND + t], 0, 1.f));
            st128(wr, vv, t * 512 + 2 * VP * 16, pack8n(acc[ND + t], 1, 1.f));
        }
    }
#pragma unroll
    for (int t = 0; t < ND; ++t) {
        const float sc = dt0 + t >= 20 ? -1.f : 1.f;
        LAS u32x4* d = (LAS u32x4*)(uintptr_t)(lds0 + t * 8192 + wid * 2048 + lane * 16);
        d[0] = pack8n(acc[t], 0, sc); d[64] = pack8n(acc[t], 1, sc);
    }
    if (GATE) *(LAS float*)(uintptr_t)(lds0 + 7 * 8192 + wid * 256 + lane * 4) = z[0];
}
#define YG_MAP const int tb = (xcd >> 2) * 8 + (i & 7), eg = i >> 3, sp = xcd & 3
constexpr int F_STG = 36864;
constexpr int F_RING = 4 * F_STG;
DI void yg_issue(const u16* __restrict__ cs, const u16* __restrict__ ss, const char* __restrict__ ws, unsigned lds0, int lw, int st) {
    const int lane = threadIdx.x & 63;
    const int b = blockIdx.x, xcd = b & 7, i = b >> 3;
    YG_MAP;
    const unsigned vrow = (unsigned)(lane >> 3) * 4096u + (unsigned)(((lane & 7) ^ ((4 * (lw & 1) + (lane >> 4)) & 7)) * 16);
    const char* rp = (const char*)(sp >= 2 ? ss : cs) + (size_t)(tb * 128 + lw * 8) * 4096 + (sp & 1) * 2048;
    const char* ab = ws + WS_AB + (size_t)((eg * 10 + (lw >> 1)) * 128 + sp * 32 + (lw & 1)) * 1024;
    const unsigned d = lds0 + lw * 1024 + (st & 3) * F_STG;
#pragma unroll
    for (int j = 0; j < 4; ++j) dma16(rp + j * 32 * 4096 + st * 128, vrow, d + j * 4096);
#pragma unroll
    for (int j = 0; j < 5; ++j) dma16c(ab + (j * 256 + st * 2) * 1024, lane * 16, d + 16384 + j * 4096);
}

DI float xhalf_max(float v) {
    auto rr = __builtin_amdgcn_permlane32_swap(__float_as_uint(v), __float_as_uint(v), false, false);
    return fmaxf(__uint_as_float(rr[0]), __uint_as_float(rr[1]));
}
DI float max16(const f32x16& s) {
    float a = fmaxf(fmaxf(s[0], s[1]), s[2]), b = fmaxf(fmaxf(s[3], s[4]), s[5]);
    a = fmaxf(fmaxf(a, s[6]), s[7]); b = fmaxf(fmaxf(b, s[8]), s[9]);
    a = fmaxf(fmaxf(a, s[10]), s[11]); b = fmaxf(fmaxf(b, s[12]), s[13]);
    a = fmaxf(fmaxf(a, s[14]), s[15]);
    return fmaxf(a, b);
}
#define EX2(v) __builtin_amdgcn_exp2f(v)
constexpr unsigned VV_T = 4 * VP * 16, VV_S = 2 * VP * 16;
constexpr unsigned ONES = 0x3F803F80u;

DI u32x4 fix_v(u32x4 v, int r) {
#pragma unroll
    for (int i = 0; i < 4; ++i) v[i] = r < 20 ? v[i] : (r == 20 ? ONES : 0u);
    return v;
}
DI f32x16 attn_safe(rsrc_t wr, int head, u32x4 qa, u32x4 qb, int lane) {
    const int r = lane & 31, h = lane >> 5;
    qb[2] = 0u;
    f32x16 o = f32x16{};
    float m = -INFINITY;
#pragma unroll 1
    for (int t = 0; t < 64; ++t) {
        const u32x4 ka = ld128(wr, WS_KA + (unsigned)(head * 64 + t) * 1024 + lane * 16);
        const u32x4 kb = ld128(wr, WS_KB + (unsigned)(head * 64 + t) * 512 + r * 16);
        const unsigned vp = WS_VV + (unsigned)((t * 4 + h) * VP + head * 20 + (r < 20 ? r : 0)) * 16;
        const u32x4 v0 = fix_v(ld128(wr, vp), r), v1 = fix_v(ld128(wr, vp + VV_S), r);
        f32x16 s = MFMA32(as_frag(ka), as_frag(qa), f32x16{});
        s = MFMA32(as_frag(kb), as_frag(qb), s);
        const float mn = fmaxf(m, xhalf_max(max16(s)));
        const float al = EX2(m - mn);
        m = mn;
#pragma unroll
        for (int i = 0; i < 16; ++i) { o[i] *= al; s[i] = EX2(s[i] - mn); }
        o = pv_mma<0>(as_frag(v0), s, o, (int)0xDDDDDDDD);
        o = pv_mma<1>(as_frag(v1), s, o, (int)0xDDDDDDDD);
    }
    return o;
}

DI void attn_finish(f32x16 o, rsrc_t wr, int head, int qt) {
    const int lane = threadIdx.x & 63, r = lane & 31, h = lane >> 5;
    auto rl = __builtin_amdgcn_permlane32_swap(__float_as_uint(o[8]), __float_as_uint(o[8]), false, false);
    float l = __uint_as_float(rl[1]);
    float z = 0.f;
#pragma unroll
    for (int k = 0; k < 12; ++k) z = __builtin_fmaf(o[k], 0.f, z);
    const bool ok = (z == 0.f) && (l >= 8.6736174e-19f) && (l <= 1.329228e36f);
    if (__builtin_expect(!__all(ok), 0)) {
        const u32x4 q0 = ld128(wr, WS_QA + (unsigned)(head * 64 + qt) * 1024 + lane * 16);
        const u32x4 q1 = ld128(wr, h ? 0x80000000u : WS_QB + (unsigned)(head * 64 + qt) * 512 + r * 16);
        o = attn_safe(wr, head, q0, q1, lane);
        rl = __builtin_amdgcn_permlane32_swap(__float_as_uint(o[8]), __float_as_uint(o[8]), false, false);
        l = __uint_as_float(rl[1]);
    }
    const float inv = __builtin_amdgcn_rcpf(l);
    const unsigned ot = (unsigned)(qt * NH + head);
    st128(wr, lane * 16, WS_OA + ot * 1024, pack8(o, 0, inv));
    if (h == 0) st64(wr, r * 8, WS_OB + ot * 256, u32x2{pk2(o[8] * inv, o[9] * inv), pk2(o[10] * inv, o[11] * inv)});
}

constexpr int KVT_B = 2880, KVG_B = 4 * KVT_B, KV1 = 3 * F_STG;
constexpr int KV_FULL = KV1 + 4 * KVG_B;
constexpr int KV_FREE = KV_FULL + 16;
constexpr int W_CNT = KV_FREE + 16;
constexpr int LDS_SCR = W_CNT + 16;
DI unsigned kv_off(unsigned g) { return g < 9 ? g * KVG_B : KV1 + ((g - 9) & 3) * KVG_B; }
DI void kv_wait_free(unsigned lds0, unsigned n) {
    for (;;) {
        const u32x4 f = *(const volatile LAS u32x4*)(uintptr_t)(lds0 + KV_FREE);
        if (__builtin_amdgcn_readfirstlane(min(min(f[0], f[1]), min(f[2], f[3]))) >= n) break;
        __builtin_amdgcn_s_sleep(8);
    }
    asm volatile("" ::: "memory");
}
DI void lds_put(unsigned addr, unsigned v) { *(volatile LAS unsigned*)(uintptr_t)addr = v; }

DI void phase_attn(char* __restrict__ ws, unsigned lds0, int wid) {
    const int lane = threadIdx.x & 63, r = lane & 31, h = lane >> 5;
    const int b = blockIdx.x, head = (b & 7) * 4 + (b >> 6), qt0 = ((b >> 3) & 7) * 8 + wid * 2;
    const rsrc_t wr = ws_rsrc(ws);
    bar_raw();
    u32x4 qa[2];
    u32x2 qbv[2];
#pragma unroll
    for (int i = 0; i < 2; ++i) {
        qa[i] = ld128(wr, WS_QA + (unsigned)(head * 64 + qt0 + i) * 1024 + lane * 16);
        qbv[i] = ld64(wr, h ? 0x80000000u : WS_QB + (unsigned)(head * 64 + qt0 + i) * 512 + r * 16);
    }
    const int rv = r <= 20 ? r : 0x8000;
    const unsigned oka = lds0 + lane * 16, okb = lds0 + 1024 + r * 16 + h * 8, ov = lds0 + 1536u + (unsigned)(h * 21 + rv) * 16u;
    unsigned afull = lds0 + KV_FULL, afree = lds0 + KV_FREE + wid * 4;
    PIN(afull); PIN(afree);
    __builtin_amdgcn_s_setprio(2);
    bar_raw();
    bf16x8 ka[2], v0[2], v1[2];
    u32x2 kb[2];
    lptr pka = lbase(oka), pkb = lbase(okb), pv = lbase(ov), pkn;
#define LDK(n, T) do { ka[n] = lds128(pka + (T) * KVT_B); kb[n] = lds64(pkb + (T) * KVT_B); SBAR(); } while (0)
#define LDV(n, T) do { v0[n] = lds128(pv + (T) * KVT_B); v1[n] = lds128(pv + (T) * KVT_B + 42 * 16); SBAR(); } while (0)
    LDK(0, 0); LDK(1, 1); LDV(0, 0);
#pragma unroll
    for (int i = 0; i < 2; ++i) {
        f32x16 t = MFMA32(ka[0], as_frag(qa[i]), f32x16{});
        t = MFMA8(kb[0], qbv[i], t);
        const float m0 = xhalf_max(max16(t));
        qbv[i][0] = h ? (pk2(-(m0 + 40.f), 0.f) & 0xFFFFu) : qbv[i][0];
    }
    f32x16 oA = f32x16{}, oB = f32x16{}, s0, s1, s2, s3 = f32x16{};
    int idx = (int)0xDDDDDDDD;
    PIN(idx);
    v0[1] = ka[0]; v1[1] = ka[0];
    s0 = MFMA32(ka[0], as_frag(qa[0]), f32x16{});
    s0 = MFMA8(kb[0], qbv[0], s0);
    SBAR();
#define GRP(MF, H, S, i) do { MF; SBAR(); H; SBAR(); S[i] = EX2(S[i]); S[i + 1] = EX2(S[i + 1]); S[i + 2] = EX2(S[i + 2]); S[i + 3] = EX2(S[i + 3]); PIN(S); SBAR(); } while (0)
#define SLOT(X, N, kn, Q, P, O, vn, H0, H1, H2, H3) do { \
        GRP(N = MFMA32(ka[kn], as_frag(qa[Q]), f32x16{}), H0, X, 0); \
        GRP(N = MFMA8(kb[kn], qbv[Q], N), H1, X, 4); \
        GRP(O = pv_mma<0>(v0[vn], P, O, idx), H2, X, 8); \
        GRP(O = pv_mma<1>(v1[vn], P, O, idx), H3, X, 12); } while (0)
#define RKA(n, T) ka[n] = lds128(pka + (T) * KVT_B)
#define RKB(n, T) kb[n] = lds64(pkb + (T) * KVT_B)
#define RV0(n, T) v0[n] = lds128(pv + (T) * KVT_B)
#define RV1(n, T) v1[n] = lds128(pv + (T) * KVT_B + 42 * 16)
#define LGKM0 __builtin_amdgcn_s_waitcnt(0xC07F)
#define NOP (void)0
    unsigned nx = KVG_B, gv = 0, need = 2;
    lptr pvn;
#define SPIN(x) asm volatile("" : "+s"(x))
#define GROUP(WRAP) do { \
        u32x2 full; unsigned fm, sfull; \
        SLOT(s0, s1, 0, 1, s3, oB, 1, LGKM0, RKA(0, 2), RKB(0, 2), RV0(1, 1)); \
        SLOT(s1, s2, 1, 0, s0, oA, 0, RV1(1, 1), full = *(const volatile LAS u32x2*)(uintptr_t)afull, do { gv += 1; PIN(gv); } while (0), pkn = lbase(okb + nx)); \
        SLOT(s2, s3, 1, 1, s1, oB, 0, LGKM0, RKA(1, 3), RKB(1, 3), RV0(0, 2)); \
        SLOT(s3, s0, 0, 0, s2, oA, 1, RV1(0, 2), do { fm = min(full[0], full[1]); PIN(fm); } while (0), do { sfull = __builtin_amdgcn_readfirstlane(fm); SPIN(sfull); } while (0), pka = lbase(oka + nx)); \
        pkb = pkn; \
          \
        SLOT(s0, s1, 0, 1, s3, oB, 1, \
             do { if (__builtin_expect(sfull < need, 0)) { \
                      do { __builtin_amdgcn_s_sleep(1); full = *(const volatile LAS u32x2*)(uintptr_t)afull; } while ((unsigned)__builtin_amdgcn_readfirstlane(min(full[0], full[1])) < need); } \
                  asm volatile("" ::: "memory"); LGKM0; } while (0), \
             RKA(0, 0), RKB(0, 0), RV0(1, 3)); \
        SLOT(s1, s2, 1, 0, s0, oA, 0, RV1(1, 3), pvn = lbase(ov + nx), do { nx += KVG_B; SPIN(nx); } while (0), \
             do { if (WRAP) { nx = nx == 9 * KVG_B ? KV1 : nx; SPIN(nx); } } while (0)); \
        pv = pvn; \
        SLOT(s2, s3, 1, 1, s1, oB, 0, LGKM0, RKA(1, 1), RKB(1, 1), RV0(0, 0)); \
        SLOT(s3, s0, 0, 0, s2, oA, 1, RV1(0, 0), lds_put(afree, gv)  , do { need += 1; SPIN(need); } while (0), \
             do { if (WRAP) { nx = nx == KV1 + 4 * KVG_B ? KV1 : nx; SPIN(nx); } } while (0)); } while (0)
#pragma unroll 1
    do { GROUP(false); GROUP(true); } while (need < 18);
    oB = pv_mma<0>(v0[1], s3, oB, idx);
    oB = pv_mma<1>(v1[1], s3, oB, idx);
    __builtin_amdgcn_s_setprio(0);
    attn_finish(oA, wr, head, qt0);
    attn_finish(oB, wr, head, qt0 + 1);
}
DI void w_add(unsigned lds0) {
    if ((threadIdx.x & 63) == 0) __hip_atomic_fetch_add((LAS unsigned*)(uintptr_t)(lds0 + W_CNT), 1u, __ATOMIC_RELAXED, __HIP_MEMORY_SCOPE_WORKGROUP);
}
DI void w_wait(unsigned lds0, unsigned n) {
    while ((unsigned)__builtin_amdgcn_readfirstlane(*(const volatile LAS unsigned*)(uintptr_t)(lds0 + W_CNT)) < n) __builtin_amdgcn_s_sleep(4);
    asm volatile("" ::: "memory");
}
struct WoFrag { bf16x8 f[5][5]; };
DI void wo_stage(const u16* __restrict__ wo, unsigned lds0, int lw) {
    const int lane = threadIdx.x & 63, b = blockIdx.x, xcd = b & 7, i = b >> 3;
    YG_MAP;
    (void)tb;
    unsigned wv[5];
#pragma unroll
    for (int j = 0; j < 5; ++j) { const unsigned ci = j * 64u + lane, row = (ci * 6554u) >> 16; wv[j] = row * 1280u + (ci - row * 10u) * 16u; }
    kv_wait_free(lds0, 3 + 2 * lw);
#pragma unroll 1
    for (int n = lw * 5; n < lw * 5 + 5; ++n)
#pragma unroll
        for (int j = 0; j < 5; ++j) dma16((const char*)wo + (eg * 2 + (sp >> 1)) * 160 + n * 32 * 1280, wv[j], lds0 + (n * 5 + j) * 1024);
}
DI void wo_pickup(WoFrag& W, unsigned lds0, int lw) {
    const int lane = threadIdx.x & 63, c = lane & 15, q = lane >> 4;
    wait_vm<0>();
    const unsigned wl = lds0 + (unsigned)(lw * 160 + (c >> 2)) * 160u + (c & 3) * 8;
    const lptr p0 = lbase(wl + (unsigned)((q >> 1) * 20 + (q & 1) * 4) * 160u), p1 = lbase(wl + (unsigned)(q * 40 + 16) * 160u);
#pragma unroll
    for (int n = 0; n < 5; ++n)
#pragma unroll
        for (int t = 0; t < 5; ++t) {
            const lptr pp = (n < 4 ? p0 + n * 6400 : p1) + t * 32;
            const s16x4 lo = ldstr(pp), hi = ldstr(pp + (n < 4 ? 8 : 20) * 160);
            W.f[n][t] = __builtin_bit_cast(bf16x8, __builtin_shufflevector(lo, hi, 0, 1, 2, 3, 4, 5, 6, 7));
        }
#pragma unroll
    for (int n = 0; n < 5; ++n)
#pragma unroll
        for (int t = 0; t < 5; ++t) PIN(W.f[n][t]);
    w_add(lds0);
}
struct AbTiles { u32x4 v[7][2]; unsigned z; };
DI void ab_take(AbTiles& A, unsigned lds0, int w) {
    const int lane = threadIdx.x & 63;
#pragma unroll
    for (int t = 0; t < 7; ++t)
#pragma unroll
        for (int n = 0; n < 2; ++n) A.v[t][n] = *(const LAS u32x4*)(uintptr_t)(lds0 + t * 8192 + w * 2048 + n * 1024 + lane * 16);
    A.z = *(const LAS unsigned*)(uintptr_t)(lds0 + 7 * 8192 + w * 256 + lane * 4);
}
DI void ab_store(const AbTiles& A, const char* __restrict__ ws, int w) {
    const int lane = threadIdx.x & 63, b = blockIdx.x, xcd = b & 7, i = b >> 3, gl = i >> 3;
    const int nt = gl == 3 ? 7 : 1, dt0 = gl == 3 ? 7 * (xcd >> 1) : 28 + (xcd >> 1) * 3 + gl, stile = ((xcd & 1) * 8 + (i & 7)) * 4 + w;
#pragma unroll
    for (int t = 0; t < 7; ++t) {
        if (t < nt) {
            const int dt = dt0 + t, et = dt >= 20 ? dt - 20 : dt, ks = (dt >= 20 ? 64 : 0) + stile;
            const unsigned dst = WS_AB + (unsigned)((et * 2 + ((lane >> 4) & 1)) * 128 + ks) * 1024u + (unsigned)((lane >> 5) * 256 + (lane & 15) * 16);
            st128(ws_rsrc(ws), dst, 0, A.v[t][0]);
            st128(ws_rsrc(ws), dst, 512, A.v[t][1]);
        }
    }
    if (gl == 3 && lane < 32) st32(ws_rsrc(ws), ((xcd >> 1) * SEQ + stile * 32 + lane) * 4, WS_G, A.z);
}
DI void ab_publish(const char* __restrict__ ws, unsigned lds0) {
    wait_vm<0>();
    if ((threadIdx.x & 63) == 0 && __hip_atomic_fetch_add((LAS unsigned*)(uintptr_t)(lds0 + W_CNT + 4), 1u, __ATOMIC_RELAXED, __HIP_MEMORY_SCOPE_WORKGROUP) == 3u)
        __hip_atomic_fetch_add((gu32*)(ws + WS_FLAG + (272 + (blockIdx.x & 1)) * 128), 1u, RLX_AGENT);
}
DI void attn_produce(const u16* __restrict__ wo, const char* __restrict__ ws, unsigned lds0, int pw, WoFrag& W) {
    const int lane = threadIdx.x & 63;
    const int b = blockIdx.x, head = (b & 7) * 4 + (b >> 6);
    __builtin_amdgcn_s_setprio(3);
    AbTiles A;
    ab_take(A, lds0, pw);
    unsigned so[6], sd[6];
#pragma unroll
    for (int j = 0; j < 6; ++j) {
        const int cc = min(j * 64 + lane, 359), tl = pw * 2 + (cc >= 180), c = cc >= 180 ? cc - 180 : cc;
        const int v = c - 96, g = v / 21, d = v - g * 21;
        so[j] = c < 64 ? WS_KA + (unsigned)(head * 64 + tl) * 1024 + c * 16
              : c < 96 ? WS_KB + (unsigned)(head * 64 + tl) * 512 + (c - 64) * 16
                       : WS_VV + (unsigned)((tl * 4 + g) * VP + (d < 20 ? head * 20 + d : 640)) * 16;
        sd[j] = c < 64 ? 4096u : c < 96 ? 2048u : 4 * VV_T;
    }
    auto issue = [&](unsigned g) {
        const unsigned dst = lds0 + kv_off(g) + pw * 2 * KVT_B;
#pragma unroll
        for (int j = 0; j < 5; ++j) { dma16c(ws, so[j], dst + j * 1024); so[j] += sd[j]; }
        dma16_lo<40>(ws, so[5], dst + 5 * 1024); so[5] += sd[5];
    };
#pragma unroll
    for (int j = 0; j < 6; ++j) { PIN(so[j]); PIN(sd[j]); }
    if (pw == 1) wait_count((gu32*)(ws + WS_FLAG + (264 + (b & 7)) * 128), 0u, 48u);
    bar();
    issue(0); issue(1); issue(2);
    if (pw == 0 && lane < 6) lds_put(lds0 + KV_FREE + lane * 4, 0u);
    lds_put(lds0 + KV_FULL + pw * 4, 1u);
    wait_vm<12>(); bar();
#pragma unroll 1
    for (unsigned g = 3; g < 13; ++g) {
        issue(g); wait_vm<12>(); lds_put(lds0 + KV_FULL + pw * 4, g - 1);
        if (g == 6) ab_store(A, ws, pw);
    }
    ab_publish(ws, lds0);
    lds_put(lds0 + KV_FULL + pw * 4, 13u);
    wo_stage(wo, lds0, pw);
    wo_pickup(W, lds0, pw);
#pragma unroll 1
    for (unsigned g = 13; g < 16; ++g) {
        wait_vm<0>(); lds_put(lds0 + KV_FULL + pw * 4, g);
        kv_wait_free(lds0, g - 3);
        issue(g);
    }
    wait_vm<0>(); lds_put(lds0 + KV_FULL + pw * 4, 17u);
}
DI void attn_helper(const u16* __restrict__ cs, const u16* __restrict__ ss, const u16* __restrict__ wo,
                    const char* __restrict__ ws, unsigned lds0, int hw, WoFrag& W) {
    const int lane = threadIdx.x & 63;
    const int b = blockIdx.x, xcd = b & 7, i = b >> 3;
    YG_MAP;
    AbTiles A;
    ab_take(A, lds0, 2 + hw);
    if (b == LEADER && hw == 1) echo_rest(ws);
    bar(); bar_raw();
    __builtin_amdgcn_s_setprio(3);
    kv_wait_free(lds0, 1);
    ab_store(A, ws, 2 + hw);
    ab_publish(ws, lds0);
    const char* pfp = (const char*)(sp >= 2 ? ss : cs) + (size_t)(tb * 128 + eg * 32 + hw * 16) * 4096 + (sp & 1) * 2048;
#pragma unroll 1
    for (int n = 0; n < 16; ++n) {
        if (n == 10 + 3 * hw) wo_stage(wo, lds0, 2 + hw);
        touch(pfp + n * 128, (lane >> 2) * 4096, lds0 + LDS_SCR);
        __builtin_amdgcn_s_sleep(8);
    }
    wait_count((gu32*)(ws + WS_FLAG + (272 + hw) * 128), 0u, 128u);
    wo_pickup(W, lds0, 2 + hw);
}

constexpr int Y_OWN0 = 81920, Y_OWN1 = Y_OWN0 + 5120;
DI void tail_load(const u16* __restrict__ cs, const u16* __restrict__ ss, char* __restrict__ ws, unsigned lds0, int wid, const WoFrag& W) {
    const int lane = threadIdx.x & 63, c = lane & 15, q = lane >> 4;
    const int b = blockIdx.x, xcd = b & 7, i = b >> 3;
    YG_MAP;
    (void)eg;
    const int sb = tb * 2 + (sp & 1);
    const int lw = wid - 4;
    w_wait(lds0, 4);
#pragma unroll 1
    for (int st = 0; st < 3; ++st) yg_issue(cs, ss, ws, lds0, lw, st);
    __builtin_amdgcn_s_setprio(0);
    wait_vm<0>(); bar();
#pragma unroll 1
    for (int st = 3; st < 16; ++st) {
        yg_issue(cs, ss, ws, lds0, lw, st);
        wait_vm<18>(); bar_raw();
        if (st == 6 && threadIdx.x == 256) __hip_atomic_fetch_add((gu32*)(ws + WS_FLAG + (256 + (i & 7)) * 128), 1u, RLX_AGENT);
    }
    const rsrc_t wr = ws_rsrc(ws);
    bf16x8 fo[4][5];
    const unsigned oa = WS_OA + (unsigned)(sb * 2 * NH + lw * 8) * 1024u + q * 512 + c * 16;
    const unsigned ob = WS_OB + (unsigned)(sb * 2 * NH + lw * 8) * 256u + q * 512 + c * 8;
    auto o12 = [&](int v) {
        SBAR();
#pragma unroll
        for (int u = 2 * v; u < 2 * v + 2; ++u) {
#pragma unroll
            for (int n = 0; n < 4; ++n) fo[u][n] = as_frag(ld128(wr, oa + v * NH * 1024 + n * 2048 + (u & 1) * 256));
            const u32x2 x0 = ld64(wr, ob + v * NH * 256 + (u & 1) * 128), x1 = ld64(wr, ob + v * NH * 256 + (u & 1) * 128 + 256);
            fo[u][4] = as_frag(u32x4{x0[0], x0[1], x1[0], x1[1]});
        }
        SBAR();
    };
    wait_vm<9>(); bar_raw();
    o12(0); wait_vm<12>(); bar_raw();
    o12(1);
    LAS f32x4* xw = (LAS f32x4*)(uintptr_t)(lds0 + lw * 20480 + lane * 16);
#pragma unroll
    for (int u = 0; u < 4; ++u) {
        f32x4 acc[5];
#pragma unroll
        for (int t = 0; t < 5; ++t) acc[t] = MFMA16(W.f[0][t], fo[u][0], f32x4{});
#pragma unroll
        for (int n = 1; n < 5; ++n)
#pragma unroll
            for (int t = 0; t < 5; ++t) acc[t] = MFMA16(W.f[n][t], fo[u][n], acc[t]);
#pragma unroll
        for (int t = 0; t < 5; ++t) xw[(u * 5 + t) * 64] = acc[t];
        SBAR();
    }
    bar();
}
DI void tail_mma(char* __restrict__ ws, u16* __restrict__ out, unsigned lds0, int wid) {
    const int lane = threadIdx.x & 63, c = lane & 15, q = lane >> 4;
    const int b = blockIdx.x, xcd = b & 7, i = b >> 3;
    YG_MAP;
    const int sb = tb * 2 + (sp & 1), ng = eg * 2 + (sp >> 1);
    __builtin_amdgcn_s_setprio(0);
    const rsrc_t wr = ws_rsrc(ws);
    const int rh = wid & 1, kk = wid >> 1;
    {
        const int ch = wid >> 1;
        f32x4 acc[4][5];
#pragma unroll
        for (int u = 0; u < 4; ++u)
#pragma unroll
            for (int t = 0; t < 5; ++t) acc[u][t] = f32x4{};
        bf16x8 frE[2][4], fnE[2][5], frO[2][4], fnO[2][5];
        const unsigned af = lds0 + 16384 + ch * 10240 + lane * 16;
        const unsigned a0 = lds0 + (unsigned)(rh * 64 + c) * 128u + (unsigned)((q ^ (c >> 1)) * 16);
        auto step = [&](bf16x8 (&fr)[2][4], bf16x8 (&fn)[2][5], const bf16x8 (&pr)[2][4], const bf16x8 (&pn)[2][5], int st, bool ld, bool mm) {
            lptr p, r0, r1;
            if (ld) {
                bar();
                const unsigned so = (st & 3) * F_STG;
                p = lbase(af + so); r0 = lbase(a0 + so); r1 = lbase((a0 ^ 64u) + so);
            }
#pragma unroll
            for (int k = 0; k < 2; ++k)
#pragma unroll
                for (int t = 0; t < 5; ++t) {
                    if (ld) {
                        fn[k][t] = lds128(p + t * 2048 + k * 1024);
                        if (t < 4) fr[k][t] = lds128((k ? r1 : r0) + t * 2048);
                    }
                    SBAR();
                    if (mm) {
#pragma unroll
                        for (int u = 0; u < 4; ++u) acc[u][t] = MFMA16(pn[k][t], pr[k][u], acc[u][t]);
                    }
                    SBAR();
                }
        };
        gu32* ocnt = (gu32*)(ws + WS_FLAG + (256 + (sb >> 2)) * 128);
        unsigned oseen = 0;
        const int poll_at = wid == 0 ? 8 : 99;
        step(frE, fnE, frO, fnO, 0, true, false);
#pragma unroll 1
        for (int st = 0; st < 14; st += 2) {
            if (st == poll_at + 4) {
                asm volatile("s_waitcnt vmcnt(0)" : "+v"(oseen)::"memory");
                wait_count(ocnt, oseen, 32u);
            }
            step(frO, fnO, frE, fnE, st + 1, true, true);
            if (st == 2) wait_vm<0>();
            if (st == poll_at) poll1(oseen, ocnt);
            step(frE, fnE, frO, fnO, st + 2, true, true);
        }
        step(frO, fnO, frE, fnE, 15, true, true);
        step(frE, fnE, frO, fnO, 16, false, true);
        const unsigned yp = WS_YP + (unsigned)(((tb * 4 + eg) * 4 + wid) * 4 + sp) * 10240u + lane * 16;
        u32x4 pk[2][5];
#pragma unroll
        for (int u = 0; u < 2; ++u)
#pragma unroll
            for (int t = 0; t < 5; ++t) {
                const f32x4 v0 = acc[2 * u][t], v1 = acc[2 * u + 1][t];
                pk[u][t] = u32x4{pk2(v0[0], v0[1]), pk2(v0[2], v0[3]), pk2(v1[0], v1[1]), pk2(v1[2], v1[3])};
            }
        if (wid != sp) {
#pragma unroll
            for (int u = 0; u < 2; ++u)
#pragma unroll
                for (int t = 0; t < 5; ++t) st128(wr, yp, (u * 5 + t) * 1024, pk[u][t]);
        } else {
#pragma unroll
            for (int u = 0; u < 2; ++u)
#pragma unroll
                for (int t = 0; t < 5; ++t) *(LAS u32x4*)(uintptr_t)(lds0 + (u ? Y_OWN1 : Y_OWN0) + t * 1024 + lane * 16) = pk[u][t];
        }
        SBAR();
        wait_vm<0>();
        if (lane == 0 && wid != sp) __hip_atomic_fetch_add((gu32*)(ws + WS_FLAG + ((tb * 4 + eg) * 4 + wid) * 128), 1u, RLX_AGENT);
    }
    const int t0 = kk * 3;
    float z0[4], z1[4];
#pragma unroll
    for (int n = 0; n < 4; ++n) {
        z0[n] = __uint_as_float(__builtin_amdgcn_raw_buffer_load_b32(wr, WS_G + (n * SEQ + sb * 64 + rh * 32 + c) * 4, 0, SC1));
        z1[n] = __uint_as_float(__builtin_amdgcn_raw_buffer_load_b32(wr, WS_G + (n * SEQ + sb * 64 + rh * 32 + 16 + c) * 4, 0, SC1));
    }
    wait_count((gu32*)(ws + WS_FLAG + ((tb * 4 + eg) * 4 + sp) * 128), 0u, 3u);
    u32x4 yv[4][3];
    const unsigned yb = WS_YP + (unsigned)(((tb * 4 + eg) * 4 + sp) * 4) * 10240u + (unsigned)(rh * 5 + t0) * 1024u + lane * 16;
#pragma unroll
    for (int t = 0; t < 3; ++t) {
#pragma unroll
        for (int v = 0; v < 3; ++v) yv[v][t] = ld128(wr, yb + ((sp + 1 + v) & 3) * 10240 + (t0 + t < 5 ? t : 0) * 1024);
    }
    const float g0 = 1.0f / (1.0f + __expf(-((z0[0] + z0[1]) + (z0[2] + z0[3])))), g1 = 1.0f / (1.0f + __expf(-((z1[0] + z1[1]) + (z1[2] + z1[3]))));
    SBAR();
    bar();
#pragma unroll
    for (int t = 0; t < 3; ++t) yv[3][t] = *(const LAS u32x4*)(uintptr_t)(lds0 + (rh ? Y_OWN1 : Y_OWN0) + (t0 + t < 5 ? t0 + t : 0) * 1024 + lane * 16);
    const LAS f32x4* xr = (const LAS f32x4*)(uintptr_t)(lds0 + rh * 10240 + t0 * 1024 + lane * 16);
    u16* op = out + (size_t)(sb * 64 + rh * 32 + c) * DM + ng * 80 + t0 * 16 + q * 4;
#pragma unroll
    for (int t = 0; t < 3; ++t) {
        if (t0 + t < 5) {
#pragma unroll
            for (int u = 0; u < 2; ++u) {
                const float g = u ? g1 : g0;
                const f32x4 y = {(bflo(yv[0][t][2 * u]) + bflo(yv[1][t][2 * u])) + (bflo(yv[2][t][2 * u]) + bflo(yv[3][t][2 * u])),
                                 (bfhi(yv[0][t][2 * u]) + bfhi(yv[1][t][2 * u])) + (bfhi(yv[2][t][2 * u]) + bfhi(yv[3][t][2 * u])),
                                 (bflo(yv[0][t][2 * u + 1]) + bflo(yv[1][t][2 * u + 1])) + (bflo(yv[2][t][2 * u + 1]) + bflo(yv[3][t][2 * u + 1])),
                                 (bfhi(yv[0][t][2 * u + 1]) + bfhi(yv[1][t][2 * u + 1])) + (bfhi(yv[2][t][2 * u + 1]) + bfhi(yv[3][t][2 * u + 1]))};
                const f32x4 v = ((xr[(u * 5 + t) * 64] + xr[(20 + u * 5 + t) * 64]) + (xr[(40 + u * 5 + t) * 64] + xr[(60 + u * 5 + t) * 64])) * g + y * (1.0f - g);
                *(u32x2*)(op + (size_t)u * 16 * DM + t * 16) = u32x2{pk2(v[0], v[1]), pk2(v[2], v[3])};
            }
        }
    }
}

constexpr int LDS_B = LDS_SCR + 256;

#define CODE_LINES 228
__global__ __launch_bounds__(512, 2) void k_all(const u16* __restrict__ x, const u16* __restrict__ wqkv, const u16* __restrict__ cd,
                                             const u16* __restrict__ sd, char* __restrict__ ws, const u16* __restrict__ wo, const u16* __restrict__ gw,
                                             const u16* __restrict__ cs, const u16* __restrict__ ss, u16* __restrict__ out) {
    extern __shared__ __attribute__((aligned(16))) char smem[];
    const unsigned lds0 = (unsigned)(uintptr_t)smem;
    const int wid = __builtin_amdgcn_readfirstlane(threadIdx.x >> 6);
    const int b = blockIdx.x, xcd = b & 7, i = b >> 3;
    const int rb = (xcd & 1) * 8 + (i & 7), cq = xcd >> 1, gl = i >> 3;
    asm volatile("" ::"s"(x), "s"(wqkv), "s"(cd), "s"(sd));
    if (wid < 4) {
        if (i < 2) {
            const char* pc;
            asm volatile("s_getpc_b64 %0" : "=s"(pc));
            touch((const char*)((uintptr_t)pc & ~(uintptr_t)127), min(threadIdx.x, (unsigned)CODE_LINES) * 128, lds0 + P_RING + 64);
        }
        if (threadIdx.x == 64) {
            const unsigned n = __hip_atomic_load((gu32*)(ws + WS_RSP + b * 4), RLX_AGENT) + 1u;
            __hip_atomic_store((gu32*)(ws + WS_REQ + b * 4), n, RLX_AGENT);
            *(volatile LAS unsigned*)(uintptr_t)(lds0 + P_RING) = n;
        }
        if (b == LEADER) {
            const unsigned tl = threadIdx.x;
            st128(ws_rsrc(ws), (tl * VP + 640) * 16, WS_VV, u32x4{ONES, ONES, ONES, ONES});
            st32(ws_rsrc(ws), tl * 128, WS_FLAG, 0u);
            if (tl < 18) st32(ws_rsrc(ws), (256 + tl) * 128, WS_FLAG, 0u);
            st32(ws_rsrc(ws), tl * 4, WS_ACK, 0u);
            wait_vm<0>();
        }
        asm volatile("s_waitcnt lgkmcnt(0)" ::: "memory");
        if (gl == 3) proj_mma<2>(ws, gw, rb, cq, lds0, wid);
        else if (cq * 3 + gl < 8) proj_mma<0>(ws, gw, rb, cq * 3 + gl, lds0, wid);
        else proj_mma<1>(ws, gw, rb, cq * 3 + gl, lds0, wid);
        wait_vm<0>();
    } else proj_load(x, wqkv, cd, sd, ws, rb, cq, gl, lds0, wid - 4);
    bar();
    if (wid == 5) {
        const int lane = threadIdx.x & 63;
        if (gl != 3 && lane < 2) __hip_atomic_fetch_add((gu32*)(ws + WS_FLAG + (264 + ((cq * 3 + gl) & 3) * 2 + lane) * 128), 1u, RLX_AGENT);
    }
    if (wid < 4) {
        phase_attn(ws, lds0, wid);
        tail_mma(ws, out, lds0, wid);
    } else {
        WoFrag W;
        if (wid < 6) attn_produce(wo, ws, lds0, wid - 4, W); else attn_helper(cs, ss, wo, ws, lds0, wid - 6, W);
        tail_load(cs, ss, ws, lds0, wid, W);
    }
}

extern "C" void kernel_launch(void* const* d_in, const int* in_sizes, int n_in, void* d_out, int out_size, void* d_ws, size_t ws_size,
                              hipStream_t stream) {
    const u16* x = (const u16*)d_in[0];
    const u16* wqkv = (const u16*)d_in[1];
    const u16* wo = (const u16*)d_in[2];
    const u16* cs = (const u16*)d_in[4];
    const u16* ss = (const u16*)d_in[5];
    const u16* cd = (const u16*)d_in[6];
    const u16* sd = (const u16*)d_in[7];
    const u16* gw = (const u16*)d_in[8];
    char* ws = (char*)d_ws;
    (void)hipFuncSetAttribute((const void*)k_all, hipFuncAttributeMaxDynamicSharedMemorySize, LDS_B);
    k_all<<<256, 512, LDS_B, stream>>>(x, wqkv, cd, sd, ws, wo, gw, cs, ss, (u16*)d_out);
}
```
